```python
import torch

def _softmax(x, axis=-1):
    return torch.softmax(x, dim=axis)
def _gelu(x):
    return torch.nn.functional.gelu(x, approximate="tanh")
def _silu(x):
    return torch.nn.functional.silu(x)
def _sigmoid(x):
    return torch.sigmoid(x)
def _layernorm(x, axis=-1, eps=1e-5):
    m = x.mean(dim=axis, keepdim=True)
    v = x.var(dim=axis, keepdim=True, unbiased=False)
    return (x - m) / torch.sqrt(v + eps)
def _scatter_add(tgt, idx, src, axis=0):
    return tgt.clone().index_add_(axis, idx, src)
def _segment_sum(src, seg, n):
    out = torch.zeros((n,) + src.shape[1:], dtype=src.dtype)
    return out.index_add_(0, seg, src)
def _pad(x, pw):
    flat = tuple(v for pair in reversed(pw) for v in pair)
    return torch.nn.functional.pad(x, flat)

kernel_name = 'parallel_sum__ssm_conv_h3__swiglu_ffn__prompt'

def setup_inputs():

    _w = (lambda a: (lambda t: t.to(torch.bfloat16) if t.is_floating_point() else t)(torch.as_tensor(a)))
    def _A_setup():
        B, S, D = 1, 2048, 640
        ks = 4
        return {
            "q": _w((lambda *s: torch.randn(*s, dtype=torch.bfloat16) * 0.05)(B, S, D)),
            "k": _w((lambda *s: torch.randn(*s, dtype=torch.bfloat16) * 0.8)(B, S, D)),
            "v": _w((lambda *s: torch.randn(*s, dtype=torch.bfloat16) * 0.4)(B, S, D)),
            "shift_k": _w((lambda *s: torch.randn(*s, dtype=torch.bfloat16) * 0.8)(D, ks) * 0.5),
            "ssm_k": _w((lambda *s: torch.randn(*s, dtype=torch.bfloat16) * 0.8)(D, S) * (1.0 / S ** 0.5)),
            "ssm_D": _w((lambda *s: torch.randn(*s, dtype=torch.bfloat16) * 0.4)(D)),
        }
    _A_in = _A_setup()

    def _B_setup():
        B, S, D, F = 1, 2048, 640, 1728
        return {
            "x": _w((lambda *s: torch.randn(*s, dtype=torch.bfloat16) * 0.05)(B, S, D)),
            "w_gate": _w((lambda *s: torch.randn(*s, dtype=torch.bfloat16) * 0.05)(D, F)),
            "w_up": _w((lambda *s: torch.randn(*s, dtype=torch.bfloat16) * 0.05)(D, F)),
            "w_down": _w((lambda *s: torch.randn(*s, dtype=torch.bfloat16) * 0.05)(F, D)),
        }
    _B_in = _B_setup()

    return {
        "A_q": _A_in["q"],
        "A_k": _A_in["k"],
        "A_v": _A_in["v"],
        "A_shift_k": _A_in["shift_k"],
        "A_ssm_k": _A_in["ssm_k"],
        "A_ssm_D": _A_in["ssm_D"],
        "B_x": _B_in["x"],
        "B_w_gate": _B_in["w_gate"],
        "B_w_up": _B_in["w_up"],
        "B_w_down": _B_in["w_down"],
    }

def reference(A_q, A_k, A_v, A_shift_k, A_ssm_k, A_ssm_D, B_x, B_w_gate, B_w_up, B_w_down):
    def _A_ref(q, k, v, shift_k, ssm_k, ssm_D):
        B, S, D = q.shape
        ks = shift_k.shape[-1]
        k_pad = _pad(k, ((0, 0), (ks - 1, 0), (0, 0)))
        k_shift = torch.zeros_like(k)
        for t in range(ks):
            k_shift = k_shift + k_pad[:, t:t + S, :] * shift_k[:, ks - 1 - t][None, None, :]
        kv = k_shift * v
        L = 2 * S
        kv_pad = _pad(kv, ((0, 0), (0, S), (0, 0)))
        kf_pad = _pad(ssm_k, ((0, 0), (0, S)))
        kv_f = (lambda x, axes=(-2,-1): torch.fft.rfft2(x.float(), dim=axes))(kv_pad[:, None, :, :], axes=(1, 2))
        kf_f = (lambda x, axes=(-2,-1): torch.fft.rfft2(x.float(), dim=axes))(kf_pad[:, None, :], axes=(1, 2))
        kf_f = (lambda x, *d: x.permute(*d))(kf_f, 1, 2, 0)
        y_f = kv_f * kf_f[None, :, :, :]
        y = (lambda x, s, axes=(-2,-1): torch.fft.irfft2(x, s=s, dim=axes).to(torch.bfloat16))(y_f, (1, L), axes=(1, 2))[:, 0, :S, :]
        y = y + kv * ssm_D[None, None, :]
        return q * y

    def _B_ref(x, w_gate, w_up, w_down):
        g = _silu(x @ w_gate)
        u = x @ w_up
        return (g * u) @ w_down

    x = A_q
    xn = _layernorm(x, axis=-1)
    return x + _A_ref(xn, A_k, A_v, A_shift_k, A_ssm_k, A_ssm_D) + _B_ref(xn, B_w_gate, B_w_up, B_w_down)
```

```cpp
#include <hip/hip_runtime.h>
#include <hip/hip_bf16.h>
#include <stdint.h>
typedef unsigned short bf16_t;
constexpr int S=2048, D=640, F=1728;
__device__ __forceinline__ float b2f(bf16_t x){ return __uint_as_float(((unsigned)x)<<16); }
__device__ __forceinline__ bf16_t f2b(float f){ unsigned u=__float_as_uint(f); u += 0x7fffu + ((u>>16)&1u); return (bf16_t)(u>>16); }
__device__ __forceinline__ float rb(float f){ return b2f(f2b(f)); }

__global__ void k_prep(const bf16_t* q,const bf16_t* k,const bf16_t* v,const bf16_t* sk,bf16_t* xn,bf16_t* kv){
  int t=blockIdx.x; int tid=threadIdx.x;
  __shared__ float red[256];
  float s=0; for(int d=tid;d<D;d+=256) s+=b2f(q[t*D+d]);
  red[tid]=s; __syncthreads();
  for(int o=128;o;o>>=1){ if(tid<o) red[tid]+=red[tid+o]; __syncthreads(); }
  float mean=red[0]/D; __syncthreads();
  float s2=0; for(int d=tid;d<D;d+=256){ float x=b2f(q[t*D+d])-mean; s2+=x*x; }
  red[tid]=s2; __syncthreads();
  for(int o=128;o;o>>=1){ if(tid<o) red[tid]+=red[tid+o]; __syncthreads(); }
  float var=red[0]/D;
  float mb=rb(mean), vb=rb(var); float sb=rb(sqrtf(rb(vb+1e-5f)));
  for(int d=tid;d<D;d+=256){
    xn[t*D+d]=f2b(rb(b2f(q[t*D+d])-mb)/sb);
    float acc=0.f;
    for(int j=0;j<4;j++){
      int tt=t+j-3; float kk = tt>=0? b2f(k[tt*D+d]):0.f;
      acc = rb(acc + rb(kk*b2f(sk[d*4+3-j])));
    }
    kv[t*D+d]=f2b(acc*b2f(v[t*D+d]));
  }
}
__global__ void k_conv(const bf16_t* kv,const bf16_t* h,const bf16_t* Dv,const bf16_t* xn,const bf16_t* x, float* a_out){
  int idx=blockIdx.x*256+threadIdx.x; int t=idx/D, d=idx%D;
  float acc=0; for(int s=0;s<=t;s++) acc+=b2f(kv[s*D+d])*b2f(h[d*S+(t-s)]);
  float y=rb(acc); y=rb(y+rb(b2f(kv[t*D+d])*b2f(Dv[d])));
  float a=rb(b2f(xn[t*D+d])*y);
  a_out[idx]=rb(b2f(x[idx])+a);
}
__global__ void k_g1(const bf16_t* xn,const bf16_t* wg,const bf16_t* wu,bf16_t* hm){
  int idx=blockIdx.x*256+threadIdx.x; int t=idx/F, f=idx%F;
  float g=0,u=0; for(int k=0;k<D;k++){ float x=b2f(xn[t*D+k]); g+=x*b2f(wg[k*F+f]); u+=x*b2f(wu[k*F+f]); }
  g=rb(g); u=rb(u); float sg=rb(g/(1.f+expf(-g)));
  hm[idx]=f2b(sg*u);
}
__global__ void k_g2(const bf16_t* hm,const bf16_t* wd,const float* a_out,bf16_t* out){
  int idx=blockIdx.x*256+threadIdx.x; int t=idx/D, d=idx%D;
  float acc=0; for(int f=0;f<F;f++) acc+=b2f(hm[t*F+f])*b2f(wd[f*D+d]);
  out[idx]=f2b(a_out[idx]+rb(acc));
}
extern "C" void kernel_launch(void* const* d_in, const int* in_sizes, int n_in, void* d_out, int out_size, void* d_ws, size_t ws_size, hipStream_t stream){
  const bf16_t* q=(const bf16_t*)d_in[0]; const bf16_t* k=(const bf16_t*)d_in[1]; const bf16_t* v=(const bf16_t*)d_in[2];
  const bf16_t* sk=(const bf16_t*)d_in[3]; const bf16_t* h=(const bf16_t*)d_in[4]; const bf16_t* Dv=(const bf16_t*)d_in[5];
  const bf16_t* wg=(const bf16_t*)d_in[7]; const bf16_t* wu=(const bf16_t*)d_in[8]; const bf16_t* wd=(const bf16_t*)d_in[9];
  char* ws=(char*)d_ws;
  bf16_t* xn=(bf16_t*)ws; bf16_t* kv=(bf16_t*)(ws+(4<<20)); bf16_t* hm=(bf16_t*)(ws+(8<<20)); float* a_out=(float*)(ws+(20<<20));
  k_prep<<<S,256,0,stream>>>(q,k,v,sk,xn,kv);
  k_conv<<<S*D/256,256,0,stream>>>(kv,h,Dv,xn,q,a_out);
  k_g1<<<S*F/256,256,0,stream>>>(xn,wg,wu,hm);
  k_g2<<<S*D/256,256,0,stream>>>(hm,wd,a_out,(bf16_t*)d_out);
}
```

```cpp
#include <hip/hip_runtime.h>
#include <stdint.h>
typedef unsigned short u16;
typedef __bf16 bf16x8 __attribute__((ext_vector_type(8)));
typedef __bf16 bf16x2 __attribute__((ext_vector_type(2)));
typedef short s16x4 __attribute__((ext_vector_type(4)));
typedef float f32x2 __attribute__((ext_vector_type(2)));
typedef float f32x4 __attribute__((ext_vector_type(4)));
typedef unsigned u32x2 __attribute__((ext_vector_type(2)));
typedef unsigned u32x4 __attribute__((ext_vector_type(4)));
typedef int i32x4 __attribute__((ext_vector_type(4)));
#define LAS __attribute__((address_space(3)))
#define GAS __attribute__((address_space(1)))
#define DI __device__ __forceinline__
typedef GAS unsigned gu32;

constexpr int S_ = 2048, D_ = 640, F_ = 1728;
constexpr unsigned CW_EPOCH = 0;
constexpr unsigned CW_ARRIVE = 1024;
constexpr unsigned CW_KV = 4096;
constexpr unsigned CW_ST = 16384;
constexpr unsigned CW_HM = 24576;
constexpr unsigned CW_Z = 40960;
constexpr unsigned WS_STATS = 65536;
constexpr unsigned WS_KVT = 131072;
constexpr unsigned WS_ZT = WS_KVT + D_ * S_ * 2;
constexpr unsigned WS_HM = WS_ZT + D_ * S_ * 2;
constexpr unsigned WS_END = WS_HM + 54 * 128 * 1024;
constexpr unsigned WS_DBG = 32u << 20;
constexpr int G1_SLOT = 49152;
constexpr int L_CS = 153856, L_ST = L_CS + 512, L_S2 = L_ST + 2048, L_END = L_S2 + 512 + 64 + 256;
constexpr int L_HR = 0, L_WR = 49152, G2_WSLOT = 20480;
constexpr int L_CVG = 90112, L_CVP = 69632;
constexpr int L_ZS = 131072, ZS_RS = 144;
constexpr int L_CP = L_CVG + 2 * 21216, L_PF = L_CS - 64;
constexpr int L_PFP = L_S2 + 512 - 0;
constexpr int G2_RS = 336, G2_PART = 64 * G2_RS;
constexpr int CV_H = 4160, CV_U = 4 * CV_H, CV_SZ = CV_U + 4576;
static_assert(L_ZS + 80 * ZS_RS <= L_PF, "lds");
static_assert(L_CP + 2 * 8192 <= L_PF, "lds");
static_assert(L_CVP + 4 * CV_SZ + 64 <= L_S2, "lds");
constexpr int NCH_P = 6;
constexpr int NCH_G = D_ - 40 * NCH_P;
static_assert(NCH_G <= 2 * 216 && NCH_G % 2 == 0, "conv split");
constexpr unsigned SPIN_MAX = 1u << 16;

struct Params {
  const u16 *q, *k, *v, *sk, *h, *dv, *wg, *wu, *wd;
  u16* out; char* ws;
};

DI i32x4 make_srd(const void* p, unsigned bytes) {
  i32x4 r; unsigned long long a = (unsigned long long)p;
  r.x = (int)(unsigned)a;
  r.y = (int)((unsigned)(a >> 32) & 0xffffu);
  r.z = (int)bytes; r.w = 0x00020000; return r;
}
DI void dma16(const i32x4& srd, unsigned voff, unsigned soff, unsigned lds) {
  asm volatile("s_mov_b32 m0, %3\n\ts_nop 0\n\tbuffer_load_dwordx4 %0, %1, %2 offen lds" :: "v"(voff), "s"(srd), "s"(soff), "s"(lds) : "memory");
}
DI void dma16_sc1(const i32x4& srd, unsigned voff, unsigned soff, unsigned lds) {
  asm volatile("s_mov_b32 m0, %3\n\ts_nop 0\n\tbuffer_load_dwordx4 %0, %1, %2 offen sc1 lds" :: "v"(voff), "s"(srd), "s"(soff), "s"(lds) : "memory");
}
DI void ld16(u32x4& d, const i32x4& srd, unsigned voff, unsigned soff) { asm volatile("buffer_load_dwordx4 %0, %1, %2, %3 offen" : "=v"(d) : "v"(voff), "s"(srd), "s"(soff) : "memory"); }
DI void ld16_sc1(u32x4& d, const i32x4& srd, unsigned voff, unsigned soff) { asm volatile("buffer_load_dwordx4 %0, %1, %2, %3 offen sc1" : "=v"(d) : "v"(voff), "s"(srd), "s"(soff) : "memory"); }
DI void ld8_sc1(u32x2& d, const i32x4& srd, unsigned voff, unsigned soff) { asm volatile("buffer_load_dwordx2 %0, %1, %2, %3 offen sc1" : "=v"(d) : "v"(voff), "s"(srd), "s"(soff) : "memory"); }
DI void ld8(u32x2& d, const i32x4& srd, unsigned voff, unsigned soff) { asm volatile("buffer_load_dwordx2 %0, %1, %2, %3 offen" : "=v"(d) : "v"(voff), "s"(srd), "s"(soff) : "memory"); }
DI void ld4_sc1(unsigned& d, const i32x4& srd, unsigned voff) { asm volatile("buffer_load_dword %0, %1, %2, 0 offen sc1" : "=v"(d) : "v"(voff), "s"(srd) : "memory"); }
DI void touch4(const i32x4& srd, unsigned voff, unsigned soff, unsigned lds) {
  asm volatile("s_mov_b32 m0, %3\n\ts_nop 0\n\tbuffer_load_dword %0, %1, %2 offen lds" :: "v"(voff), "s"(srd), "s"(soff), "s"(lds) : "memory");
}
DI void touch4_sc1(const i32x4& srd, unsigned voff, unsigned soff, unsigned lds) {
  asm volatile("s_mov_b32 m0, %3\n\ts_nop 0\n\tbuffer_load_dword %0, %1, %2 offen sc1 lds" :: "v"(voff), "s"(srd), "s"(soff), "s"(lds) : "memory");
}
#define WAITV(n) asm volatile("s_waitcnt vmcnt(%0)" :: "i"(n) : "memory")
#define BAR() asm volatile("s_waitcnt lgkmcnt(0)\n\ts_barrier" ::: "memory")
#define PIN(x) asm volatile("" : "+v"(x))
DI float b2f(u16 x) { return __uint_as_float(((unsigned)x) << 16); }
DI float blo(unsigned x) { return __uint_as_float(x << 16); }
DI float bhi(unsigned x) { return __uint_as_float(x & 0xffff0000u); }
DI unsigned pk2(float lo, float hi) { bf16x2 r = {(__bf16)lo, (__bf16)hi}; return __builtin_bit_cast(unsigned, r); }
DI float rbf(float x) { return (float)(__bf16)x; }
DI float dot2(unsigned a, unsigned b, float c) { return __builtin_amdgcn_fdot2_f32_bf16(__builtin_bit_cast(bf16x2, a), __builtin_bit_cast(bf16x2, b), c, false); }
template <int CTRL> DI float dpp_add(float x) { return x + __int_as_float(__builtin_amdgcn_update_dpp(0, __float_as_int(x), CTRL, 0xf, 0xf, true)); }
DI float row16_sum(float x) { x = dpp_add<0x128>(x); x = dpp_add<0x124>(x); x = dpp_add<0x122>(x); x = dpp_add<0x121>(x); return x; }
DI bf16x8 ldtr(LAS char* p0, LAS char* p1) {
  s16x4 a = __builtin_amdgcn_ds_read_tr16_b64_v4i16((LAS s16x4*)p0), b = __builtin_amdgcn_ds_read_tr16_b64_v4i16((LAS s16x4*)p1);
  return __builtin_bit_cast(bf16x8, __builtin_shufflevector(a, b, 0, 1, 2, 3, 4, 5, 6, 7));
}
DI void poll_all(const char* ws, unsigned off, unsigned tag) {
  gu32* a = (gu32*)(ws + off);
#pragma unroll 1
  for (unsigned it = 0; it < SPIN_MAX; ++it) {
    const unsigned v = __hip_atomic_load(a, __ATOMIC_RELAXED, __HIP_MEMORY_SCOPE_AGENT);
    if (__all(v == tag)) break;
    __builtin_amdgcn_s_sleep(2);
  }
  asm volatile("" ::: "memory");
}
DI void flag_set(const char* ws, unsigned off, unsigned tag, int lane) {
  if (lane == 0) __hip_atomic_store((gu32*)(ws + off), tag, __ATOMIC_RELAXED, __HIP_MEMORY_SCOPE_AGENT);
}
#define STAMP_CY(idx) do {} while (0)
#define STAMP_AT(idx) do {} while (0)

template <int NX, class Hook>
DI void task_kv(const Params& p, int cg, int tb, int lane, Hook hook) {
  const int c0 = cg * 64 + 8 * (lane & 7), t0 = tb * 64 + 8 * (lane >> 3);
  const i32x4 rk = make_srd(p.k, S_ * D_ * 2), rv = make_srd(p.v, S_ * D_ * 2), rsk = make_srd(p.sk, D_ * 8);
  u32x4 kk[11], vv[8], ss[4];
#pragma unroll
  for (int j = 0; j < 4; ++j) ld16(ss[j], rsk, (unsigned)(c0 * 8), j * 16);
#pragma unroll
  for (int j = 0; j < 11; ++j) { const int t = t0 - 3 + j; ld16(kk[j], rk, t >= 0 ? (unsigned)(t * 1280 + c0 * 2) : 0xfffffff0u, 0); }
#pragma unroll
  for (int j = 0; j < 8; ++j) ld16(vv[j], rv, (unsigned)(t0 * 1280 + c0 * 2), j * 1280);
  hook();
  const __amdgpu_buffer_rsrc_t ro = __builtin_amdgcn_make_buffer_rsrc((void*)(p.ws + WS_KVT), 0, D_ * S_ * 2, 0x00020000);
  WAITV(NX);
  STAMP_AT(12);
#pragma unroll
  for (int j = 0; j < 4; ++j) PIN(ss[j]);
#pragma unroll
  for (int j = 0; j < 11; ++j) PIN(kk[j]);
#pragma unroll
  for (int j = 0; j < 8; ++j) PIN(vv[j]);
#pragma unroll
  for (int cp = 0; cp < 4; ++cp) {
    const f32x2 s0 = {blo(ss[cp].x), blo(ss[cp].z)}, s1 = {bhi(ss[cp].x), bhi(ss[cp].z)}, s2 = {blo(ss[cp].y), blo(ss[cp].w)}, s3 = {bhi(ss[cp].y), bhi(ss[cp].w)};
    f32x2 kx[11];
#pragma unroll
    for (int j = 0; j < 11; ++j) { const unsigned w = kk[j][cp]; kx[j] = (f32x2){blo(w), bhi(w)}; }
    f32x2 o[8];
    __builtin_amdgcn_sched_barrier(0);
#pragma unroll
    for (int j = 0; j < 8; ++j) o[j] = kx[j] * s3;
    __builtin_amdgcn_sched_barrier(0);
#pragma unroll
    for (int j = 0; j < 8; ++j) o[j] = kx[j + 1] * s2 + o[j];
    __builtin_amdgcn_sched_barrier(0);
#pragma unroll
    for (int j = 0; j < 8; ++j) o[j] = kx[j + 2] * s1 + o[j];
    __builtin_amdgcn_sched_barrier(0);
#pragma unroll
    for (int j = 0; j < 8; ++j) o[j] = kx[j + 3] * s0 + o[j];
    __builtin_amdgcn_sched_barrier(0);
    f32x2 vx[8];
#pragma unroll
    for (int j = 0; j < 8; ++j) { const unsigned w = vv[j][cp]; vx[j] = (f32x2){blo(w), bhi(w)}; }
    __builtin_amdgcn_sched_barrier(0);
#pragma unroll
    for (int j = 0; j < 8; ++j) o[j] = o[j] * vx[j];
    __builtin_amdgcn_sched_barrier(0);
    const u32x4 r0 = {pk2(o[7].x, o[6].x), pk2(o[5].x, o[4].x), pk2(o[3].x, o[2].x), pk2(o[1].x, o[0].x)};
    const u32x4 r1 = {pk2(o[7].y, o[6].y), pk2(o[5].y, o[4].y), pk2(o[3].y, o[2].y), pk2(o[1].y, o[0].y)};
    __builtin_amdgcn_raw_buffer_store_b128(r0, ro, (unsigned)((c0 + 2 * cp) * 4096 + (2040 - t0) * 2), 0, 16);
    __builtin_amdgcn_raw_buffer_store_b128(r1, ro, (unsigned)((c0 + 2 * cp + 1) * 4096 + (2040 - t0) * 2), 0, 16);
  }
}
template <bool PRE>
DI void task_stats(const Params& p, int id, int lane) {
  const i32x4 rq = make_srd(p.q, S_ * D_ * 2);
  const int row = id * 4 + (lane >> 4), l16 = lane & 15;
  u32x4 x[5];
#pragma unroll
  for (int j = 0; j < 5; ++j) ld16(x[j], rq, (unsigned)row * 1280u + l16 * 16, j * 256);
  const __amdgpu_buffer_rsrc_t rs = __builtin_amdgcn_make_buffer_rsrc((void*)(p.ws + WS_STATS), 0, S_ * 8, 0x00020000);
  if (PRE) BAR();
  WAITV(0);
  STAMP_AT(12);
#pragma unroll
  for (int j = 0; j < 5; ++j) PIN(x[j]);
  float s4[4] = {0.f, 0.f, 0.f, 0.f};
#pragma unroll
  for (int j = 0; j < 5; ++j)
#pragma unroll
    for (int e = 0; e < 4; ++e) s4[e] = dot2(x[j][e], 0x3f803f80u, s4[e]);
  const float mean = row16_sum((s4[0] + s4[1]) + (s4[2] + s4[3])) * (1.0f / 640.0f);
  f32x2 q2[2] = {{0.f, 0.f}, {0.f, 0.f}};
#pragma unroll
  for (int j = 0; j < 5; ++j)
#pragma unroll
    for (int e = 0; e < 4; ++e) { const unsigned w = x[j][e]; const f32x2 d = (f32x2){blo(w), bhi(w)} - mean; q2[e & 1] = d * d + q2[e & 1]; }
  const float var = row16_sum((q2[0].x + q2[0].y) + (q2[1].x + q2[1].y)) * (1.0f / 640.0f);
  const float mb = rbf(mean), sb = rbf(__builtin_amdgcn_sqrtf(rbf(rbf(var) + 1e-5f)));
  u32x2 o; o.x = __float_as_uint(mb); o.y = __float_as_uint(1.0f / sb);
  __builtin_amdgcn_raw_buffer_store_b64(o, rs, l16 == 0 ? (unsigned)row * 8u : 0xfffffff0u, 0, 16);
}

struct ConvIn { u32x4 L0[4], L1[4], L2[4], U[4]; unsigned dv; };
DI void conv_load_h(const Params& p, ConvIn& in, int c, int lane) {
  const i32x4 rh = make_srd(p.h, D_ * S_ * 2), rd = make_srd(p.dv, D_ * 2);
  const unsigned vo = lane * 16, so = c * 4096u;
  asm volatile("buffer_load_ushort %0, %1, %2, 0 offen" : "=v"(in.dv) : "v"(c * 2u), "s"(rd) : "memory");
#pragma unroll
  for (int i = 0; i < 4; ++i) { ld16(in.L0[i], rh, vo, so + i * 1024); ld16(in.L1[i], rh, vo + 4, so + i * 1024); ld16(in.L2[i], rh, vo + 8, so + i * 1024); }
}
DI void conv_load_u(const Params& p, ConvIn& in, int c, int lane) {
  const i32x4 ru = make_srd(p.ws + WS_KVT, D_ * S_ * 2);
#pragma unroll
  for (int i = 0; i < 4; ++i) ld16_sc1(in.U[i], ru, lane * 16, c * 4096u + i * 1024);
}
DI void conv_pin(ConvIn& in) {
#pragma unroll
  for (int i = 0; i < 4; ++i) { PIN(in.L0[i]); PIN(in.L1[i]); PIN(in.L2[i]); PIN(in.U[i]); }
  PIN(in.dv);
}
DI void conv_build(const ConvIn& in, LAS char* cb, int lane) {
#pragma unroll
  for (int i = 0; i < 4; ++i) {
    const unsigned o = (i * 64 + lane + 2) * 16;
    u32x4 c0, c2;
#pragma unroll
    for (int e = 0; e < 4; ++e) { c0[e] = __builtin_amdgcn_alignbit(in.L1[i][e], in.L0[i][e], 16); c2[e] = __builtin_amdgcn_alignbit(in.L2[i][e], in.L1[i][e], 16); }
    *(LAS u32x4*)(cb + 0 * CV_H + o) = c0;
    *(LAS u32x4*)(cb + 1 * CV_H + o) = in.L1[i];
    *(LAS u32x4*)(cb + 2 * CV_H + o) = c2;
    *(LAS u32x4*)(cb + 3 * CV_H + o) = in.L2[i];
    *(LAS u32x4*)(cb + CV_U + (i * 64 + lane) * 16) = in.U[i];
  }
  const u32x4 z4 = {0u, 0u, 0u, 0u};
  if (lane < 30) *(LAS u32x4*)(cb + CV_U + 4096 + lane * 16) = z4;
  if (lane == 0) {
    const unsigned w0 = in.L0[0].x, w1 = in.L0[0].y;
#pragma unroll
    for (int cc = 0; cc < 4; ++cc) *(LAS u32x4*)(cb + cc * CV_H) = z4;
    *(LAS u32x4*)(cb + 0 * CV_H + 16) = (u32x4){0u, 0u, 0u, w0 << 16};
    *(LAS u32x4*)(cb + 1 * CV_H + 16) = (u32x4){0u, 0u, 0u, w0};
    *(LAS u32x4*)(cb + 2 * CV_H + 16) = (u32x4){0u, 0u, w0 << 16, __builtin_amdgcn_alignbit(w1, w0, 16)};
    *(LAS u32x4*)(cb + 3 * CV_H + 16) = (u32x4){0u, 0u, w0, w1};
  }
}
template <int TRIPS>
DI void conv_mfma(LAS char* cb, int d0, int lane, f32x4 (&acc)[8]) {
  const int r = lane & 15, kg = lane >> 4, kap = kg >> 1, bp = kg & 1;
  unsigned a0 = (unsigned)(size_t)cb + (r & 3) * CV_H + 32 * kap + 16 * bp + 2 * (r & 12) + d0 * 64;
  unsigned a1 = a0 + 8;
  unsigned ub = (unsigned)(size_t)cb + CV_U + (4064 - 3584) - 32 * r + 32 * kap + 16 * bp + d0 * 64;
  PIN(a0); PIN(a1); PIN(ub);
#pragma unroll
  for (int g = 0; g < 8; ++g) acc[g] = (f32x4){0.f, 0.f, 0.f, 0.f};
  bf16x8 A[8], B[8];
#define CV_LDA(g) do { const s16x4 x0_ = *(LAS s16x4*)(size_t)(a0 + (g) * 512), x1_ = *(LAS s16x4*)(size_t)(a1 + (g) * 512); \
    A[g] = __builtin_bit_cast(bf16x8, __builtin_shufflevector(x0_, x1_, 0, 1, 2, 3, 4, 5, 6, 7)); } while (0)
#define CV_LDB(g) do { B[g] = *(LAS bf16x8*)(size_t)(ub + (7 - (g)) * 512); } while (0)
#pragma unroll
  for (int g = 0; g < 8; ++g) { CV_LDA(g); CV_LDB(7 - g); }
#pragma unroll 1
  for (int d = 0; d < TRIPS; ++d) {
    a0 += 64; a1 += 64; ub += 64; PIN(a0); PIN(a1); PIN(ub);
#pragma unroll
    for (int gm = 0; gm < 8; ++gm) {
#pragma unroll
      for (int gp = 7 - gm; gp >= 0; --gp) acc[gm + gp] = __builtin_amdgcn_mfma_f32_16x16x32_bf16(A[gm], B[gp], acc[gm + gp], 0, 0, 0);
      __builtin_amdgcn_sched_barrier(0);
      CV_LDA(gm); CV_LDB(7 - gm);
      __builtin_amdgcn_sched_barrier(0);
    }
  }
}
DI void conv_store(const Params& p, LAS char* cb, int c, int lane, unsigned dvb, const f32x4 (&acc)[8]) {
  const int r = lane & 15, kg = lane >> 4;
  const float dv = __uint_as_float(dvb << 16);
  const __amdgpu_buffer_rsrc_t rz = __builtin_amdgcn_make_buffer_rsrc((void*)(p.ws + WS_ZT), 0, D_ * S_ * 2, 0x00020000);
  u32x2 z[8];
#pragma unroll
  for (int g = 0; g < 8; ++g) {
    const int t = 256 * g + 16 * r + 4 * kg;
    const u32x2 kv = *(LAS u32x2*)(cb + CV_U + (2044 - t) * 2);
    const float y0 = fmaf(bhi(kv.y), dv, acc[g][0]), y1 = fmaf(blo(kv.y), dv, acc[g][1]), y2 = fmaf(bhi(kv.x), dv, acc[g][2]), y3 = fmaf(blo(kv.x), dv, acc[g][3]);
    z[g].x = pk2(y0, y1); z[g].y = pk2(y2, y3);
  }
#pragma unroll
  for (int g = 0; g < 8; g += 2) {
    const auto s0 = __builtin_amdgcn_permlane16_swap(z[g].x, z[g + 1].x, false, false);
    const auto s1 = __builtin_amdgcn_permlane16_swap(z[g].y, z[g + 1].y, false, false);
    const u32x4 o = {s0[0], s1[0], s0[1], s1[1]};
    const int gg = g + (kg & 1);
    __builtin_amdgcn_raw_buffer_store_b128(o, rz, c * 4096u + (256 * gg + 16 * r + 4 * (kg & 2)) * 2, 0, 16);
  }
}
struct ConvHalf { u32x4 La[4], Lb[4], U[2]; unsigned dv; };
DI void convh_load_h(const Params& p, ConvHalf& in, int c, int half, int lane) {
  const i32x4 rh = make_srd(p.h, D_ * S_ * 2), rd = make_srd(p.dv, D_ * 2);
  const unsigned vo = lane * 16 + half * 4, so = c * 4096u;
  asm volatile("buffer_load_ushort %0, %1, %2, 0 offen" : "=v"(in.dv) : "v"(c * 2u), "s"(rd) : "memory");
#pragma unroll
  for (int i = 0; i < 4; ++i) { ld16(in.La[i], rh, vo, so + i * 1024); ld16(in.Lb[i], rh, vo + 4, so + i * 1024); }
}
DI void convh_load_u(const Params& p, ConvHalf& in, int c, int half, int lane) {
  const i32x4 ru = make_srd(p.ws + WS_KVT, D_ * S_ * 2);
#pragma unroll
  for (int i = 0; i < 2; ++i) ld16_sc1(in.U[i], ru, lane * 16, c * 4096u + (half * 2 + i) * 1024);
}
DI void convh_build(ConvHalf& in, LAS char* cb, int half, int lane) {
#pragma unroll
  for (int i = 0; i < 4; ++i) { PIN(in.La[i]); PIN(in.Lb[i]); }
  PIN(in.U[0]); PIN(in.U[1]); PIN(in.dv);
  LAS char* hb = cb + 2 * half * CV_H;
#pragma unroll
  for (int i = 0; i < 4; ++i) {
    const unsigned o = (i * 64 + lane + 2) * 16;
    u32x4 c0;
#pragma unroll
    for (int e = 0; e < 4; ++e) c0[e] = __builtin_amdgcn_alignbit(in.Lb[i][e], in.La[i][e], 16);
    *(LAS u32x4*)(hb + o) = c0;
    *(LAS u32x4*)(hb + CV_H + o) = in.Lb[i];
  }
#pragma unroll
  for (int i = 0; i < 2; ++i) *(LAS u32x4*)(cb + CV_U + ((half * 2 + i) * 64 + lane) * 16) = in.U[i];
  if (half == 0) {
    const u32x4 z4 = {0u, 0u, 0u, 0u};
    if (lane < 30) *(LAS u32x4*)(cb + CV_U + 4096 + lane * 16) = z4;
    if (lane == 0) {
      const unsigned w0 = in.La[0].x, w1 = in.La[0].y;
#pragma unroll
      for (int cc = 0; cc < 4; ++cc) *(LAS u32x4*)(cb + cc * CV_H) = z4;
      *(LAS u32x4*)(cb + 0 * CV_H + 16) = (u32x4){0u, 0u, 0u, w0 << 16};
      *(LAS u32x4*)(cb + 1 * CV_H + 16) = (u32x4){0u, 0u, 0u, w0};
      *(LAS u32x4*)(cb + 2 * CV_H + 16) = (u32x4){0u, 0u, w0 << 16, __builtin_amdgcn_alignbit(w1, w0, 16)};
      *(LAS u32x4*)(cb + 3 * CV_H + 16) = (u32x4){0u, 0u, w0, w1};
    }
  }
}

DI void g2_issue_w(const i32x4& srdD, const unsigned (&vd)[5], unsigned smb, int nt, int w, int j) {
  const int fb = 4 * j + w < 54 ? 4 * j + w : 53;
  const unsigned so = nt * 160u + fb * 40960u, lo = smb + L_WR + (j & 3) * G2_WSLOT + w * 5120;
#pragma unroll
  for (int i = 0; i < 5; ++i) dma16(srdD, vd[i], so, lo + i * 1024);
}
DI void g2_wd_offsets(unsigned (&vd)[5], int lane) {
#pragma unroll
  for (int i = 0; i < 5; ++i) { const int idx = i * 64 + lane, rho = idx / 10, ch = idx - rho * 10; vd[i] = rho * 1280 + ch * 16; }
}
template <bool CONV>
DI void g1_loader(const Params& p, LAS char* sm, unsigned smb, int rb, int ft, int mt, int nt, int hw, int lane, unsigned& tag, int cch, unsigned& dvb, u32x4 (&wy)[3][5]) {
  const i32x4 srdQ = make_srd(p.q, S_ * D_ * 2), srdW = make_srd((hw >> 1) ? p.wu : p.wg, D_ * F_ * 2), srdC = make_srd(p.ws, WS_KVT);
  const unsigned vq0 = (lane >> 3) * 1280 + (((lane & 7) ^ (lane >> 4)) * 16);
  const unsigned vq1 = (lane >> 3) * 1280 + (((lane & 7) ^ (4 + (lane >> 4))) * 16);
  const unsigned vw0 = (lane >> 3) * 3456 + (((lane & 7) ^ (2 * ((lane >> 4) & 1))) * 16);
  const unsigned vw1 = (lane >> 3) * 3456 + (((lane & 7) ^ (2 * (((lane >> 4) & 1) | 2))) * 16);
  const unsigned qb = (unsigned)(rb * 256 + 64 * hw) * 1280u, wb = (unsigned)(32 * (hw & 1)) * 3456u + ft * 128u;
  const unsigned lq = smb + hw * 8192, lw = smb + 32768 + (hw >> 1) * 8192 + (hw & 1) * 4096;
  auto issue = [&](int s, int slot) {
    const unsigned so = slot * G1_SLOT;
#pragma unroll
    for (int i = 0; i < 4; ++i) dma16(srdW, (i & 1) ? vw1 : vw0, wb + (s * 64 + 8 * i) * 3456u, lw + so + i * 1024);
#pragma unroll
    for (int i = 0; i < 8; ++i) dma16(srdQ, (i & 1) ? vq1 : vq0, qb + i * 10240u + s * 128u, lq + so + i * 1024);
  };
  unsigned ep; ld4_sc1(ep, srdC, CW_EPOCH);
  BAR();
  issue(0, 0); issue(1, 1);
  WAITV(12);
  PIN(ep); tag = __builtin_amdgcn_readfirstlane(ep) + 1u;
  if ((hw & 1) == 0 && lane == 0) *(LAS unsigned*)(sm + L_PF + (hw >> 1) * 4) = ~tag;
  BAR();
  if (hw == 0) flag_set(p.ws, CW_ARRIVE + blockIdx.x * 4, tag, lane);
  issue(2, 2);
  const i32x4 srdD = make_srd(p.wd, F_ * D_ * 2);
  unsigned vd[5]; g2_wd_offsets(vd, lane);
  unsigned f1 = 0, f2 = 0; u32x2 st, st2; ConvHalf in;
  const unsigned f1off = lane < 16 ? CW_ST + (rb * 64 + hw * 16 + lane) * 16 : CW_ST + (mt * 16 + (lane & 15)) * 16;
  const unsigned f2off = CW_KV + ((CONV ? (cch >> 6) : 0) * 32 + (lane & 31)) * 16;
#pragma unroll
  for (int s = 1; s < 10; ++s) {
    const int nslot = (s + 2) % 3;
    if (s == 5) { if (CONV) WAITV(21); else WAITV(12); }
    else if (s == 6) WAITV(13);
    else if (s == 8) { if (CONV) WAITV(15); else WAITV(14); }
    else if (s == 9) WAITV(10);
    else WAITV(12);
    BAR();
    if (s == 4 && CONV) convh_load_h(p, in, cch, hw & 1, lane);
    if (s == 5) ld4_sc1(f1, srdC, f1off);
    if (s == 7) {
      PIN(f1);
      if (!__all(f1 == tag)) poll_all(p.ws, f1off, tag);
      ld8_sc1(st, srdC, WS_STATS + (rb * 256 + hw * 64 + lane) * 8, 0);
      ld8_sc1(st2, srdC, WS_STATS + (mt * 64 + lane) * 8, 0);
      if (CONV) ld4_sc1(f2, srdC, f2off);
    }
    if (s <= 7) issue(s + 2, nslot);
    if (s == 8) { g2_issue_w(srdD, vd, smb, nt, hw, 0); g2_issue_w(srdD, vd, smb, nt, hw, 1); }
    if (s == 9) {
      if (CONV) {
        PIN(f2);
        if (!__all(f2 == tag)) poll_all(p.ws, f2off, tag);
        convh_load_u(p, in, cch, hw & 1, lane);
      }
      PIN(st); PIN(st2);
      *(LAS u32x2*)(sm + L_ST + (hw * 64 + lane) * 8) = st;
      if (hw == 0) *(LAS u32x2*)(sm + L_S2 + lane * 8) = st2;
    }
  }
  BAR();
#pragma unroll
  for (int j = 0; j < 3; ++j)
#pragma unroll
    for (int i = 0; i < 5; ++i) ld16(wy[j][i], srdD, vd[i], nt * 160u + (4 * (j + 2) + hw) * 40960u);
  if (CONV) { WAITV(15); convh_build(in, sm + L_CVG + (hw >> 1) * CV_SZ, hw & 1, lane); dvb = in.dv; }
  WAITV(0);
#pragma unroll
  for (int j = 0; j < 3; ++j)
#pragma unroll
    for (int i = 0; i < 5; ++i) PIN(wy[j][i]);
}

template <bool SWAP>
DI void g1_epilogue(const Params& p, LAS char* sm, int rb, int ft, int wr, int wc, int lane, const f32x4 (&a1)[2][8], const f32x4 (&a2)[2][8], const f32x4 (&csg)[2], const f32x4 (&csu)[2], const f32x2 (&st)[8]) {
  const __amdgpu_buffer_rsrc_t rh = __builtin_amdgcn_make_buffer_rsrc((void*)(p.ws + WS_HM), 0, 54 * 128 * 1024, 0x00020000);
  const unsigned hb = (unsigned)((ft * 2 + wc) * 128 + rb * 16 + wr * 8) * 1024u + lane * 16;
#pragma unroll
  for (int m = 0; m < 8; ++m) {
    asm volatile("; gated epilogue, operand order %0" :: "i"(SWAP ? 1 : 0));
    const float nm = -st[m].x, rs = st[m].y, rsl = rs * -1.44269504089f, rs2 = rs * rs;
    f32x2 tg[4], tu[4], e[4];
#pragma unroll
    for (int h = 0; h < 4; ++h) {
      const int a = h >> 1, i = (h & 1) * 2;
      const f32x4 ga = SWAP ? a2[a][m] : a1[a][m], ua = SWAP ? a1[a][m] : a2[a][m];
      tg[h] = (f32x2){csg[a][i], csg[a][i + 1]} * nm + (f32x2){ga[i], ga[i + 1]};
      tu[h] = (f32x2){csu[a][i], csu[a][i + 1]} * nm + (f32x2){ua[i], ua[i + 1]};
    }
#pragma unroll
    for (int h = 0; h < 4; ++h) { const f32x2 gl = tg[h] * rsl; e[h].x = __builtin_amdgcn_exp2f(gl.x); e[h].y = __builtin_amdgcn_exp2f(gl.y); }
#pragma unroll
    for (int h = 0; h < 4; ++h) { tu[h] = (tg[h] * tu[h]) * rs2; e[h] = e[h] + 1.0f; }
#pragma unroll
    for (int h = 0; h < 4; ++h) { e[h].x = __builtin_amdgcn_rcpf(e[h].x); e[h].y = __builtin_amdgcn_rcpf(e[h].y); }
    u32x4 w;
#pragma unroll
    for (int h = 0; h < 4; ++h) { const f32x2 hv = tu[h] * e[h]; w[h] = pk2(hv.x, hv.y); }
    __builtin_amdgcn_raw_buffer_store_b128(w, rh, hb + m * 1024, 0, 16);
  }
}
DI void g1_math(const Params& p, LAS char* sm, int rb, int ft, int wid, int lane, unsigned tag, unsigned taskflag) {
  const int wr = wid >> 1, wc = wid & 1;
  const int n = lane & 15, kg = lane >> 4, q4 = (lane & 15) >> 2, pp = lane & 3;
  f32x4 a1[2][8], a2[2][8], cs[2];
#pragma unroll
  for (int a = 0; a < 2; ++a) {
    cs[a] = (f32x4){0.f, 0.f, 0.f, 0.f};
#pragma unroll
    for (int m = 0; m < 8; ++m) { a1[a][m] = (f32x4){0.f, 0.f, 0.f, 0.f}; a2[a][m] = (f32x4){0.f, 0.f, 0.f, 0.f}; }
  }
  u32x4 ones_ = {0x3f803f80u, 0x3f803f80u, 0x3f803f80u, 0x3f803f80u};
  PIN(ones_);
  const bf16x8 ones = __builtin_bit_cast(bf16x8, ones_);
  const unsigned tbase = (wr * 128 + n) * 128 + ((kg ^ ((n >> 1) & 7)) * 16);
  const int sw = 2 * (((q4 >> 1) & 1) | ((kg & 1) << 1));
  const unsigned wb0 = 32768 + (8 * kg + q4) * 128 + (((4 * wc + 0 + (pp >> 1)) ^ sw) * 16) + 8 * (pp & 1);
  const unsigned wb1 = 32768 + (8 * kg + q4) * 128 + (((4 * wc + 2 + (pp >> 1)) ^ sw) * 16) + 8 * (pp & 1);
  const unsigned o1 = wr * 8192, o2 = 8192 - o1;
  bf16x8 tP[8], tQ[8], gP[2], gQ[2], uP[2], uQ[2];
#define G1_RD(T, G, U, so, ks) do { LAS char* tp_ = sm + (so) + (tbase ^ ((ks) * 64)); \
    LAS char* w10_ = sm + (so) + wb0 + o1 + (ks) * 4096; LAS char* w11_ = sm + (so) + wb1 + o1 + (ks) * 4096; \
    LAS char* w20_ = sm + (so) + wb0 + o2 + (ks) * 4096; LAS char* w21_ = sm + (so) + wb1 + o2 + (ks) * 4096; \
    G[0] = ldtr(w10_, w10_ + 512); T[0] = *(LAS bf16x8*)(tp_); U[0] = ldtr(w20_, w20_ + 512); G[1] = ldtr(w11_, w11_ + 512); U[1] = ldtr(w21_, w21_ + 512); \
    _Pragma("unroll") for (int m = 1; m < 8; ++m) T[m] = *(LAS bf16x8*)(tp_ + 2048 * m); } while (0)
#define G1_MM(T, G, U) do { _Pragma("unroll") for (int m = 0; m < 8; ++m) { _Pragma("unroll") for (int a = 0; a < 2; ++a) { \
    a1[a][m] = __builtin_amdgcn_mfma_f32_16x16x32_bf16(G[a], T[m], a1[a][m], 0, 0, 0); a2[a][m] = __builtin_amdgcn_mfma_f32_16x16x32_bf16(U[a], T[m], a2[a][m], 0, 0, 0); } } } while (0)
#define G1_CS(G) do { cs[0] = __builtin_amdgcn_mfma_f32_16x16x32_bf16(G[0], ones, cs[0], 0, 0, 0); cs[1] = __builtin_amdgcn_mfma_f32_16x16x32_bf16(G[1], ones, cs[1], 0, 0, 0); } while (0)
#define G1_SCHED() do { _Pragma("unroll") for (int i_ = 0; i_ < 16; ++i_) { __builtin_amdgcn_sched_group_barrier(0x8, 1, 0); __builtin_amdgcn_sched_group_barrier(0x100, 1, 0); } __builtin_amdgcn_sched_group_barrier(0x8, 18, 0); } while (0)
  unsigned so = 0;
  BAR();
  STAMP_AT(1); STAMP_CY(14);
  G1_RD(tP, gP, uP, so, 0);
  G1_RD(tQ, gQ, uQ, so, 1); G1_MM(tP, gP, uP); G1_CS(gP); G1_SCHED();
#pragma unroll 1
  for (int s = 1; s < 10; ++s) {
    so = so == 2 * G1_SLOT ? 0 : so + G1_SLOT;
    BAR();
    if (s == 2 && taskflag) { WAITV(0); flag_set(p.ws, taskflag, tag, lane); }
    G1_RD(tP, gP, uP, so, 0); G1_MM(tQ, gQ, uQ); G1_CS(gQ); G1_SCHED();
    G1_RD(tQ, gQ, uQ, so, 1); G1_MM(tP, gP, uP); G1_CS(gP); G1_SCHED();
  }
  G1_CS(gQ);
  if (n == 0) { *(LAS f32x4*)(sm + L_CS + (wr * 64 + wc * 32 + 4 * kg) * 4) = cs[0]; *(LAS f32x4*)(sm + L_CS + (wr * 64 + wc * 32 + 16 + 4 * kg) * 4) = cs[1]; }
  BAR();
  f32x4 csg[2], csu[2];
#pragma unroll
  for (int a = 0; a < 2; ++a) { csg[a] = *(LAS f32x4*)(sm + L_CS + (wc * 32 + 16 * a + 4 * kg) * 4); csu[a] = *(LAS f32x4*)(sm + L_CS + (64 + wc * 32 + 16 * a + 4 * kg) * 4); }
  f32x2 st[8];
#pragma unroll
  for (int m = 0; m < 8; ++m) st[m] = *(LAS f32x2*)(sm + L_ST + (wr * 128 + 16 * m + n) * 8);
  G1_MM(tQ, gQ, uQ);
  STAMP_AT(2); STAMP_CY(15);
  if (wr == 0) g1_epilogue<false>(p, sm, rb, ft, wr, wc, lane, a1, a2, csg, csu, st);
  else g1_epilogue<true>(p, sm, rb, ft, wr, wc, lane, a1, a2, csg, csu, st);
  STAMP_AT(3);
}

template <int NPRE> constexpr int g2_w_after(int j) { return (j >= 1 && j + 3 <= 13 && j + 3 >= NPRE) ? 5 : 0; }
template <int NPRE> constexpr int g2_ops_after(int j) { return (j == 5 ? 1 : 0) + (j == 7 ? 3 : 0) + ((j >= 1 && j + 2 <= 13) ? 4 : 0) + g2_w_after<NPRE>(j); }
template <int NPRE> constexpr int g2_wait(int j) {
  return j == 0 ? 4 : (j == 1 ? g2_ops_after<NPRE>(1) : g2_w_after<NPRE>(j - 1) + g2_ops_after<NPRE>(j));
}
template <bool ISP>
DI void g2_loader(const Params& p, LAS char* sm, unsigned smb, int mt, int nt, int w, int lane, unsigned tag, unsigned hmflag) {
  const i32x4 srdD = make_srd(p.wd, F_ * D_ * 2), srdH = make_srd(p.ws + WS_HM, 54 * 128 * 1024), srdC = make_srd(p.ws, WS_KVT);
  unsigned vd[5]; g2_wd_offsets(vd, lane);
  const unsigned vh = lane * 16;
  auto fbof = [&](int j) { const int fb = 4 * j + w; return fb < 54 ? fb : 53; };
  auto issueH = [&](int j) {
    const unsigned so = mt * 4096u + fbof(j) * 131072u, lo = smb + L_HR + (j % 3) * 16384 + w * 4096;
#pragma unroll
    for (int i = 0; i < 4; ++i) {
      dma16_sc1(srdH, vh, so + i * 1024, lo + i * 1024);
    }
  };
  constexpr int XB = ISP ? 1 : 5, NREG = ISP ? 9 : 7, NPRE = XB + NREG;
  u32x4 wreg[NREG][5];
  if (ISP) g2_issue_w(srdD, vd, smb, nt, w, 0);
  else BAR();
#pragma unroll
  for (int j = 0; j < NREG; ++j) {
#pragma unroll
    for (int i = 0; i < 5; ++i) ld16(wreg[j][i], srdD, vd[i], nt * 160u + fbof(j + XB) * 40960u);
    if (!ISP && j == 0) {
      WAITV(5);
      flag_set(p.ws, hmflag, tag, lane);
      STAMP_AT(4);
    }
  }
  {
    const unsigned junk = smb + L_END - 256;
#pragma unroll
    for (int r = 0; r < (14 - NPRE + 3) / 4; ++r) {
      const int idx = r * 64 + lane;
      int j = NPRE + (idx >> 4); j = j < 14 ? j : 13;
      touch4(srdD, (unsigned)(fbof(j) * 32 + (mt & 3) * 8 + ((idx & 15) >> 1)) * 1280u + nt * 160u + (idx & 1) * 128u, 0, junk);
    }
  }
  if (ISP && w == 0) {
    poll_all(p.ws, CW_ST + (mt * 16 + (lane & 15)) * 16, tag);
    u32x2 sv; ld8_sc1(sv, srdC, WS_STATS + (mt * 64 + lane) * 8, 0);
    WAITV(0); PIN(sv);
    *(LAS u32x2*)(sm + L_S2 + lane * 8) = sv;
  }
  if (ISP && blockIdx.x == 255 && w == 3) {
    poll_all(p.ws, CW_ARRIVE + lane * 4, tag); poll_all(p.ws, CW_ARRIVE + 256 + lane * 4, tag);
    poll_all(p.ws, CW_ARRIVE + 512 + lane * 4, tag); poll_all(p.ws, CW_ARRIVE + 768 + lane * 4, tag);
    flag_set(p.ws, CW_EPOCH, tag, lane);
  }
  { const int j = lane < 14 ? lane : 0; poll_all(p.ws, CW_HM + (((mt >> 2) * 2 + ((mt >> 1) & 1)) * 54 + fbof(j)) * 16, tag); }
  STAMP_AT(5);
  { const int j = (lane >> 2) < 14 ? (lane >> 2) : 13;
    touch4_sc1(srdH, (unsigned)(fbof(j) * 128 + mt * 4 + (lane & 3)) * 1024u + nt * 128u, 0, smb + L_END - 256); }
  issueH(0); issueH(1); issueH(2);
  WAITV(8);
#pragma unroll
  for (int j = 0; j < NREG; ++j)
#pragma unroll
    for (int i = 0; i < 5; ++i) PIN(wreg[j][i]);
  BAR();
  STAMP_AT(6);
  unsigned zf = 0; u32x4 zr[3];
  const unsigned zoff = CW_Z + (nt * 80 + w * 20 + (lane < 20 ? lane : 0)) * 16;
  auto put_w = [&](int j) {
#pragma unroll
    for (int i = 0; i < 5; ++i) *(LAS u32x4*)(sm + L_WR + (j & 3) * G2_WSLOT + w * 5120 + i * 1024 + lane * 16) = wreg[j - XB][i];
  };
#pragma unroll
  for (int j = 0; j < 14; ++j) {
    if (j == 0) {
#pragma unroll
      for (int jj = XB; jj < 4; ++jj) put_w(jj);
    } else if (j + 3 >= XB && j + 3 < NPRE) put_w(j + 3);
    if (j == 5) ld4_sc1(zf, srdC, zoff);
    if (j == 7) {
      PIN(zf);
      if (!__all(zf == tag)) poll_all(p.ws, zoff, tag);
      const i32x4 srdZ = make_srd(p.ws + WS_ZT, D_ * S_ * 2);
#pragma unroll
      for (int r = 0; r < 3; ++r) {
        int task = r * 64 + lane; task = task < 160 ? task : 159;
        ld16_sc1(zr[r], srdZ, (unsigned)(nt * 80 + w * 20 + (task >> 3)) * 4096u + (mt * 64 + (task & 7) * 8) * 2, 0);
      }
    }
    if (j >= 1 && j + 2 <= 13) issueH(j + 2);
    if (g2_w_after<NPRE>(j)) g2_issue_w(srdD, vd, smb, nt, w, j + 3);
    if (j == 9) {
#pragma unroll
      for (int r = 0; r < 3; ++r) PIN(zr[r]);
#pragma unroll
      for (int r = 0; r < 3; ++r) { const int task = r * 64 + lane; if (task < 160) *(LAS u32x4*)(sm + L_ZS + (w * 20 + (task >> 3)) * ZS_RS + (task & 7) * 16) = zr[r]; }
    }
    if (j < 13) WAITV(g2_wait<NPRE>(j));
    BAR();
  }
}

template <int NY>
DI void g2_math(const Params& p, LAS char* sm, int w, int lane, unsigned tag, int zc, const u32x4 (&wy)[3][5]) {
  f32x4 acc[5][4];
#pragma unroll
  for (int a = 0; a < 5; ++a)
#pragma unroll
    for (int b = 0; b < 4; ++b) acc[a][b] = (f32x4){0.f, 0.f, 0.f, 0.f};
  const int kg = lane >> 4, q4 = (lane & 15) >> 2, pp = lane & 3;
  LAS char* hrd = sm + L_HR + w * 4096 + lane * 16;
  LAS char* wrd = sm + L_WR + w * 5120 + (4 * kg + q4) * 160 + pp * 8;
  bf16x8 hP[4], wP[5], hQ[4], wQ[5];
#define G2_RD(H, W, j) do { LAS char* hp_ = hrd + ((j) % 3) * 16384; LAS char* wp_ = wrd + ((j) & 3) * G2_WSLOT; \
    W[0] = ldtr(wp_, wp_ + 2560); H[0] = *(LAS bf16x8*)(hp_); H[1] = *(LAS bf16x8*)(hp_ + 1024); H[2] = *(LAS bf16x8*)(hp_ + 2048); H[3] = *(LAS bf16x8*)(hp_ + 3072); \
    _Pragma("unroll") for (int a = 1; a < 5; ++a) W[a] = ldtr(wp_ + a * 32, wp_ + a * 32 + 2560); } while (0)
#define G2_MM(H, W) do { _Pragma("unroll") for (int a = 0; a < 5; ++a) _Pragma("unroll") for (int b = 0; b < 4; ++b) \
    acc[a][b] = __builtin_amdgcn_mfma_f32_16x16x32_bf16(W[a], H[b], acc[a][b], 0, 0, 0); } while (0)
#define G2_SCHED() do { _Pragma("unroll") for (int i_ = 0; i_ < 14; ++i_) { __builtin_amdgcn_sched_group_barrier(0x8, 1, 0); __builtin_amdgcn_sched_group_barrier(0x100, 1, 0); } __builtin_amdgcn_sched_group_barrier(0x8, 6, 0); } while (0)
  if (zc >= 0) WAITV(4); else WAITV(0);
  BAR();
  STAMP_AT(9);
  G2_RD(hP, wP, 0);
  auto put_y = [&](int j) {
#pragma unroll
    for (int i = 0; i < 5; ++i) *(LAS u32x4*)(sm + L_WR + (j & 3) * G2_WSLOT + w * 5120 + i * 1024 + lane * 16) = wy[j - 2][i];
  };
  if (NY) { put_y(2); put_y(3); }
  if (zc >= 0) {
    WAITV(0);
    flag_set(p.ws, CW_Z + zc * 16, tag, lane);
  }
  BAR();
  G2_RD(hQ, wQ, 1); G2_MM(hP, wP); G2_SCHED();
  if (NY) put_y(4);
#pragma unroll
  for (int j = 2; j < 14; j += 2) {
    BAR();
    G2_RD(hP, wP, j); G2_MM(hQ, wQ); G2_SCHED();
    BAR();
    G2_RD(hQ, wQ, j + 1); G2_MM(hP, wP); G2_SCHED();
  }
  BAR();
  if (w < 2) G2_MM(hQ, wQ);
  STAMP_AT(10);
#pragma unroll
  for (int a = 0; a < 5; ++a)
#pragma unroll
    for (int b = 0; b < 4; ++b) *(LAS f32x4*)(sm + w * G2_PART + (16 * b + (lane & 15)) * G2_RS + a * 64 + kg * 16) = acc[a][b];
}
DI void g2_final_task(const Params& p, LAS char* sm, int mt, int nt, int gt, int lane, const u32x4& x) {
  const int b = gt / 10, ch = gt - b * 10, tk = b * 16 + (lane & 15);
  f32x4 a0 = (f32x4){0.f, 0.f, 0.f, 0.f}, a1 = a0;
#pragma unroll
  for (int w = 0; w < 4; ++w) { a0 += *(LAS f32x4*)(sm + w * G2_PART + tk * G2_RS + ch * 32); a1 += *(LAS f32x4*)(sm + w * G2_PART + tk * G2_RS + ch * 32 + 16); }
  LAS char* zp = sm + L_ZS + (ch * 8 + ((lane & 15) >> 2)) * ZS_RS + b * 32 + (lane & 3) * 8;
  const u32x2 z0 = __builtin_bit_cast(u32x2, __builtin_amdgcn_ds_read_tr16_b64_v4i16((LAS s16x4*)zp));
  const u32x2 z1 = __builtin_bit_cast(u32x2, __builtin_amdgcn_ds_read_tr16_b64_v4i16((LAS s16x4*)(zp + 4 * ZS_RS)));
  const f32x2 st = *(LAS f32x2*)(sm + L_S2 + tk * 8);
  const float rs = st.y, nmr = -st.x * rs;
  float o[8];
#pragma unroll
  for (int e = 0; e < 8; ++e) {
    const float xv = (e & 1) ? bhi(x[e >> 1]) : blo(x[e >> 1]);
    const unsigned zw = e < 4 ? z0[(e & 3) >> 1] : z1[(e & 3) >> 1];
    const float z = (e & 1) ? bhi(zw) : blo(zw);
    const float xn = fmaf(xv, rs, nmr);
    o[e] = fmaf(xn, z, xv) + (e < 4 ? a0[e] : a1[e - 4]);
  }
  const u32x4 r = {pk2(o[0], o[1]), pk2(o[2], o[3]), pk2(o[4], o[5]), pk2(o[6], o[7])};
  *(GAS u32x4*)(p.out + (mt * 64 + tk) * 640 + nt * 80 + ch * 8) = r;
}
DI u32x4 final_x(const Params& p, int mt, int nt, int gt, int lane) {
  const int b = gt / 10, ch = gt - b * 10;
  return *(const GAS u32x4*)(p.q + (mt * 64 + b * 16 + (lane & 15)) * 640 + nt * 80 + ch * 8);
}

DI void pair_send(LAS char* pb, LAS unsigned* pf, unsigned tag, int lane, const f32x4 (&acc)[8]) {
#pragma unroll
  for (int g = 0; g < 8; ++g) *(LAS f32x4*)(pb + g * 1024 + lane * 16) = acc[g];
  asm volatile("s_waitcnt lgkmcnt(0)" ::: "memory");
  if (lane == 0) *(volatile LAS unsigned*)pf = tag;
}
DI void pair_recv(LAS char* pb, LAS unsigned* pf, unsigned tag, int lane, f32x4 (&acc)[8]) {
#pragma unroll 1
  for (unsigned it = 0; it < (1u << 22); ++it) { if (*(volatile LAS unsigned*)pf == tag) break; __builtin_amdgcn_s_sleep(1); }
  asm volatile("" ::: "memory");
#pragma unroll
  for (int g = 0; g < 8; ++g) acc[g] += *(LAS f32x4*)(pb + g * 1024 + lane * 16);
}

__global__ __launch_bounds__(512, 2) void fused_kernel(Params p) {
  extern __shared__ __attribute__((aligned(16))) char smem_raw[];
  LAS char* sm = (LAS char*)smem_raw;
  const unsigned smb = (unsigned)(size_t)sm;
  const int lane = threadIdx.x & 63, wid = __builtin_amdgcn_readfirstlane(threadIdx.x >> 6);
  const int b = blockIdx.x, x = b & 7, i = b >> 3;
  const int mt = x * 4 + (i & 3), nt = i >> 2;
  STAMP_AT(0);
  if (i < 27) {
    const int mm = 27 * (x & 3) + i, ft = mm >> 2, rb = 4 * (x >> 2) + (mm & 3);
    if (wid < 4) {
      const unsigned ep = __hip_atomic_load((gu32*)(p.ws + CW_EPOCH), __ATOMIC_RELAXED, __HIP_MEMORY_SCOPE_AGENT);
      unsigned taskflag = 0;
      const int id = wid < 2 ? 2 * b + wid : 432 + b;
      if (wid < 3 && id < 512) { task_stats<true>(p, id, lane); taskflag = CW_ST + id * 16; }
      else BAR();
      const unsigned tag = __builtin_amdgcn_readfirstlane(ep) + 1u;
      asm volatile("" :: "s"(tag) : "memory");
      STAMP_AT(13);
      g1_math(p, sm, rb, ft, wid, lane, tag, taskflag);
      g2_loader<false>(p, sm, smb, mt, nt, wid, lane, tag, CW_HM + ((rb * 2 + (wid >> 1)) * 54 + ft * 2 + (wid & 1)) * 16);
    } else {
      const int hw = wid - 4;
      const bool conv = 2 * b < NCH_G;
      const int cch = 2 * b + (hw >> 1);
      unsigned tag, dvb = 0; int zc = -1;
      if (conv) {
        u32x4 wy[3][5];
        g1_loader<true>(p, sm, smb, rb, ft, mt, nt, hw, lane, tag, cch, dvb, wy);
        BAR();
        STAMP_AT(7);
        LAS char* cb = sm + L_CVG + (hw >> 1) * CV_SZ;
        LAS char* pb = sm + L_CP + (hw >> 1) * 8192;
        LAS unsigned* pf = (LAS unsigned*)(sm + L_PF + (hw >> 1) * 4);
        f32x4 acc[8];
        conv_mfma<4>(cb, (hw & 1) * 4, lane, acc);
        if (hw & 1) pair_send(pb, pf, tag, lane, acc);
        else { pair_recv(pb, pf, tag, lane, acc); conv_store(p, cb, cch, lane, dvb, acc); zc = cch; }
        STAMP_AT(8);
        g2_math<3>(p, sm, hw, lane, tag, zc, wy);
      } else {
        u32x4 wy[3][5];
        g1_loader<false>(p, sm, smb, rb, ft, mt, nt, hw, lane, tag, cch, dvb, wy);
        BAR();
        g2_math<3>(p, sm, hw, lane, tag, zc, wy);
      }
    }
  } else {
    const int cg = i - 27, pb = x * 5 + cg;
    const unsigned ep = __hip_atomic_load((gu32*)(p.ws + CW_EPOCH), __ATOMIC_RELAXED, __HIP_MEMORY_SCOPE_AGENT);
    const int hw = wid - 4, c0 = NCH_G + pb * NCH_P;
    if (wid < 4) {
      task_kv<0>(p, 2 * cg + (wid & 1), 4 * x + (wid >> 1), lane, [] {});
      const unsigned tag = __builtin_amdgcn_readfirstlane(ep) + 1u;
      asm volatile("" :: "s"(tag) : "memory");
      WAITV(0);
      flag_set(p.ws, CW_KV + ((2 * cg + (wid & 1)) * 32 + 4 * x + (wid >> 1)) * 16, tag, lane);
      BAR();
      if (wid == 0) flag_set(p.ws, CW_ARRIVE + b * 4, tag, lane);
      STAMP_AT(1);
      BAR();
      g2_loader<true>(p, sm, smb, mt, nt, wid, lane, tag, 0);
    } else {
      LAS char* cb = sm + L_CVP + hw * CV_SZ;
      const int cA = c0 + hw, cB = c0 + 4 + (hw >> 1);
      ConvIn in;
      task_kv<13>(p, 2 * cg + (wid & 1), 4 * x + (wid >> 1), lane, [&] { conv_load_h(p, in, cA, lane); });
      const unsigned tag = __builtin_amdgcn_readfirstlane(ep) + 1u;
      asm volatile("" :: "s"(tag) : "memory");
      if ((hw & 1) == 0 && lane == 0) *(LAS unsigned*)(sm + L_PFP + (hw >> 1) * 4) = ~tag;
      WAITV(0);
      flag_set(p.ws, CW_KV + ((2 * cg + (wid & 1)) * 32 + 4 * x + (wid >> 1)) * 16, tag, lane);
      BAR();
      STAMP_AT(1);
      poll_all(p.ws, CW_KV + (((lane < 32 ? cA : cB) >> 6) * 32 + (lane & 31)) * 16, tag);
      conv_load_u(p, in, cA, lane);
      WAITV(0);
      conv_pin(in);
      conv_build(in, cb, lane);
      unsigned dvb = in.dv;
      conv_load_h(p, in, cB, lane);
      conv_load_u(p, in, cB, lane);
      BAR();
      STAMP_AT(2);
      f32x4 acc[8];
      conv_mfma<8>(cb, 0, lane, acc);
      conv_store(p, cb, cA, lane, dvb, acc);
      STAMP_AT(3);
      WAITV(4);
      conv_pin(in);
      conv_build(in, cb, lane);
      dvb = in.dv;
      STAMP_AT(7);
      conv_mfma<4>(cb, (hw & 1) * 4, lane, acc);
      WAITV(0);
      flag_set(p.ws, CW_Z + cA * 16, tag, lane);
      LAS unsigned* pf = (LAS unsigned*)(sm + L_PFP + (hw >> 1) * 4);
      int zc = -1;
      if (hw & 1) pair_send(cb, pf, tag, lane, acc);
      else { pair_recv(cb + CV_SZ, pf, tag, lane, acc); conv_store(p, cb, cB, lane, dvb, acc); zc = cB; }
      STAMP_AT(8);
      u32x4 wy[3][5];
      g2_math<0>(p, sm, hw, lane, tag, zc, wy);
    }
  }
  const int gt0 = wid * 4 + (lane >> 4), gt1 = 32 + gt0;
  const u32x4 x0 = final_x(p, mt, nt, gt0, lane);
  u32x4 x1 = x0;
  if (wid < 2) x1 = final_x(p, mt, nt, gt1, lane);
  BAR();
  g2_final_task(p, sm, mt, nt, gt0, lane, x0);
  if (wid < 2) g2_final_task(p, sm, mt, nt, gt1, lane, x1);
  STAMP_AT(11);
}

extern "C" void kernel_launch(void* const* d_in, const int* in_sizes, int n_in, void* d_out, int out_size, void* d_ws, size_t ws_size, hipStream_t stream) {
  Params p;
  p.q = (const u16*)d_in[0]; p.k = (const u16*)d_in[1]; p.v = (const u16*)d_in[2]; p.sk = (const u16*)d_in[3]; p.h = (const u16*)d_in[4]; p.dv = (const u16*)d_in[5];
  p.wg = (const u16*)d_in[7]; p.wu = (const u16*)d_in[8]; p.wd = (const u16*)d_in[9]; p.out = (u16*)d_out; p.ws = (char*)d_ws;
  (void)hipFuncSetAttribute((const void*)fused_kernel, hipFuncAttributeMaxDynamicSharedMemorySize, L_END);
  fused_kernel<<<256, 512, L_END, stream>>>(p);
}
#define HAVE_INTER 1
#define HM_K16 1
```

```cpp
#include <hip/hip_runtime.h>
#include <stdint.h>
typedef unsigned short u16;
typedef __bf16 bf16x8 __attribute__((ext_vector_type(8)));
typedef __bf16 bf16x2 __attribute__((ext_vector_type(2)));
typedef short s16x4 __attribute__((ext_vector_type(4)));
typedef float f32x2 __attribute__((ext_vector_type(2)));
typedef float f32x4 __attribute__((ext_vector_type(4)));
typedef unsigned u32x2 __attribute__((ext_vector_type(2)));
typedef unsigned u32x4 __attribute__((ext_vector_type(4)));
typedef int i32x4 __attribute__((ext_vector_type(4)));
#define LAS __attribute__((address_space(3)))
#define GAS __attribute__((address_space(1)))
#define DI __device__ __forceinline__
typedef GAS unsigned gu32;

#define TOUCH 1
#define TOUCH_CODE 1
#define CODE_BACK 0
#define NT_G1 0
#define NT_KV 1
#define NT_H 1
#define NT_WD 0
constexpr int S_ = 2048, D_ = 640, F_ = 1728;
constexpr unsigned CW_EPOCH = 0;
constexpr unsigned CW_ARRIVE = 1024;
constexpr unsigned CW_KV = 4096;
constexpr unsigned CW_ST = 16384;
constexpr unsigned CW_HM = 24576;
constexpr unsigned CW_Z = 40960;
constexpr unsigned WS_STATS = 65536;
constexpr unsigned WS_KVT = 131072;
constexpr unsigned WS_ZT = WS_KVT + D_ * S_ * 2;
constexpr unsigned WS_HM = WS_ZT + D_ * S_ * 2;
constexpr unsigned WS_END = WS_HM + 54 * 128 * 1024;
constexpr unsigned WS_DBG = 32u << 20;
constexpr int G1_SLOT = 49152;
constexpr int L_CS = 153856, L_ST = L_CS + 512, L_S2 = L_ST + 2048, L_END = L_S2 + 512 + 64 + 256;
constexpr int L_HR = 0, L_WR = 49152, G2_WSLOT = 20480;
constexpr int L_CVG = 90112, L_CVP = 69632;
constexpr int L_ZS = 131072, ZS_RS = 144;
constexpr int L_CP = L_CVG + 2 * 21216, L_PF = L_CS - 64;
constexpr int L_PFP = L_S2 + 512 - 0;
constexpr int G2_RS = 336, G2_PART = 64 * G2_RS;
constexpr int CV_H = 4160, CV_U = 4 * CV_H, CV_SZ = CV_U + 4576;
static_assert(L_ZS + 80 * ZS_RS <= L_PF, "lds");
static_assert(L_CP + 2 * 8192 <= L_PF, "lds");
static_assert(L_CVP + 4 * CV_SZ + 64 <= L_S2, "lds");
constexpr int NCH_P = 6;
constexpr int NCH_G = D_ - 40 * NCH_P;
static_assert(NCH_G <= 2 * 216 && NCH_G % 2 == 0, "conv split");
constexpr unsigned SPIN_MAX = 1u << 16;

struct Params {
  const u16 *q, *k, *v, *sk, *h, *dv, *wg, *wu, *wd;
  u16* out; char* ws;
};

typedef const __attribute__((address_space(4))) Params* KP;
DI Params reload(KP kp) {
  asm volatile("" : "+s"(kp));
  Params r; r.q = kp->q; r.k = kp->k; r.v = kp->v; r.sk = kp->sk; r.h = kp->h; r.dv = kp->dv; r.wg = kp->wg; r.wu = kp->wu; r.wd = kp->wd; r.out = kp->out; r.ws = kp->ws;
  return r;
}
DI i32x4 make_srd(const void* p, unsigned bytes) {
  i32x4 r; unsigned long long a = (unsigned long long)p;
  r.x = (int)(unsigned)a;
  r.y = (int)((unsigned)(a >> 32) & 0xffffu);
  r.z = (int)bytes; r.w = 0x00020000; return r;
}
DI void dma16(const i32x4& srd, unsigned voff, unsigned soff, unsigned lds) {
  asm volatile("s_mov_b32 m0, %3\n\ts_nop 0\n\tbuffer_load_dwordx4 %0, %1, %2 offen lds" :: "v"(voff), "s"(srd), "s"(soff), "s"(lds) : "memory");
}
DI void dma16_sc1(const i32x4& srd, unsigned voff, unsigned soff, unsigned lds) {
  asm volatile("s_mov_b32 m0, %3\n\ts_nop 0\n\tbuffer_load_dwordx4 %0, %1, %2 offen sc1 lds" :: "v"(voff), "s"(srd), "s"(soff), "s"(lds) : "memory");
}
template <int NT = 0>
DI void dma16i(const i32x4& srd, unsigned voff, unsigned sbase, int simm, unsigned lbase, int limm) {
  unsigned t;
  if (NT) asm volatile("s_add_u32 m0, %4, %6\n\ts_add_u32 %0, %3, %5\n\tbuffer_load_dwordx4 %1, %2, %0 offen nt lds" : "=&s"(t) : "v"(voff), "s"(srd), "s"(sbase), "s"(lbase), "i"(simm), "i"(limm) : "memory", "scc");
  else asm volatile("s_add_u32 m0, %4, %6\n\ts_add_u32 %0, %3, %5\n\tbuffer_load_dwordx4 %1, %2, %0 offen lds" : "=&s"(t) : "v"(voff), "s"(srd), "s"(sbase), "s"(lbase), "i"(simm), "i"(limm) : "memory", "scc");
}
DI void dma16i_sc1(const i32x4& srd, unsigned voff, unsigned sbase, int simm, unsigned lbase, int limm) {
  unsigned t;
  asm volatile("s_add_u32 m0, %4, %6\n\ts_add_u32 %0, %3, %5\n\tbuffer_load_dwordx4 %1, %2, %0 offen sc1 lds" : "=&s"(t) : "v"(voff), "s"(srd), "s"(sbase), "s"(lbase), "i"(simm), "i"(limm) : "memory", "scc");
}
template <int NT = 0>
DI void ld16i(u32x4& d, const i32x4& srd, unsigned voff, unsigned sbase, int simm) {
  unsigned t;
  if (NT) asm volatile("s_add_u32 %1, %4, %5\n\tbuffer_load_dwordx4 %0, %2, %3, %1 offen nt" : "=v"(d), "=&s"(t) : "v"(voff), "s"(srd), "s"(sbase), "i"(simm) : "memory", "scc");
  else asm volatile("s_add_u32 %1, %4, %5\n\tbuffer_load_dwordx4 %0, %2, %3, %1 offen" : "=v"(d), "=&s"(t) : "v"(voff), "s"(srd), "s"(sbase), "i"(simm) : "memory", "scc");
}
template <int NT = 0>
DI void ld16(u32x4& d, const i32x4& srd, unsigned voff, unsigned soff) {
  if (NT) asm volatile("buffer_load_dwordx4 %0, %1, %2, %3 offen nt" : "=v"(d) : "v"(voff), "s"(srd), "s"(soff) : "memory");
  else asm volatile("buffer_load_dwordx4 %0, %1, %2, %3 offen" : "=v"(d) : "v"(voff), "s"(srd), "s"(soff) : "memory");
}
DI void ld16_sc1(u32x4& d, const i32x4& srd, unsigned voff, unsigned soff) { asm volatile("buffer_load_dwordx4 %0, %1, %2, %3 offen sc1" : "=v"(d) : "v"(voff), "s"(srd), "s"(soff) : "memory"); }
DI void ld8_sc1(u32x2& d, const i32x4& srd, unsigned voff, unsigned soff) { asm volatile("buffer_load_dwordx2 %0, %1, %2, %3 offen sc1" : "=v"(d) : "v"(voff), "s"(srd), "s"(soff) : "memory"); }
DI void ld8(u32x2& d, const i32x4& srd, unsigned voff, unsigned soff) { asm volatile("buffer_load_dwordx2 %0, %1, %2, %3 offen" : "=v"(d) : "v"(voff), "s"(srd), "s"(soff) : "memory"); }
DI void ld4_sc1(unsigned& d, const i32x4& srd, unsigned voff) { asm volatile("buffer_load_dword %0, %1, %2, 0 offen sc1" : "=v"(d) : "v"(voff), "s"(srd) : "memory"); }
DI void touchg(const void* base, unsigned voff, unsigned lds) {
  asm volatile("s_mov_b32 m0, %2\n\ts_nop 0\n\tglobal_load_lds_dword %0, %1" :: "v"(voff), "s"(base), "s"(lds) : "memory");
}
DI void touch4(const i32x4& srd, unsigned voff, unsigned soff, unsigned lds) {
  asm volatile("s_mov_b32 m0, %3\n\ts_nop 0\n\tbuffer_load_dword %0, %1, %2 offen lds" :: "v"(voff), "s"(srd), "s"(soff), "s"(lds) : "memory");
}
DI void touch4_sc1(const i32x4& srd, unsigned voff, unsigned soff, unsigned lds) {
  asm volatile("s_mov_b32 m0, %3\n\ts_nop 0\n\tbuffer_load_dword %0, %1, %2 offen sc1 lds" :: "v"(voff), "s"(srd), "s"(soff), "s"(lds) : "memory");
}
#define WAITV(n) asm volatile("s_waitcnt vmcnt(%0)" :: "i"(n) : "memory")
#define BAR() asm volatile("s_waitcnt lgkmcnt(0)\n\ts_barrier" ::: "memory")
#define PIN(x) asm volatile("" : "+v"(x))
DI float b2f(u16 x) { return __uint_as_float(((unsigned)x) << 16); }
DI float blo(unsigned x) { return __uint_as_float(x << 16); }
DI float bhi(unsigned x) { return __uint_as_float(x & 0xffff0000u); }
DI unsigned pk2(float lo, float hi) { bf16x2 r = {(__bf16)lo, (__bf16)hi}; return __builtin_bit_cast(unsigned, r); }
DI float rbf(float x) { return (float)(__bf16)x; }
DI float dot2(unsigned a, unsigned b, float c) { return __builtin_amdgcn_fdot2_f32_bf16(__builtin_bit_cast(bf16x2, a), __builtin_bit_cast(bf16x2, b), c, false); }
template <int CTRL> DI float dpp_add(float x) { return x + __int_as_float(__builtin_amdgcn_update_dpp(0, __float_as_int(x), CTRL, 0xf, 0xf, true)); }
DI float row16_sum(float x) { x = dpp_add<0x128>(x); x = dpp_add<0x124>(x); x = dpp_add<0x122>(x); x = dpp_add<0x121>(x); return x; }
DI bf16x8 ldtr(LAS char* p0, LAS char* p1) {
  s16x4 a = __builtin_amdgcn_ds_read_tr16_b64_v4i16((LAS s16x4*)p0), b = __builtin_amdgcn_ds_read_tr16_b64_v4i16((LAS s16x4*)p1);
  return __builtin_bit_cast(bf16x8, __builtin_shufflevector(a, b, 0, 1, 2, 3, 4, 5, 6, 7));
}
DI void poll_all(const char* ws, unsigned off, unsigned tag) {
  gu32* a = (gu32*)(ws + off);
#pragma unroll 1
  for (unsigned it = 0; it < SPIN_MAX; ++it) {
    const unsigned v = __hip_atomic_load(a, __ATOMIC_RELAXED, __HIP_MEMORY_SCOPE_AGENT);
    if (__all(v == tag)) break;
    __builtin_amdgcn_s_sleep(2);
  }
  asm volatile("" ::: "memory");
}
DI void flag_set(const char* ws, unsigned off, unsigned tag, int lane) {
  if (lane == 0) __hip_atomic_store((gu32*)(ws + off), tag, __ATOMIC_RELAXED, __HIP_MEMORY_SCOPE_AGENT);
}
#define STAMP_CY(idx) do {} while (0)
#define STAMP_AT(idx) do {} while (0)

template <int NX, class Hook>
DI void task_kv(const Params& p, int cg, int tb, int lane, Hook hook) {
  const int c0 = cg * 64 + 8 * (lane & 7), t0 = tb * 64 + 8 * (lane >> 3);
  const i32x4 rk = make_srd(p.k, S_ * D_ * 2), rv = make_srd(p.v, S_ * D_ * 2), rsk = make_srd(p.sk, D_ * 8);
  u32x4 kk[11], vv[8], ss[4];
#pragma unroll
  for (int j = 0; j < 4; ++j) ld16(ss[j], rsk, (unsigned)(c0 * 8), j * 16);
#pragma unroll
  for (int j = 0; j < 11; ++j) { const int t = t0 - 3 + j; ld16<NT_KV>(kk[j], rk, t >= 0 ? (unsigned)(t * 1280 + c0 * 2) : 0xfffffff0u, 0); }
#pragma unroll
  for (int j = 0; j < 8; ++j) ld16<NT_KV>(vv[j], rv, (unsigned)(t0 * 1280 + c0 * 2), j * 1280);
  hook();
  const __amdgpu_buffer_rsrc_t ro = __builtin_amdgcn_make_buffer_rsrc((void*)(p.ws + WS_KVT), 0, D_ * S_ * 2, 0x00020000);
  WAITV(NX);
  STAMP_AT(12);
#pragma unroll
  for (int j = 0; j < 4; ++j) PIN(ss[j]);
#pragma unroll
  for (int j = 0; j < 11; ++j) PIN(kk[j]);
#pragma unroll
  for (int j = 0; j < 8; ++j) PIN(vv[j]);
#pragma unroll
  for (int cp = 0; cp < 4; ++cp) {
    const f32x2 s0 = {blo(ss[cp].x), blo(ss[cp].z)}, s1 = {bhi(ss[cp].x), bhi(ss[cp].z)}, s2 = {blo(ss[cp].y), blo(ss[cp].w)}, s3 = {bhi(ss[cp].y), bhi(ss[cp].w)};
    f32x2 kx[11];
#pragma unroll
    for (int j = 0; j < 11; ++j) { const unsigned w = kk[j][cp]; kx[j] = (f32x2){blo(w), bhi(w)}; }
    f32x2 o[8];
    __builtin_amdgcn_sched_barrier(0);
#pragma unroll
    for (int j = 0; j < 8; ++j) o[j] = kx[j] * s3;
    __builtin_amdgcn_sched_barrier(0);
#pragma unroll
    for (int j = 0; j < 8; ++j) o[j] = kx[j + 1] * s2 + o[j];
    __builtin_amdgcn_sched_barrier(0);
#pragma unroll
    for (int j = 0; j < 8; ++j) o[j] = kx[j + 2] * s1 + o[j];
    __builtin_amdgcn_sched_barrier(0);
#pragma unroll
    for (int j = 0; j < 8; ++j) o[j] = kx[j + 3] * s0 + o[j];
    __builtin_amdgcn_sched_barrier(0);
    f32x2 vx[8];
#pragma unroll
    for (int j = 0; j < 8; ++j) { const unsigned w = vv[j][cp]; vx[j] = (f32x2){blo(w), bhi(w)}; }
    __builtin_amdgcn_sched_barrier(0);
#pragma unroll
    for (int j = 0; j < 8; ++j) o[j] = o[j] * vx[j];
    __builtin_amdgcn_sched_barrier(0);
    const u32x4 r0 = {pk2(o[7].x, o[6].x), pk2(o[5].x, o[4].x), pk2(o[3].x, o[2].x), pk2(o[1].x, o[0].x)};
    const u32x4 r1 = {pk2(o[7].y, o[6].y), pk2(o[5].y, o[4].y), pk2(o[3].y, o[2].y), pk2(o[1].y, o[0].y)};
    __builtin_amdgcn_raw_buffer_store_b128(r0, ro, (unsigned)((c0 + 2 * cp) * 4096 + (2040 - t0) * 2), 0, 16);
    __builtin_amdgcn_raw_buffer_store_b128(r1, ro, (unsigned)((c0 + 2 * cp + 1) * 4096 + (2040 - t0) * 2), 0, 16);
  }
}
template <bool PRE>
DI void task_stats(const Params& p, int id, int lane) {
  const i32x4 rq = make_srd(p.q, S_ * D_ * 2);
  const int row = id * 4 + (lane >> 4), l16 = lane & 15;
  u32x4 x[5];
#pragma unroll
  for (int j = 0; j < 5; ++j) ld16<NT_G1>(x[j], rq, (unsigned)row * 1280u + l16 * 16, j * 256);
  const __amdgpu_buffer_rsrc_t rs = __builtin_amdgcn_make_buffer_rsrc((void*)(p.ws + WS_STATS), 0, S_ * 8, 0x00020000);
  if (PRE) BAR();
  WAITV(0);
  STAMP_AT(12);
#pragma unroll
  for (int j = 0; j < 5; ++j) PIN(x[j]);
  float s4[4] = {0.f, 0.f, 0.f, 0.f};
#pragma unroll
  for (int j = 0; j < 5; ++j)
#pragma unroll
    for (int e = 0; e < 4; ++e) s4[e] = dot2(x[j][e], 0x3f803f80u, s4[e]);
  const float mean = row16_sum((s4[0] + s4[1]) + (s4[2] + s4[3])) * (1.0f / 640.0f);
  f32x2 q2[2] = {{0.f, 0.f}, {0.f, 0.f}};
#pragma unroll
  for (int j = 0; j < 5; ++j)
#pragma unroll
    for (int e = 0; e < 4; ++e) { const unsigned w = x[j][e]; const f32x2 d = (f32x2){blo(w), bhi(w)} - mean; q2[e & 1] = d * d + q2[e & 1]; }
  const float var = row16_sum((q2[0].x + q2[0].y) + (q2[1].x + q2[1].y)) * (1.0f / 640.0f);
  const float mb = rbf(mean), sb = rbf(__builtin_amdgcn_sqrtf(rbf(rbf(var) + 1e-5f)));
  u32x2 o; o.x = __float_as_uint(mb); o.y = __float_as_uint(1.0f / sb);
  __builtin_amdgcn_raw_buffer_store_b64(o, rs, l16 == 0 ? (unsigned)row * 8u : 0xfffffff0u, 0, 16);
}

struct ConvIn { u32x4 L0[4], L1[4], L2[4], U[4]; unsigned dv; };
DI void conv_load_h(const Params& p, ConvIn& in, int c, int lane) {
  const i32x4 rh = make_srd(p.h, D_ * S_ * 2), rd = make_srd(p.dv, D_ * 2);
  const unsigned vo = lane * 16, so = c * 4096u;
  asm volatile("buffer_load_ushort %0, %1, %2, 0 offen" : "=v"(in.dv) : "v"(c * 2u), "s"(rd) : "memory");
#pragma unroll
  for (int i = 0; i < 4; ++i) { ld16<NT_H>(in.L0[i], rh, vo, so + i * 1024); ld16(in.L1[i], rh, vo + 4, so + i * 1024); ld16(in.L2[i], rh, vo + 8, so + i * 1024); }
}
DI void conv_load_u(const Params& p, ConvIn& in, int c, int lane) {
  const i32x4 ru = make_srd(p.ws + WS_KVT, D_ * S_ * 2);
#pragma unroll
  for (int i = 0; i < 4; ++i) ld16_sc1(in.U[i], ru, lane * 16, c * 4096u + i * 1024);
}
DI void conv_pin(ConvIn& in) {
#pragma unroll
  for (int i = 0; i < 4; ++i) { PIN(in.L0[i]); PIN(in.L1[i]); PIN(in.L2[i]); PIN(in.U[i]); }
  PIN(in.dv);
}
DI void conv_build(const ConvIn& in, LAS char* cb, int lane) {
#pragma unroll
  for (int i = 0; i < 4; ++i) {
    const unsigned o = (i * 64 + lane + 2) * 16;
    u32x4 c0, c2;
#pragma unroll
    for (int e = 0; e < 4; ++e) { c0[e] = __builtin_amdgcn_alignbit(in.L1[i][e], in.L0[i][e], 16); c2[e] = __builtin_amdgcn_alignbit(in.L2[i][e], in.L1[i][e], 16); }
    *(LAS u32x4*)(cb + 0 * CV_H + o) = c0;
    *(LAS u32x4*)(cb + 1 * CV_H + o) = in.L1[i];
    *(LAS u32x4*)(cb + 2 * CV_H + o) = c2;
    *(LAS u32x4*)(cb + 3 * CV_H + o) = in.L2[i];
    *(LAS u32x4*)(cb + CV_U + (i * 64 + lane) * 16) = in.U[i];
  }
  const u32x4 z4 = {0u, 0u, 0u, 0u};
  if (lane < 30) *(LAS u32x4*)(cb + CV_U + 4096 + lane * 16) = z4;
  if (lane == 0) {
    const unsigned w0 = in.L0[0].x, w1 = in.L0[0].y;
#pragma unroll
    for (int cc = 0; cc < 4; ++cc) *(LAS u32x4*)(cb + cc * CV_H) = z4;
    *(LAS u32x4*)(cb + 0 * CV_H + 16) = (u32x4){0u, 0u, 0u, w0 << 16};
    *(LAS u32x4*)(cb + 1 * CV_H + 16) = (u32x4){0u, 0u, 0u, w0};
    *(LAS u32x4*)(cb + 2 * CV_H + 16) = (u32x4){0u, 0u, w0 << 16, __builtin_amdgcn_alignbit(w1, w0, 16)};
    *(LAS u32x4*)(cb + 3 * CV_H + 16) = (u32x4){0u, 0u, w0, w1};
  }
}
template <int TRIPS>
DI void conv_mfma(LAS char* cb, int d0, int lane, f32x4 (&acc)[8]) {
  const int r = lane & 15, kg = lane >> 4, kap = kg >> 1, bp = kg & 1;
  unsigned a0 = (unsigned)(size_t)cb + (r & 3) * CV_H + 32 * kap + 16 * bp + 2 * (r & 12) + d0 * 64;
  unsigned a1 = a0 + 8;
  unsigned ub = (unsigned)(size_t)cb + CV_U + (4064 - 3584) - 32 * r + 32 * kap + 16 * bp + d0 * 64;
  PIN(a0); PIN(a1); PIN(ub);
#pragma unroll
  for (int g = 0; g < 8; ++g) acc[g] = (f32x4){0.f, 0.f, 0.f, 0.f};
  bf16x8 A[8], B[8];
#define CV_LDA(g) do { const s16x4 x0_ = *(LAS s16x4*)(size_t)(a0 + (g) * 512), x1_ = *(LAS s16x4*)(size_t)(a1 + (g) * 512); \
    A[g] = __builtin_bit_cast(bf16x8, __builtin_shufflevector(x0_, x1_, 0, 1, 2, 3, 4, 5, 6, 7)); } while (0)
#define CV_LDB(g) do { B[g] = *(LAS bf16x8*)(size_t)(ub + (7 - (g)) * 512); } while (0)
#pragma unroll
  for (int g = 0; g < 8; ++g) { CV_LDA(g); CV_LDB(7 - g); }
#pragma unroll 1
  for (int d = 0; d < TRIPS; ++d) {
    a0 += 64; a1 += 64; ub += 64; PIN(a0); PIN(a1); PIN(ub);
#pragma unroll
    for (int gm = 0; gm < 8; ++gm) {
#pragma unroll
      for (int gp = 7 - gm; gp >= 0; --gp) acc[gm + gp] = __builtin_amdgcn_mfma_f32_16x16x32_bf16(A[gm], B[gp], acc[gm + gp], 0, 0, 0);
      __builtin_amdgcn_sched_barrier(0);
      CV_LDA(gm); CV_LDB(7 - gm);
      __builtin_amdgcn_sched_barrier(0);
    }
  }
}
DI void conv_store(const Params& p, LAS char* cb, int c, int lane, unsigned dvb, const f32x4 (&acc)[8]) {
  const int r = lane & 15, kg = lane >> 4;
  const float dv = __uint_as_float(dvb << 16);
  const __amdgpu_buffer_rsrc_t rz = __builtin_amdgcn_make_buffer_rsrc((void*)(p.ws + WS_ZT), 0, D_ * S_ * 2, 0x00020000);
  u32x2 z[8];
#pragma unroll
  for (int g = 0; g < 8; ++g) {
    const int t = 256 * g + 16 * r + 4 * kg;
    const u32x2 kv = *(LAS u32x2*)(cb + CV_U + (2044 - t) * 2);
    const float y0 = fmaf(bhi(kv.y), dv, acc[g][0]), y1 = fmaf(blo(kv.y), dv, acc[g][1]), y2 = fmaf(bhi(kv.x), dv, acc[g][2]), y3 = fmaf(blo(kv.x), dv, acc[g][3]);
    z[g].x = pk2(y0, y1); z[g].y = pk2(y2, y3);
  }
#pragma unroll
  for (int g = 0; g < 8; g += 2) {
    const auto s0 = __builtin_amdgcn_permlane16_swap(z[g].x, z[g + 1].x, false, false);
    const auto s1 = __builtin_amdgcn_permlane16_swap(z[g].y, z[g + 1].y, false, false);
    const u32x4 o = {s0[0], s1[0], s0[1], s1[1]};
    const int gg = g + (kg & 1);
    __builtin_amdgcn_raw_buffer_store_b128(o, rz, c * 4096u + (256 * gg + 16 * r + 4 * (kg & 2)) * 2, 0, 16);
  }
}
struct ConvHalf { u32x4 La[4], Lb[4], U[2]; unsigned dv; };
DI void convh_load_h(const Params& p, ConvHalf& in, int c, int half, int lane) {
  const i32x4 rh = make_srd(p.h, D_ * S_ * 2), rd = make_srd(p.dv, D_ * 2);
  const unsigned vo = lane * 16 + half * 4, so = c * 4096u;
  asm volatile("buffer_load_ushort %0, %1, %2, 0 offen" : "=v"(in.dv) : "v"(c * 2u), "s"(rd) : "memory");
#pragma unroll
  for (int i = 0; i < 4; ++i) { ld16<NT_H>(in.La[i], rh, vo, so + i * 1024); ld16(in.Lb[i], rh, vo + 4, so + i * 1024); }
}
DI void convh_load_u(const Params& p, ConvHalf& in, int c, int half, int lane) {
  const i32x4 ru = make_srd(p.ws + WS_KVT, D_ * S_ * 2);
#pragma unroll
  for (int i = 0; i < 2; ++i) ld16_sc1(in.U[i], ru, lane * 16, c * 4096u + (half * 2 + i) * 1024);
}
DI void convh_build(ConvHalf& in, LAS char* cb, int half, int lane) {
#pragma unroll
  for (int i = 0; i < 4; ++i) { PIN(in.La[i]); PIN(in.Lb[i]); }
  PIN(in.U[0]); PIN(in.U[1]); PIN(in.dv);
  LAS char* hb = cb + 2 * half * CV_H;
#pragma unroll
  for (int i = 0; i < 4; ++i) {
    const unsigned o = (i * 64 + lane + 2) * 16;
    u32x4 c0;
#pragma unroll
    for (int e = 0; e < 4; ++e) c0[e] = __builtin_amdgcn_alignbit(in.Lb[i][e], in.La[i][e], 16);
    *(LAS u32x4*)(hb + o) = c0;
    *(LAS u32x4*)(hb + CV_H + o) = in.Lb[i];
  }
#pragma unroll
  for (int i = 0; i < 2; ++i) *(LAS u32x4*)(cb + CV_U + ((half * 2 + i) * 64 + lane) * 16) = in.U[i];
  if (half == 0) {
    const u32x4 z4 = {0u, 0u, 0u, 0u};
    if (lane < 30) *(LAS u32x4*)(cb + CV_U + 4096 + lane * 16) = z4;
    if (lane == 0) {
      const unsigned w0 = in.La[0].x, w1 = in.La[0].y;
#pragma unroll
      for (int cc = 0; cc < 4; ++cc) *(LAS u32x4*)(cb + cc * CV_H) = z4;
      *(LAS u32x4*)(cb + 0 * CV_H + 16) = (u32x4){0u, 0u, 0u, w0 << 16};
      *(LAS u32x4*)(cb + 1 * CV_H + 16) = (u32x4){0u, 0u, 0u, w0};
      *(LAS u32x4*)(cb + 2 * CV_H + 16) = (u32x4){0u, 0u, w0 << 16, __builtin_amdgcn_alignbit(w1, w0, 16)};
      *(LAS u32x4*)(cb + 3 * CV_H + 16) = (u32x4){0u, 0u, w0, w1};
    }
  }
}

DI void g2_issue_w(const i32x4& srdD, const unsigned (&vd)[5], unsigned smb, int nt, int w, int j) {
  const unsigned sb = nt * 160u + (j < 13 ? w : (w < 2 ? w : 1)) * 40960u, lb = smb + L_WR + w * 5120;
#pragma unroll
  for (int i = 0; i < 5; ++i) dma16i<NT_WD>(srdD, vd[i], sb, j * 163840, lb, (j & 3) * G2_WSLOT + i * 1024);
}
DI void g2_wd_offsets(unsigned (&vd)[5], int lane) {
#pragma unroll
  for (int i = 0; i < 5; ++i) { const int idx = i * 64 + lane, rho = idx / 10, ch = idx - rho * 10; vd[i] = rho * 1280 + ch * 16; }
}
template <bool CONV>
DI void g1_loader(KP kp, Params p, unsigned long long pc0, LAS char* sm, unsigned smb, int rb, int ft, int mt, int nt, int hw, int lane, unsigned& tag, int cch, unsigned& dvb, u32x4 (&wy)[3][5]) {
  const i32x4 srdQ = make_srd(p.q, S_ * D_ * 2), srdW = make_srd((hw >> 1) ? p.wu : p.wg, D_ * F_ * 2), srdC = make_srd(p.ws, WS_KVT);
  const unsigned vq0 = (lane >> 3) * 1280 + (((lane & 7) ^ (lane >> 4)) * 16);
  const unsigned vq1 = (lane >> 3) * 1280 + (((lane & 7) ^ (4 + (lane >> 4))) * 16);
  const unsigned vw0 = (lane >> 3) * 3456 + (((lane & 7) ^ (2 * ((lane >> 4) & 1))) * 16);
  const unsigned vw1 = (lane >> 3) * 3456 + (((lane & 7) ^ (2 * (((lane >> 4) & 1) | 2))) * 16);
  const unsigned qb = (unsigned)(rb * 256 + 64 * hw) * 1280u, wb = (unsigned)(32 * (hw & 1)) * 3456u + ft * 128u;
  const unsigned lq = smb + hw * 8192, lw = smb + 32768 + (hw >> 1) * 8192 + (hw & 1) * 4096;
  auto issue = [&](int s, int slot) {
#pragma unroll
    for (int i = 0; i < 4; ++i) dma16i<NT_G1>(srdW, (i & 1) ? vw1 : vw0, wb, (s * 64 + 8 * i) * 3456, lw, slot * G1_SLOT + i * 1024);
#pragma unroll
    for (int i = 0; i < 8; ++i) dma16i<NT_G1>(srdQ, (i & 1) ? vq1 : vq0, qb, i * 10240 + s * 128, lq, slot * G1_SLOT + i * 1024);
  };
  const int xq = blockIdx.x & 7, wv = (blockIdx.x >> 3) * 4 + hw;
  const unsigned junk = smb + L_END - 256;
  const i32x4 srdD = make_srd(p.wd, F_ * D_ * 2);
  auto touch_slot = [&](int sl) {
    const int g = wv >> 5, sub = wv & 31, gsel = (sl + 1) % 3, st = sl + 3;
    unsigned fo = (((27 * (xq & 3)) >> 2) + (lane & 7)) * 128u; fo = fo < 3456u ? fo : 3328u;
    const unsigned vt = (unsigned)((xq >> 2) * 1024 + (sub & 15) * 64 + lane) * 1280u + st * 128u;
    const unsigned vw = (unsigned)(st * 64 + ((sub & 15) >> 1) * 8 + (lane >> 3)) * 3456u + fo;
    const bool stage = TOUCH && g == gsel;
    const void* base = (!stage || sub < 16) ? (const void*)p.q : ((sub & 1) ? (const void*)p.wu : (const void*)p.wg);
    touchg(base, stage ? (sub < 16 ? vt : vw) : 0u, junk);
  };
  if (wv < 7) {
    touchg((const void*)(pc0 & ~127ull), (unsigned)(wv * 64 + lane) * 128u, junk);
  }
  unsigned ep; ld4_sc1(ep, srdC, CW_EPOCH);
  BAR();
  ConvHalf in;
  issue(0, 0); issue(1, 1);
  if (CONV) convh_load_h(p, in, cch, hw & 1, lane);
  touch_slot(-1);
  if (CONV) WAITV(22); else WAITV(13);
  PIN(ep); tag = __builtin_amdgcn_readfirstlane(ep) + 1u;
  if ((hw & 1) == 0 && lane == 0) *(LAS unsigned*)(sm + L_PF + (hw >> 1) * 4) = ~tag;
  BAR();
  if (hw == 0) flag_set(p.ws, CW_ARRIVE + blockIdx.x * 4, tag, lane);
  issue(2, 2); touch_slot(0);
  unsigned vd[5]; g2_wd_offsets(vd, lane);
  unsigned f1 = 0, f2 = 0; u32x2 st, st2;
  const unsigned f1off = lane < 16 ? CW_ST + (rb * 64 + hw * 16 + lane) * 16 : CW_ST + (mt * 16 + (lane & 15)) * 16;
  const unsigned f2off = CW_KV + ((CONV ? (cch >> 6) : 0) * 32 + (lane & 31)) * 16;
#pragma unroll
  for (int s = 1; s < 10; ++s) {
    const int nslot = (s + 2) % 3;
    if (s == 1) { if (CONV) WAITV(23); else WAITV(14); }
    else if (s == 6) WAITV(15);
    else if (s == 8) { if (CONV) WAITV(16); else WAITV(15); }
    else if (s == 9) WAITV(10);
    else WAITV(14);
    BAR();
    if (s == 4 || s == 7) p = reload(kp);
    if (s == 5) ld4_sc1(f1, srdC, f1off);
    if (s == 7) {
      PIN(f1);
      if (!__all(f1 == tag)) poll_all(p.ws, f1off, tag);
      ld8_sc1(st, srdC, WS_STATS + (rb * 256 + hw * 64 + lane) * 8, 0);
      ld8_sc1(st2, srdC, WS_STATS + (mt * 64 + lane) * 8, 0);
      if (CONV) ld4_sc1(f2, srdC, f2off);
    }
    if (s <= 7) issue(s + 2, nslot);
    if (s == 8) { g2_issue_w(srdD, vd, smb, nt, hw, 0); g2_issue_w(srdD, vd, smb, nt, hw, 1); }
    if (s <= 6) touch_slot(s);
    if (s == 9) {
      if (CONV) {
        PIN(f2);
        if (!__all(f2 == tag)) poll_all(p.ws, f2off, tag);
        convh_load_u(p, in, cch, hw & 1, lane);
      }
      PIN(st); PIN(st2);
      *(LAS u32x2*)(sm + L_ST + (hw * 64 + lane) * 8) = st;
      if (hw == 0) *(LAS u32x2*)(sm + L_S2 + lane * 8) = st2;
    }
  }
  BAR();
#pragma unroll
  for (int j = 0; j < 3; ++j)
#pragma unroll
    for (int i = 0; i < 5; ++i) ld16i<NT_WD>(wy[j][i], srdD, vd[i], nt * 160u + hw * 40960u, (j + 2) * 163840);
  if (CONV) { WAITV(15); convh_build(in, sm + L_CVG + (hw >> 1) * CV_SZ, hw & 1, lane); dvb = in.dv; }
  WAITV(0);
#pragma unroll
  for (int j = 0; j < 3; ++j)
#pragma unroll
    for (int i = 0; i < 5; ++i) PIN(wy[j][i]);
}

template <bool SWAP>
DI void g1_epilogue(const Params& p, LAS char* sm, int rb, int ft, int wr, int wc, int lane, const f32x4 (&a1)[2][8], const f32x4 (&a2)[2][8], const f32x4 (&csg)[2], const f32x4 (&csu)[2], const f32x2 (&st)[8]) {
  const __amdgpu_buffer_rsrc_t rh = __builtin_amdgcn_make_buffer_rsrc((void*)(p.ws + WS_HM), 0, 54 * 128 * 1024, 0x00020000);
  const unsigned hb = (unsigned)((ft * 2 + wc) * 128 + rb * 16 + wr * 8) * 1024u + lane * 16;
#pragma unroll
  for (int m = 0; m < 8; ++m) {
    asm volatile("; gated epilogue, operand order %0" :: "i"(SWAP ? 1 : 0));
    const float nm = -st[m].x, rs = st[m].y, rsl = rs * -1.44269504089f, rs2 = rs * rs;
    f32x2 tg[4], tu[4], e[4];
#pragma unroll
    for (int h = 0; h < 4; ++h) {
      const int a = h >> 1, i = (h & 1) * 2;
      const f32x4 ga = SWAP ? a2[a][m] : a1[a][m], ua = SWAP ? a1[a][m] : a2[a][m];
      tg[h] = (f32x2){csg[a][i], csg[a][i + 1]} * nm + (f32x2){ga[i], ga[i + 1]};
      tu[h] = (f32x2){csu[a][i], csu[a][i + 1]} * nm + (f32x2){ua[i], ua[i + 1]};
    }
#pragma unroll
    for (int h = 0; h < 4; ++h) { const f32x2 gl = tg[h] * rsl; e[h].x = __builtin_amdgcn_exp2f(gl.x); e[h].y = __builtin_amdgcn_exp2f(gl.y); }
#pragma unroll
    for (int h = 0; h < 4; ++h) { tu[h] = (tg[h] * tu[h]) * rs2; e[h] = e[h] + 1.0f; }
#pragma unroll
    for (int h = 0; h < 4; ++h) { e[h].x = __builtin_amdgcn_rcpf(e[h].x); e[h].y = __builtin_amdgcn_rcpf(e[h].y); }
    u32x4 w;
#pragma unroll
    for (int h = 0; h < 4; ++h) { const f32x2 hv = tu[h] * e[h]; w[h] = pk2(hv.x, hv.y); }
    __builtin_amdgcn_raw_buffer_store_b128(w, rh, hb + m * 1024, 0, 16);
  }
}
DI void g1_math(const Params& p, LAS char* sm, int rb, int ft, int wid, int lane, unsigned tag, unsigned taskflag) {
  const int wr = wid >> 1, wc = wid & 1;
  const int n = lane & 15, kg = lane >> 4, q4 = (lane & 15) >> 2, pp = lane & 3;
  f32x4 a1[2][8], a2[2][8], cs[2];
#pragma unroll
  for (int a = 0; a < 2; ++a) {
    cs[a] = (f32x4){0.f, 0.f, 0.f, 0.f};
#pragma unroll
    for (int m = 0; m < 8; ++m) { a1[a][m] = (f32x4){0.f, 0.f, 0.f, 0.f}; a2[a][m] = (f32x4){0.f, 0.f, 0.f, 0.f}; }
  }
  u32x4 ones_ = {0x3f803f80u, 0x3f803f80u, 0x3f803f80u, 0x3f803f80u};
  PIN(ones_);
  const bf16x8 ones = __builtin_bit_cast(bf16x8, ones_);
  const unsigned tbase = (wr * 128 + n) * 128 + ((kg ^ ((n >> 1) & 7)) * 16);
  const int sw = 2 * (((q4 >> 1) & 1) | ((kg & 1) << 1));
  const unsigned wb0 = 32768 + (8 * kg + q4) * 128 + (((4 * wc + 0 + (pp >> 1)) ^ sw) * 16) + 8 * (pp & 1);
  const unsigned wb1 = 32768 + (8 * kg + q4) * 128 + (((4 * wc + 2 + (pp >> 1)) ^ sw) * 16) + 8 * (pp & 1);
  const unsigned o1 = wr * 8192, o2 = 8192 - o1;
  bf16x8 tP[8], tQ[8], gP[2], gQ[2], uP[2], uQ[2];
#define G1_RD(T, G, U, so, ks) do { LAS char* tp_ = sm + (so) + (tbase ^ ((ks) * 64)); \
    LAS char* w10_ = sm + (so) + wb0 + o1 + (ks) * 4096; LAS char* w11_ = sm + (so) + wb1 + o1 + (ks) * 4096; \
    LAS char* w20_ = sm + (so) + wb0 + o2 + (ks) * 4096; LAS char* w21_ = sm + (so) + wb1 + o2 + (ks) * 4096; \
    G[0] = ldtr(w10_, w10_ + 512); T[0] = *(LAS bf16x8*)(tp_); U[0] = ldtr(w20_, w20_ + 512); G[1] = ldtr(w11_, w11_ + 512); U[1] = ldtr(w21_, w21_ + 512); \
    _Pragma("unroll") for (int m = 1; m < 8; ++m) T[m] = *(LAS bf16x8*)(tp_ + 2048 * m); } while (0)
#define G1_MM(T, G, U) do { _Pragma("unroll") for (int m = 0; m < 8; ++m) { _Pragma("unroll") for (int a = 0; a < 2; ++a) { \
    a1[a][m] = __builtin_amdgcn_mfma_f32_16x16x32_bf16(G[a], T[m], a1[a][m], 0, 0, 0); a2[a][m] = __builtin_amdgcn_mfma_f32_16x16x32_bf16(U[a], T[m], a2[a][m], 0, 0, 0); } } } while (0)
#define G1_CS(G) do { cs[0] = __builtin_amdgcn_mfma_f32_16x16x32_bf16(G[0], ones, cs[0], 0, 0, 0); cs[1] = __builtin_amdgcn_mfma_f32_16x16x32_bf16(G[1], ones, cs[1], 0, 0, 0); } while (0)
#define G1_SCHED() do { _Pragma("unroll") for (int i_ = 0; i_ < 16; ++i_) { __builtin_amdgcn_sched_group_barrier(0x8, 1, 0); __builtin_amdgcn_sched_group_barrier(0x100, 1, 0); } __builtin_amdgcn_sched_group_barrier(0x8, 18, 0); } while (0)
  unsigned so = 0;
  BAR();
  STAMP_AT(1); STAMP_CY(14);
  G1_RD(tP, gP, uP, so, 0);
  G1_RD(tQ, gQ, uQ, so, 1); G1_MM(tP, gP, uP); G1_CS(gP); G1_SCHED();
#pragma unroll 1
  for (int s = 1; s < 10; ++s) {
    so = so == 2 * G1_SLOT ? 0 : so + G1_SLOT;
    BAR();
    if (s == 2 && taskflag) { WAITV(0); flag_set(p.ws, taskflag, tag, lane); }
    G1_RD(tP, gP, uP, so, 0); G1_MM(tQ, gQ, uQ); G1_CS(gQ); G1_SCHED();
    G1_RD(tQ, gQ, uQ, so, 1); G1_MM(tP, gP, uP); G1_CS(gP); G1_SCHED();
  }
  G1_CS(gQ);
  if (n == 0) { *(LAS f32x4*)(sm + L_CS + (wr * 64 + wc * 32 + 4 * kg) * 4) = cs[0]; *(LAS f32x4*)(sm + L_CS + (wr * 64 + wc * 32 + 16 + 4 * kg) * 4) = cs[1]; }
  BAR();
  f32x4 csg[2], csu[2];
#pragma unroll
  for (int a = 0; a < 2; ++a) { csg[a] = *(LAS f32x4*)(sm + L_CS + (wc * 32 + 16 * a + 4 * kg) * 4); csu[a] = *(LAS f32x4*)(sm + L_CS + (64 + wc * 32 + 16 * a + 4 * kg) * 4); }
  f32x2 st[8];
#pragma unroll
  for (int m = 0; m < 8; ++m) st[m] = *(LAS f32x2*)(sm + L_ST + (wr * 128 + 16 * m + n) * 8);
  G1_MM(tQ, gQ, uQ);
  STAMP_AT(2); STAMP_CY(15);
  if (wr == 0) g1_epilogue<false>(p, sm, rb, ft, wr, wc, lane, a1, a2, csg, csu, st);
  else g1_epilogue<true>(p, sm, rb, ft, wr, wc, lane, a1, a2, csg, csu, st);
  STAMP_AT(3);
}

template <int NPRE> constexpr int g2_w_after(int j) { return (j >= 1 && j + 3 <= 13 && j + 3 >= NPRE) ? 5 : 0; }
template <int NPRE> constexpr int g2_ops_after(int j) { return (j == 5 ? 1 : 0) + (j == 7 ? 3 : 0) + ((j >= 1 && j + 2 <= 13) ? 4 : 0) + g2_w_after<NPRE>(j); }
template <int NPRE> constexpr int g2_wait(int j) {
  return j == 0 ? 4 : (j == 1 ? g2_ops_after<NPRE>(1) : g2_w_after<NPRE>(j - 1) + g2_ops_after<NPRE>(j));
}
template <bool ISP>
DI void g2_loader(const Params& p, LAS char* sm, unsigned smb, int mt, int nt, int w, int lane, unsigned tag, unsigned hmflag) {
  const i32x4 srdD = make_srd(p.wd, F_ * D_ * 2), srdH = make_srd(p.ws + WS_HM, 54 * 128 * 1024), srdC = make_srd(p.ws, WS_KVT);
  unsigned vd[5]; g2_wd_offsets(vd, lane);
  const unsigned vh = lane * 16;
  auto fbof = [&](int j) { const int fb = 4 * j + w; return fb < 54 ? fb : 53; };
  auto issueH = [&](int j) {
    const unsigned sb = mt * 4096u + (j < 13 ? w : (w < 2 ? w : 1)) * 131072u, lb = smb + L_HR + w * 4096;
#pragma unroll
    for (int i = 0; i < 4; ++i) dma16i_sc1(srdH, vh, sb, j * 524288 + i * 1024, lb, (j % 3) * 16384 + i * 1024);
  };
  constexpr int XB = ISP ? 1 : 5, NREG = ISP ? 9 : 7, NPRE = XB + NREG;
  u32x4 wreg[NREG][5];
  if (ISP) g2_issue_w(srdD, vd, smb, nt, w, 0);
  else BAR();
#pragma unroll
  for (int j = 0; j < NREG; ++j) {
#pragma unroll
    for (int i = 0; i < 5; ++i) ld16i<NT_WD>(wreg[j][i], srdD, vd[i], nt * 160u + w * 40960u, (j + XB) * 163840);
    if (!ISP && j == 0) {
      WAITV(5);
      flag_set(p.ws, hmflag, tag, lane);
      STAMP_AT(4);
    }
  }
  {
    const unsigned junk = smb + L_END - 256;
#pragma unroll
    for (int r = 0; r < (14 - NPRE + 3) / 4; ++r) {
      const int idx = r * 64 + lane;
      int j = NPRE + (idx >> 4); j = j < 14 ? j : 13;
      touch4(srdD, (unsigned)(fbof(j) * 32 + (mt & 3) * 8 + ((idx & 15) >> 1)) * 1280u + nt * 160u + (idx & 1) * 128u, 0, junk);
    }
  }
  if (ISP && w == 0) {
    poll_all(p.ws, CW_ST + (mt * 16 + (lane & 15)) * 16, tag);
    u32x2 sv; ld8_sc1(sv, srdC, WS_STATS + (mt * 64 + lane) * 8, 0);
    WAITV(0); PIN(sv);
    *(LAS u32x2*)(sm + L_S2 + lane * 8) = sv;
  }
  if (ISP && blockIdx.x == 255 && w == 3) {
    poll_all(p.ws, CW_ARRIVE + lane * 4, tag); poll_all(p.ws, CW_ARRIVE + 256 + lane * 4, tag);
    poll_all(p.ws, CW_ARRIVE + 512 + lane * 4, tag); poll_all(p.ws, CW_ARRIVE + 768 + lane * 4, tag);
    flag_set(p.ws, CW_EPOCH, tag, lane);
  }
  { const int j = lane < 14 ? lane : 0; poll_all(p.ws, CW_HM + (((mt >> 2) * 2 + ((mt >> 1) & 1)) * 54 + fbof(j)) * 16, tag); }
  STAMP_AT(5);
  { const int j = (lane >> 2) < 14 ? (lane >> 2) : 13;
    touch4_sc1(srdH, (unsigned)(fbof(j) * 128 + mt * 4 + (lane & 3)) * 1024u + nt * 128u, 0, smb + L_END - 256); }
  issueH(0); issueH(1); issueH(2);
  WAITV(8);
#pragma unroll
  for (int j = 0; j < NREG; ++j)
#pragma unroll
    for (int i = 0; i < 5; ++i) PIN(wreg[j][i]);
  BAR();
  STAMP_AT(6);
  unsigned zf = 0; u32x4 zr[3];
  const unsigned zoff = CW_Z + (nt * 80 + w * 20 + (lane < 20 ? lane : 0)) * 16;
  auto put_w = [&](int j) {
#pragma unroll
    for (int i = 0; i < 5; ++i) *(LAS u32x4*)(sm + L_WR + (j & 3) * G2_WSLOT + w * 5120 + i * 1024 + lane * 16) = wreg[j - XB][i];
  };
#pragma unroll
  for (int j = 0; j < 14; ++j) {
    if (j == 0) {
#pragma unroll
      for (int jj = XB; jj < 4; ++jj) put_w(jj);
    } else if (j + 3 >= XB && j + 3 < NPRE) put_w(j + 3);
    if (j == 5) ld4_sc1(zf, srdC, zoff);
    if (j == 7) {
      PIN(zf);
      if (!__all(zf == tag)) poll_all(p.ws, zoff, tag);
      const i32x4 srdZ = make_srd(p.ws + WS_ZT, D_ * S_ * 2);
#pragma unroll
      for (int r = 0; r < 3; ++r) {
        int task = r * 64 + lane; task = task < 160 ? task : 159;
        ld16_sc1(zr[r], srdZ, (unsigned)(nt * 80 + w * 20 + (task >> 3)) * 4096u + (mt * 64 + (task & 7) * 8) * 2, 0);
      }
    }
    if (j >= 1 && j + 2 <= 13) issueH(j + 2);
    if (g2_w_after<NPRE>(j)) g2_issue_w(srdD, vd, smb, nt, w, j + 3);
    if (j == 9) {
#pragma unroll
      for (int r = 0; r < 3; ++r) PIN(zr[r]);
#pragma unroll
      for (int r = 0; r < 3; ++r) { const int task = r * 64 + lane; if (task < 160) *(LAS u32x4*)(sm + L_ZS + (w * 20 + (task >> 3)) * ZS_RS + (task & 7) * 16) = zr[r]; }
    }
    if (j < 13) WAITV(g2_wait<NPRE>(j));
    BAR();
  }
}

template <int NY>
DI void g2_math(const Params& p, LAS char* sm, int w, int lane, unsigned tag, int zc, const u32x4 (&wy)[3][5]) {
  f32x4 acc[5][4];
#pragma unroll
  for (int a = 0; a < 5; ++a)
#pragma unroll
    for (int b = 0; b < 4; ++b) acc[a][b] = (f32x4){0.f, 0.f, 0.f, 0.f};
  const int kg = lane >> 4, q4 = (lane & 15) >> 2, pp = lane & 3;
  LAS char* hrd = sm + L_HR + w * 4096 + lane * 16;
  LAS char* wrd = sm + L_WR + w * 5120 + (4 * kg + q4) * 160 + pp * 8;
  bf16x8 hP[4], wP[5], hQ[4], wQ[5];
#define G2_RD(H, W, j) do { LAS char* hp_ = hrd + ((j) % 3) * 16384; LAS char* wp_ = wrd + ((j) & 3) * G2_WSLOT; \
    W[0] = ldtr(wp_, wp_ + 2560); H[0] = *(LAS bf16x8*)(hp_); H[1] = *(LAS bf16x8*)(hp_ + 1024); H[2] = *(LAS bf16x8*)(hp_ + 2048); H[3] = *(LAS bf16x8*)(hp_ + 3072); \
    _Pragma("unroll") for (int a = 1; a < 5; ++a) W[a] = ldtr(wp_ + a * 32, wp_ + a * 32 + 2560); } while (0)
#define G2_MM(H, W) do { _Pragma("unroll") for (int a = 0; a < 5; ++a) _Pragma("unroll") for (int b = 0; b < 4; ++b) \
    acc[a][b] = __builtin_amdgcn_mfma_f32_16x16x32_bf16(W[a], H[b], acc[a][b], 0, 0, 0); } while (0)
#define G2_SCHED() do { _Pragma("unroll") for (int i_ = 0; i_ < 14; ++i_) { __builtin_amdgcn_sched_group_barrier(0x8, 1, 0); __builtin_amdgcn_sched_group_barrier(0x100, 1, 0); } __builtin_amdgcn_sched_group_barrier(0x8, 6, 0); } while (0)
  if (zc >= 0) WAITV(4); else WAITV(0);
  BAR();
  STAMP_AT(9);
  G2_RD(hP, wP, 0);
  auto put_y = [&](int j) {
#pragma unroll
    for (int i = 0; i < 5; ++i) *(LAS u32x4*)(sm + L_WR + (j & 3) * G2_WSLOT + w * 5120 + i * 1024 + lane * 16) = wy[j - 2][i];
  };
  if (NY) { put_y(2); put_y(3); }
  if (zc >= 0) {
    WAITV(0);
    flag_set(p.ws, CW_Z + zc * 16, tag, lane);
  }
  BAR();
  G2_RD(hQ, wQ, 1); G2_MM(hP, wP); G2_SCHED();
  if (NY) put_y(4);
#pragma unroll
  for (int j = 2; j < 14; j += 2) {
    BAR();
    G2_RD(hP, wP, j); G2_MM(hQ, wQ); G2_SCHED();
    BAR();
    G2_RD(hQ, wQ, j + 1); G2_MM(hP, wP); G2_SCHED();
  }
  BAR();
  if (w < 2) G2_MM(hQ, wQ);
  STAMP_AT(10);
#pragma unroll
  for (int a = 0; a < 5; ++a)
#pragma unroll
    for (int b = 0; b < 4; ++b) *(LAS f32x4*)(sm + w * G2_PART + (16 * b + (lane & 15)) * G2_RS + a * 64 + kg * 16) = acc[a][b];
}
DI void g2_final_task(const Params& p, LAS char* sm, int mt, int nt, int gt, int lane, const u32x4& x) {
  const int b = gt / 10, ch = gt - b * 10, tk = b * 16 + (lane & 15);
  f32x4 a0 = (f32x4){0.f, 0.f, 0.f, 0.f}, a1 = a0;
#pragma unroll
  for (int w = 0; w < 4; ++w) { a0 += *(LAS f32x4*)(sm + w * G2_PART + tk * G2_RS + ch * 32); a1 += *(LAS f32x4*)(sm + w * G2_PART + tk * G2_RS + ch * 32 + 16); }
  LAS char* zp = sm + L_ZS + (ch * 8 + ((lane & 15) >> 2)) * ZS_RS + b * 32 + (lane & 3) * 8;
  const u32x2 z0 = __builtin_bit_cast(u32x2, __builtin_amdgcn_ds_read_tr16_b64_v4i16((LAS s16x4*)zp));
  const u32x2 z1 = __builtin_bit_cast(u32x2, __builtin_amdgcn_ds_read_tr16_b64_v4i16((LAS s16x4*)(zp + 4 * ZS_RS)));
  const f32x2 st = *(LAS f32x2*)(sm + L_S2 + tk * 8);
  const float rs = st.y, nmr = -st.x * rs;
  float o[8];
#pragma unroll
  for (int e = 0; e < 8; ++e) {
    const float xv = (e & 1) ? bhi(x[e >> 1]) : blo(x[e >> 1]);
    const unsigned zw = e < 4 ? z0[(e & 3) >> 1] : z1[(e & 3) >> 1];
    const float z = (e & 1) ? bhi(zw) : blo(zw);
    const float xn = fmaf(xv, rs, nmr);
    o[e] = fmaf(xn, z, xv) + (e < 4 ? a0[e] : a1[e - 4]);
  }
  const u32x4 r = {pk2(o[0], o[1]), pk2(o[2], o[3]), pk2(o[4], o[5]), pk2(o[6], o[7])};
  *(GAS u32x4*)(p.out + (mt * 64 + tk) * 640 + nt * 80 + ch * 8) = r;
}
DI u32x4 final_x(const Params& p, int mt, int nt, int gt, int lane) {
  const int b = gt / 10, ch = gt - b * 10;
  return *(const GAS u32x4*)(p.q + (mt * 64 + b * 16 + (lane & 15)) * 640 + nt * 80 + ch * 8);
}

DI void pair_send(LAS char* pb, LAS unsigned* pf, unsigned tag, int lane, const f32x4 (&acc)[8]) {
#pragma unroll
  for (int g = 0; g < 8; ++g) *(LAS f32x4*)(pb + g * 1024 + lane * 16) = acc[g];
  asm volatile("s_waitcnt lgkmcnt(0)" ::: "memory");
  if (lane == 0) *(volatile LAS unsigned*)pf = tag;
}
DI void pair_recv(LAS char* pb, LAS unsigned* pf, unsigned tag, int lane, f32x4 (&acc)[8]) {
#pragma unroll 1
  for (unsigned it = 0; it < (1u << 22); ++it) { if (*(volatile LAS unsigned*)pf == tag) break; __builtin_amdgcn_s_sleep(1); }
  asm volatile("" ::: "memory");
#pragma unroll
  for (int g = 0; g < 8; ++g) acc[g] += *(LAS f32x4*)(pb + g * 1024 + lane * 16);
}

__global__ __launch_bounds__(512, 2) void fused_kernel(Params p_arg) {
  unsigned long long pc0; asm volatile("s_getpc_b64 %0" : "=s"(pc0));
  const KP kp = (KP)__builtin_amdgcn_kernarg_segment_ptr();
  Params p = reload(kp);
  extern __shared__ __attribute__((aligned(16))) char smem_raw[];
  LAS char* sm = (LAS char*)smem_raw;
  const unsigned smb = (unsigned)(size_t)sm;
  const int lane = threadIdx.x & 63, wid = __builtin_amdgcn_readfirstlane(threadIdx.x >> 6);
  const int b = blockIdx.x, x = b & 7, i = b >> 3;
  const int mt = x * 4 + (i & 3), nt = i >> 2;
  STAMP_AT(0);
  if (i < 27) {
    const int mm = 27 * (x & 3) + i, ft = mm >> 2, rb = 4 * (x >> 2) + (mm & 3);
    if (wid < 4) {
      const unsigned ep = __hip_atomic_load((gu32*)(p.ws + CW_EPOCH), __ATOMIC_RELAXED, __HIP_MEMORY_SCOPE_AGENT);
      unsigned taskflag = 0;
      const int id = wid < 2 ? 2 * b + wid : 432 + b;
      if (wid < 3 && id < 512) { task_stats<true>(p, id, lane); taskflag = CW_ST + id * 16; }
      else BAR();
      const unsigned tag = __builtin_amdgcn_readfirstlane(ep) + 1u;
      asm volatile("" :: "s"(tag) : "memory");
      STAMP_AT(13);
      g1_math(p, sm, rb, ft, wid, lane, tag, taskflag);
      p = reload(kp);
      g2_loader<false>(p, sm, smb, mt, nt, wid, lane, tag, CW_HM + ((rb * 2 + (wid >> 1)) * 54 + ft * 2 + (wid & 1)) * 16);
    } else {
      const int hw = wid - 4;
      const bool conv = 2 * b < NCH_G;
      const int cch = 2 * b + (hw >> 1);
      unsigned tag, dvb = 0; int zc = -1;
      if (conv) {
        u32x4 wy[3][5];
        g1_loader<true>(kp, p, pc0, sm, smb, rb, ft, mt, nt, hw, lane, tag, cch, dvb, wy);
        p = reload(kp);
        BAR();
        STAMP_AT(7);
        LAS char* cb = sm + L_CVG + (hw >> 1) * CV_SZ;
        LAS char* pb = sm + L_CP + (hw >> 1) * 8192;
        LAS unsigned* pf = (LAS unsigned*)(sm + L_PF + (hw >> 1) * 4);
        f32x4 acc[8];
        conv_mfma<4>(cb, (hw & 1) * 4, lane, acc);
        if (hw & 1) pair_send(pb, pf, tag, lane, acc);
        else { pair_recv(pb, pf, tag, lane, acc); conv_store(p, cb, cch, lane, dvb, acc); zc = cch; }
        STAMP_AT(8);
        g2_math<3>(p, sm, hw, lane, tag, zc, wy);
      } else {
        u32x4 wy[3][5];
        g1_loader<false>(kp, p, pc0, sm, smb, rb, ft, mt, nt, hw, lane, tag, cch, dvb, wy);
        p = reload(kp);
        BAR();
        g2_math<3>(p, sm, hw, lane, tag, zc, wy);
      }
    }
  } else {
    const int cg = i - 27, pb = x * 5 + cg;
    const unsigned ep = __hip_atomic_load((gu32*)(p.ws + CW_EPOCH), __ATOMIC_RELAXED, __HIP_MEMORY_SCOPE_AGENT);
    const int hw = wid - 4, c0 = NCH_G + pb * NCH_P;
    if (wid < 4) {
      task_kv<0>(p, 2 * cg + (wid & 1), 4 * x + (wid >> 1), lane, [] {});
      const unsigned tag = __builtin_amdgcn_readfirstlane(ep) + 1u;
      asm volatile("" :: "s"(tag) : "memory");
      WAITV(0);
      flag_set(p.ws, CW_KV + ((2 * cg + (wid & 1)) * 32 + 4 * x + (wid >> 1)) * 16, tag, lane);
      BAR();
      if (wid == 0) flag_set(p.ws, CW_ARRIVE + b * 4, tag, lane);
      STAMP_AT(1);
      BAR();
      p = reload(kp);
      touchg(p.wd, (unsigned)((pb * 4 + wid) * 64 + lane) * 128u, smb + L_END - 256);
      if (pb * 4 + wid + 160 < 270) touchg(p.wd, (unsigned)((pb * 4 + wid + 160) * 64 + lane) * 128u, smb + L_END - 256);
      g2_loader<true>(p, sm, smb, mt, nt, wid, lane, tag, 0);
    } else {
      LAS char* cb = sm + L_CVP + hw * CV_SZ;
      const int cA = c0 + hw, cB = c0 + 4 + (hw >> 1);
      ConvIn in, in2;
      task_kv<13>(p, 2 * cg + (wid & 1), 4 * x + (wid >> 1), lane, [&] { conv_load_h(p, in, cA, lane); });
      conv_load_h(p, in2, cB, lane);
      const unsigned tag = __builtin_amdgcn_readfirstlane(ep) + 1u;
      asm volatile("" :: "s"(tag) : "memory");
      if ((hw & 1) == 0 && lane == 0) *(LAS unsigned*)(sm + L_PFP + (hw >> 1) * 4) = ~tag;
      WAITV(13);
      flag_set(p.ws, CW_KV + ((2 * cg + (wid & 1)) * 32 + 4 * x + (wid >> 1)) * 16, tag, lane);
      BAR();
      STAMP_AT(1);
      poll_all(p.ws, CW_KV + (((lane < 32 ? cA : cB) >> 6) * 32 + (lane & 31)) * 16, tag);
      conv_load_u(p, in, cA, lane);
      conv_load_u(p, in2, cB, lane);
      BAR();
      WAITV(4);
      conv_pin(in);
      conv_build(in, cb, lane);
      unsigned dvb = in.dv;
      STAMP_AT(2);
      f32x4 acc[8];
      conv_mfma<8>(cb, 0, lane, acc);
      conv_store(p, cb, cA, lane, dvb, acc);
      STAMP_AT(3);
      WAITV(4);
      conv_pin(in2);
      conv_build(in2, cb, lane);
      dvb = in2.dv;
      STAMP_AT(7);
      conv_mfma<4>(cb, (hw & 1) * 4, lane, acc);
      WAITV(0);
      flag_set(p.ws, CW_Z + cA * 16, tag, lane);
      LAS unsigned* pf = (LAS unsigned*)(sm + L_PFP + (hw >> 1) * 4);
      int zc = -1;
      if (hw & 1) pair_send(cb, pf, tag, lane, acc);
      else { pair_recv(cb + CV_SZ, pf, tag, lane, acc); conv_store(p, cb, cB, lane, dvb, acc); zc = cB; }
      STAMP_AT(8);
      u32x4 wy[3][5];
      g2_math<0>(p, sm, hw, lane, tag, zc, wy);
    }
  }
  p = reload(kp);
  const int gt0 = wid * 4 + (lane >> 4), gt1 = 32 + gt0;
  const u32x4 x0 = final_x(p, mt, nt, gt0, lane);
  u32x4 x1 = x0;
  if (wid < 2) x1 = final_x(p, mt, nt, gt1, lane);
  BAR();
  g2_final_task(p, sm, mt, nt, gt0, lane, x0);
  if (wid < 2) g2_final_task(p, sm, mt, nt, gt1, lane, x1);
  STAMP_AT(11);
}

extern "C" void kernel_launch(void* const* d_in, const int* in_sizes, int n_in, void* d_out, int out_size, void* d_ws, size_t ws_size, hipStream_t stream) {
  Params p;
  p.q = (const u16*)d_in[0]; p.k = (const u16*)d_in[1]; p.v = (const u16*)d_in[2]; p.sk = (const u16*)d_in[3]; p.h = (const u16*)d_in[4]; p.dv = (const u16*)d_in[5];
  p.wg = (const u16*)d_in[7]; p.wu = (const u16*)d_in[8]; p.wd = (const u16*)d_in[9]; p.out = (u16*)d_out; p.ws = (char*)d_ws;
  (void)hipFuncSetAttribute((const void*)fused_kernel, hipFuncAttributeMaxDynamicSharedMemorySize, L_END);
  fused_kernel<<<256, 512, L_END, stream>>>(p);
}
#define HAVE_INTER 1
#define HM_K16 1
```

```cpp
#include <hip/hip_runtime.h>
#include <stdint.h>
typedef unsigned short u16;
typedef __bf16 bf16x8 __attribute__((ext_vector_type(8)));
typedef __bf16 bf16x2 __attribute__((ext_vector_type(2)));
typedef short s16x4 __attribute__((ext_vector_type(4)));
typedef float f32x2 __attribute__((ext_vector_type(2)));
typedef float f32x4 __attribute__((ext_vector_type(4)));
typedef unsigned u32x2 __attribute__((ext_vector_type(2)));
typedef unsigned u32x4 __attribute__((ext_vector_type(4)));
typedef int i32x4 __attribute__((ext_vector_type(4)));
#define LAS __attribute__((address_space(3)))
#define GAS __attribute__((address_space(1)))
#define DI __device__ __forceinline__
typedef GAS unsigned gu32;

#define TOUCH 1
#define TOUCH_CODE 0
#define CODE_BACK 0
#define NT_G1 0
#define NT_KV 1
#define NT_H 1
#define NT_WD 0
#define TLBW 0
constexpr int S_ = 2048, D_ = 640, F_ = 1728;
constexpr unsigned CW_EPOCH = 0;
constexpr unsigned CW_ARRIVE = 1024;
constexpr unsigned CW_KV = 4096;
constexpr unsigned CW_ST = 16384;
constexpr unsigned CW_HM = 24576;
constexpr unsigned CW_Z = 40960;
constexpr unsigned WS_STATS = 65536;
constexpr unsigned WS_KVT = 131072;
constexpr unsigned WS_ZT = WS_KVT + D_ * S_ * 2;
constexpr unsigned WS_HM = WS_ZT + D_ * S_ * 2;
constexpr unsigned WS_END = WS_HM + 54 * 128 * 1024;
constexpr unsigned WS_DBG = 32u << 20;
constexpr int G1_SLOT = 49152;
constexpr int L_CS = 153856, L_ST = L_CS + 512, L_S2 = L_ST + 2048, L_END = L_S2 + 512 + 64 + 256;
constexpr int L_HR = 0, L_WR = 49152, G2_WSLOT = 20480;
constexpr int L_CVG = 90112, L_CVP = 69632;
constexpr int L_ZS = 131072, ZS_RS = 144;
constexpr int L_CP = L_CVG + 2 * 21216, L_PF = L_CS - 64;
constexpr int L_PFP = L_S2 + 512 - 0;
constexpr int G2_RS = 336, G2_PART = 64 * G2_RS;
constexpr int CV_H = 4160, CV_U = 4 * CV_H, CV_SZ = CV_U + 4576;
static_assert(L_ZS + 80 * ZS_RS <= L_PF, "lds");
static_assert(L_CP + 2 * 8192 <= L_PF, "lds");
static_assert(L_CVP + 4 * CV_SZ + 64 <= L_S2, "lds");
constexpr int NCH_P = 6;
constexpr int NCH_G = D_ - 40 * NCH_P;
static_assert(NCH_G <= 2 * 216 && NCH_G % 2 == 0, "conv split");
constexpr unsigned SPIN_MAX = 1u << 16;

struct Params {
  const u16 *q, *k, *v, *sk, *h, *dv, *wg, *wu, *wd;
  u16* out; char* ws;
};

typedef const __attribute__((address_space(4))) Params* KP;
DI Params reload(KP kp) {
  asm volatile("" : "+s"(kp));
  Params r; r.q = kp->q; r.k = kp->k; r.v = kp->v; r.sk = kp->sk; r.h = kp->h; r.dv = kp->dv; r.wg = kp->wg; r.wu = kp->wu; r.wd = kp->wd; r.out = kp->out; r.ws = kp->ws;
  return r;
}
DI i32x4 make_srd(const void* p, unsigned bytes) {
  i32x4 r; unsigned long long a = (unsigned long long)p;
  r.x = (int)(unsigned)a;
  r.y = (int)((unsigned)(a >> 32) & 0xffffu);
  r.z = (int)bytes; r.w = 0x00020000; return r;
}
DI void dma16(const i32x4& srd, unsigned voff, unsigned soff, unsigned lds) {
  asm volatile("s_mov_b32 m0, %3\n\ts_nop 0\n\tbuffer_load_dwordx4 %0, %1, %2 offen lds" :: "v"(voff), "s"(srd), "s"(soff), "s"(lds) : "memory");
}
DI void dma16_sc1(const i32x4& srd, unsigned voff, unsigned soff, unsigned lds) {
  asm volatile("s_mov_b32 m0, %3\n\ts_nop 0\n\tbuffer_load_dwordx4 %0, %1, %2 offen sc1 lds" :: "v"(voff), "s"(srd), "s"(soff), "s"(lds) : "memory");
}
template <int NT = 0>
DI void dma16i(const i32x4& srd, unsigned voff, unsigned sbase, int simm, unsigned lbase, int limm) {
  unsigned t;
  if (NT) asm volatile("s_add_u32 m0, %4, %6\n\ts_add_u32 %0, %3, %5\n\tbuffer_load_dwordx4 %1, %2, %0 offen nt lds" : "=&s"(t) : "v"(voff), "s"(srd), "s"(sbase), "s"(lbase), "i"(simm), "i"(limm) : "memory", "scc");
  else asm volatile("s_add_u32 m0, %4, %6\n\ts_add_u32 %0, %3, %5\n\tbuffer_load_dwordx4 %1, %2, %0 offen lds" : "=&s"(t) : "v"(voff), "s"(srd), "s"(sbase), "s"(lbase), "i"(simm), "i"(limm) : "memory", "scc");
}
DI void dma16i_sc1(const i32x4& srd, unsigned voff, unsigned sbase, int simm, unsigned lbase, int limm) {
  unsigned t;
  asm volatile("s_add_u32 m0, %4, %6\n\ts_add_u32 %0, %3, %5\n\tbuffer_load_dwordx4 %1, %2, %0 offen sc1 lds" : "=&s"(t) : "v"(voff), "s"(srd), "s"(sbase), "s"(lbase), "i"(simm), "i"(limm) : "memory", "scc");
}
template <int NT = 0>
DI void ld16i(u32x4& d, const i32x4& srd, unsigned voff, unsigned sbase, int simm) {
  unsigned t;
  if (NT) asm volatile("s_add_u32 %1, %4, %5\n\tbuffer_load_dwordx4 %0, %2, %3, %1 offen nt" : "=v"(d), "=&s"(t) : "v"(voff), "s"(srd), "s"(sbase), "i"(simm) : "memory", "scc");
  else asm volatile("s_add_u32 %1, %4, %5\n\tbuffer_load_dwordx4 %0, %2, %3, %1 offen" : "=v"(d), "=&s"(t) : "v"(voff), "s"(srd), "s"(sbase), "i"(simm) : "memory", "scc");
}
template <int NT = 0>
DI void ld16(u32x4& d, const i32x4& srd, unsigned voff, unsigned soff) {
  if (NT) asm volatile("buffer_load_dwordx4 %0, %1, %2, %3 offen nt" : "=v"(d) : "v"(voff), "s"(srd), "s"(soff) : "memory");
  else asm volatile("buffer_load_dwordx4 %0, %1, %2, %3 offen" : "=v"(d) : "v"(voff), "s"(srd), "s"(soff) : "memory");
}
DI void ld16_sc1(u32x4& d, const i32x4& srd, unsigned voff, unsigned soff) { asm volatile("buffer_load_dwordx4 %0, %1, %2, %3 offen sc1" : "=v"(d) : "v"(voff), "s"(srd), "s"(soff) : "memory"); }
DI void ld8_sc1(u32x2& d, const i32x4& srd, unsigned voff, unsigned soff) { asm volatile("buffer_load_dwordx2 %0, %1, %2, %3 offen sc1" : "=v"(d) : "v"(voff), "s"(srd), "s"(soff) : "memory"); }
DI void ld8(u32x2& d, const i32x4& srd, unsigned voff, unsigned soff) { asm volatile("buffer_load_dwordx2 %0, %1, %2, %3 offen" : "=v"(d) : "v"(voff), "s"(srd), "s"(soff) : "memory"); }
DI void ld4_sc1(unsigned& d, const i32x4& srd, unsigned voff) { asm volatile("buffer_load_dword %0, %1, %2, 0 offen sc1" : "=v"(d) : "v"(voff), "s"(srd) : "memory"); }
DI void touchg(const void* base, unsigned voff, unsigned lds) {
  asm volatile("s_mov_b32 m0, %2\n\ts_nop 0\n\tglobal_load_lds_dword %0, %1" :: "v"(voff), "s"(base), "s"(lds) : "memory");
}
DI void touchg_nt(const void* base, unsigned voff, unsigned lds) {
  asm volatile("s_mov_b32 m0, %2\n\ts_nop 0\n\tglobal_load_lds_dword %0, %1 nt" :: "v"(voff), "s"(base), "s"(lds) : "memory");
}
DI void touch4(const i32x4& srd, unsigned voff, unsigned soff, unsigned lds) {
  asm volatile("s_mov_b32 m0, %3\n\ts_nop 0\n\tbuffer_load_dword %0, %1, %2 offen lds" :: "v"(voff), "s"(srd), "s"(soff), "s"(lds) : "memory");
}
DI void touch4_sc1(const i32x4& srd, unsigned voff, unsigned soff, unsigned lds) {
  asm volatile("s_mov_b32 m0, %3\n\ts_nop 0\n\tbuffer_load_dword %0, %1, %2 offen sc1 lds" :: "v"(voff), "s"(srd), "s"(soff), "s"(lds) : "memory");
}
#define WAITV(n) asm volatile("s_waitcnt vmcnt(%0)" :: "i"(n) : "memory")
#define BAR() asm volatile("s_waitcnt lgkmcnt(0)\n\ts_barrier" ::: "memory")
#define PIN(x) asm volatile("" : "+v"(x))
DI float b2f(u16 x) { return __uint_as_float(((unsigned)x) << 16); }
DI float blo(unsigned x) { return __uint_as_float(x << 16); }
DI float bhi(unsigned x) { return __uint_as_float(x & 0xffff0000u); }
DI unsigned pk2(float lo, float hi) { bf16x2 r = {(__bf16)lo, (__bf16)hi}; return __builtin_bit_cast(unsigned, r); }
DI float rbf(float x) { return (float)(__bf16)x; }
DI float dot2(unsigned a, unsigned b, float c) { return __builtin_amdgcn_fdot2_f32_bf16(__builtin_bit_cast(bf16x2, a), __builtin_bit_cast(bf16x2, b), c, false); }
template <int CTRL> DI float dpp_add(float x) { return x + __int_as_float(__builtin_amdgcn_update_dpp(0, __float_as_int(x), CTRL, 0xf, 0xf, true)); }
DI float row16_sum(float x) { x = dpp_add<0x128>(x); x = dpp_add<0x124>(x); x = dpp_add<0x122>(x); x = dpp_add<0x121>(x); return x; }
DI bf16x8 ldtr(LAS char* p0, LAS char* p1) {
  s16x4 a = __builtin_amdgcn_ds_read_tr16_b64_v4i16((LAS s16x4*)p0), b = __builtin_amdgcn_ds_read_tr16_b64_v4i16((LAS s16x4*)p1);
  return __builtin_bit_cast(bf16x8, __builtin_shufflevector(a, b, 0, 1, 2, 3, 4, 5, 6, 7));
}
DI void poll_all(const char* ws, unsigned off, unsigned tag) {
  gu32* a = (gu32*)(ws + off);
#pragma unroll 1
  for (unsigned it = 0; it < SPIN_MAX; ++it) {
    const unsigned v = __hip_atomic_load(a, __ATOMIC_RELAXED, __HIP_MEMORY_SCOPE_AGENT);
    if (__all(v == tag)) break;
    __builtin_amdgcn_s_sleep(2);
  }
  asm volatile("" ::: "memory");
}
DI void flag_set(const char* ws, unsigned off, unsigned tag, int lane) {
  if (lane == 0) __hip_atomic_store((gu32*)(ws + off), tag, __ATOMIC_RELAXED, __HIP_MEMORY_SCOPE_AGENT);
}
#define STAMP_CY(idx) do {} while (0)
#define STAMP_AT(idx) do {} while (0)

template <int NX, class Hook>
DI void task_kv(const Params& p, int cg, int tb, int lane, Hook hook) {
  const int c0 = cg * 64 + 8 * (lane & 7), t0 = tb * 64 + 8 * (lane >> 3);
  const i32x4 rk = make_srd(p.k, S_ * D_ * 2), rv = make_srd(p.v, S_ * D_ * 2), rsk = make_srd(p.sk, D_ * 8);
  u32x4 kk[11], vv[8], ss[4];
#pragma unroll
  for (int j = 0; j < 4; ++j) ld16(ss[j], rsk, (unsigned)(c0 * 8), j * 16);
#pragma unroll
  for (int j = 0; j < 11; ++j) { const int t = t0 - 3 + j; ld16<NT_KV>(kk[j], rk, t >= 0 ? (unsigned)(t * 1280 + c0 * 2) : 0xfffffff0u, 0); }
#pragma unroll
  for (int j = 0; j < 8; ++j) ld16<NT_KV>(vv[j], rv, (unsigned)(t0 * 1280 + c0 * 2), j * 1280);
  hook();
  const __amdgpu_buffer_rsrc_t ro = __builtin_amdgcn_make_buffer_rsrc((void*)(p.ws + WS_KVT), 0, D_ * S_ * 2, 0x00020000);
  WAITV(NX);
  STAMP_AT(12);
#pragma unroll
  for (int j = 0; j < 4; ++j) PIN(ss[j]);
#pragma unroll
  for (int j = 0; j < 11; ++j) PIN(kk[j]);
#pragma unroll
  for (int j = 0; j < 8; ++j) PIN(vv[j]);
#pragma unroll
  for (int cp = 0; cp < 4; ++cp) {
    const f32x2 s0 = {blo(ss[cp].x), blo(ss[cp].z)}, s1 = {bhi(ss[cp].x), bhi(ss[cp].z)}, s2 = {blo(ss[cp].y), blo(ss[cp].w)}, s3 = {bhi(ss[cp].y), bhi(ss[cp].w)};
    f32x2 kx[11];
#pragma unroll
    for (int j = 0; j < 11; ++j) { const unsigned w = kk[j][cp]; kx[j] = (f32x2){blo(w), bhi(w)}; }
    f32x2 o[8];
    __builtin_amdgcn_sched_barrier(0);
#pragma unroll
    for (int j = 0; j < 8; ++j) o[j] = kx[j] * s3;
    __builtin_amdgcn_sched_barrier(0);
#pragma unroll
    for (int j = 0; j < 8; ++j) o[j] = kx[j + 1] * s2 + o[j];
    __builtin_amdgcn_sched_barrier(0);
#pragma unroll
    for (int j = 0; j < 8; ++j) o[j] = kx[j + 2] * s1 + o[j];
    __builtin_amdgcn_sched_barrier(0);
#pragma unroll
    for (int j = 0; j < 8; ++j) o[j] = kx[j + 3] * s0 + o[j];
    __builtin_amdgcn_sched_barrier(0);
    f32x2 vx[8];
#pragma unroll
    for (int j = 0; j < 8; ++j) { const unsigned w = vv[j][cp]; vx[j] = (f32x2){blo(w), bhi(w)}; }
    __builtin_amdgcn_sched_barrier(0);
#pragma unroll
    for (int j = 0; j < 8; ++j) o[j] = o[j] * vx[j];
    __builtin_amdgcn_sched_barrier(0);
    const u32x4 r0 = {pk2(o[7].x, o[6].x), pk2(o[5].x, o[4].x), pk2(o[3].x, o[2].x), pk2(o[1].x, o[0].x)};
    const u32x4 r1 = {pk2(o[7].y, o[6].y), pk2(o[5].y, o[4].y), pk2(o[3].y, o[2].y), pk2(o[1].y, o[0].y)};
    __builtin_amdgcn_raw_buffer_store_b128(r0, ro, (unsigned)((c0 + 2 * cp) * 4096 + (2040 - t0) * 2), 0, 16);
    __builtin_amdgcn_raw_buffer_store_b128(r1, ro, (unsigned)((c0 + 2 * cp + 1) * 4096 + (2040 - t0) * 2), 0, 16);
  }
}
template <bool PRE>
DI void task_stats(const Params& p, int id, int lane) {
  const i32x4 rq = make_srd(p.q, S_ * D_ * 2);
  const int row = id * 4 + (lane >> 4), l16 = lane & 15;
  u32x4 x[5];
#pragma unroll
  for (int j = 0; j < 5; ++j) ld16<NT_G1>(x[j], rq, (unsigned)row * 1280u + l16 * 16, j * 256);
  const __amdgpu_buffer_rsrc_t rs = __builtin_amdgcn_make_buffer_rsrc((void*)(p.ws + WS_STATS), 0, S_ * 8, 0x00020000);
  if (PRE) BAR();
  WAITV(0);
  STAMP_AT(12);
#pragma unroll
  for (int j = 0; j < 5; ++j) PIN(x[j]);
  float s4[4] = {0.f, 0.f, 0.f, 0.f};
#pragma unroll
  for (int j = 0; j < 5; ++j)
#pragma unroll
    for (int e = 0; e < 4; ++e) s4[e] = dot2(x[j][e], 0x3f803f80u, s4[e]);
  const float mean = row16_sum((s4[0] + s4[1]) + (s4[2] + s4[3])) * (1.0f / 640.0f);
  f32x2 q2[2] = {{0.f, 0.f}, {0.f, 0.f}};
#pragma unroll
  for (int j = 0; j < 5; ++j)
#pragma unroll
    for (int e = 0; e < 4; ++e) { const unsigned w = x[j][e]; const f32x2 d = (f32x2){blo(w), bhi(w)} - mean; q2[e & 1] = d * d + q2[e & 1]; }
  const float var = row16_sum((q2[0].x + q2[0].y) + (q2[1].x + q2[1].y)) * (1.0f / 640.0f);
  const float mb = rbf(mean), sb = rbf(__builtin_amdgcn_sqrtf(rbf(rbf(var) + 1e-5f)));
  u32x2 o; o.x = __float_as_uint(mb); o.y = __float_as_uint(1.0f / sb);
  __builtin_amdgcn_raw_buffer_store_b64(o, rs, l16 == 0 ? (unsigned)row * 8u : 0xfffffff0u, 0, 16);
}

struct ConvIn { u32x4 L0[4], L1[4], L2[4], U[4]; unsigned dv; };
DI void conv_load_h(const Params& p, ConvIn& in, int c, int lane) {
  const i32x4 rh = make_srd(p.h, D_ * S_ * 2), rd = make_srd(p.dv, D_ * 2);
  const unsigned vo = lane * 16, so = c * 4096u;
  asm volatile("buffer_load_ushort %0, %1, %2, 0 offen" : "=v"(in.dv) : "v"(c * 2u), "s"(rd) : "memory");
#pragma unroll
  for (int i = 0; i < 4; ++i) { ld16<NT_H>(in.L0[i], rh, vo, so + i * 1024); ld16(in.L1[i], rh, vo + 4, so + i * 1024); ld16(in.L2[i], rh, vo + 8, so + i * 1024); }
}
DI void conv_load_u(const Params& p, ConvIn& in, int c, int lane) {
  const i32x4 ru = make_srd(p.ws + WS_KVT, D_ * S_ * 2);
#pragma unroll
  for (int i = 0; i < 4; ++i) ld16_sc1(in.U[i], ru, lane * 16, c * 4096u + i * 1024);
}
DI void conv_pin(ConvIn& in) {
#pragma unroll
  for (int i = 0; i < 4; ++i) { PIN(in.L0[i]); PIN(in.L1[i]); PIN(in.L2[i]); PIN(in.U[i]); }
  PIN(in.dv);
}
DI void conv_build(const ConvIn& in, LAS char* cb, int lane) {
#pragma unroll
  for (int i = 0; i < 4; ++i) {
    const unsigned o = (i * 64 + lane + 2) * 16;
    u32x4 c0, c2;
#pragma unroll
    for (int e = 0; e < 4; ++e) { c0[e] = __builtin_amdgcn_alignbit(in.L1[i][e], in.L0[i][e], 16); c2[e] = __builtin_amdgcn_alignbit(in.L2[i][e], in.L1[i][e], 16); }
    *(LAS u32x4*)(cb + 0 * CV_H + o) = c0;
    *(LAS u32x4*)(cb + 1 * CV_H + o) = in.L1[i];
    *(LAS u32x4*)(cb + 2 * CV_H + o) = c2;
    *(LAS u32x4*)(cb + 3 * CV_H + o) = in.L2[i];
    *(LAS u32x4*)(cb + CV_U + (i * 64 + lane) * 16) = in.U[i];
  }
  const u32x4 z4 = {0u, 0u, 0u, 0u};
  if (lane < 30) *(LAS u32x4*)(cb + CV_U + 4096 + lane * 16) = z4;
  if (lane == 0) {
    const unsigned w0 = in.L0[0].x, w1 = in.L0[0].y;
#pragma unroll
    for (int cc = 0; cc < 4; ++cc) *(LAS u32x4*)(cb + cc * CV_H) = z4;
    *(LAS u32x4*)(cb + 0 * CV_H + 16) = (u32x4){0u, 0u, 0u, w0 << 16};
    *(LAS u32x4*)(cb + 1 * CV_H + 16) = (u32x4){0u, 0u, 0u, w0};
    *(LAS u32x4*)(cb + 2 * CV_H + 16) = (u32x4){0u, 0u, w0 << 16, __builtin_amdgcn_alignbit(w1, w0, 16)};
    *(LAS u32x4*)(cb + 3 * CV_H + 16) = (u32x4){0u, 0u, w0, w1};
  }
}
template <int TRIPS>
DI void conv_mfma(LAS char* cb, int d0, int lane, f32x4 (&acc)[8]) {
  const int r = lane & 15, kg = lane >> 4, kap = kg >> 1, bp = kg & 1;
  unsigned a0 = (unsigned)(size_t)cb + (r & 3) * CV_H + 32 * kap + 16 * bp + 2 * (r & 12) + d0 * 64;
  unsigned a1 = a0 + 8;
  unsigned ub = (unsigned)(size_t)cb + CV_U + (4064 - 3584) - 32 * r + 32 * kap + 16 * bp + d0 * 64;
  PIN(a0); PIN(a1); PIN(ub);
#pragma unroll
  for (int g = 0; g < 8; ++g) acc[g] = (f32x4){0.f, 0.f, 0.f, 0.f};
  bf16x8 A[8], B[8];
#define CV_LDA(g) do { const s16x4 x0_ = *(LAS s16x4*)(size_t)(a0 + (g) * 512), x1_ = *(LAS s16x4*)(size_t)(a1 + (g) * 512); \
    A[g] = __builtin_bit_cast(bf16x8, __builtin_shufflevector(x0_, x1_, 0, 1, 2, 3, 4, 5, 6, 7)); } while (0)
#define CV_LDB(g) do { B[g] = *(LAS bf16x8*)(size_t)(ub + (7 - (g)) * 512); } while (0)
#pragma unroll
  for (int g = 0; g < 8; ++g) { CV_LDA(g); CV_LDB(7 - g); }
#pragma unroll 1
  for (int d = 0; d < TRIPS; ++d) {
    a0 += 64; a1 += 64; ub += 64; PIN(a0); PIN(a1); PIN(ub);
#pragma unroll
    for (int gm = 0; gm < 8; ++gm) {
#pragma unroll
      for (int gp = 7 - gm; gp >= 0; --gp) acc[gm + gp] = __builtin_amdgcn_mfma_f32_16x16x32_bf16(A[gm], B[gp], acc[gm + gp], 0, 0, 0);
      __builtin_amdgcn_sched_barrier(0);
      CV_LDA(gm); CV_LDB(7 - gm);
      __builtin_amdgcn_sched_barrier(0);
    }
  }
}
DI void conv_store(const Params& p, LAS char* cb, int c, int lane, unsigned dvb, const f32x4 (&acc)[8]) {
  const int r = lane & 15, kg = lane >> 4;
  const float dv = __uint_as_float(dvb << 16);
  const __amdgpu_buffer_rsrc_t rz = __builtin_amdgcn_make_buffer_rsrc((void*)(p.ws + WS_ZT), 0, D_ * S_ * 2, 0x00020000);
  u32x2 z[8];
#pragma unroll
  for (int g = 0; g < 8; ++g) {
    const int t = 256 * g + 16 * r + 4 * kg;
    const u32x2 kv = *(LAS u32x2*)(cb + CV_U + (2044 - t) * 2);
    const float y0 = fmaf(bhi(kv.y), dv, acc[g][0]), y1 = fmaf(blo(kv.y), dv, acc[g][1]), y2 = fmaf(bhi(kv.x), dv, acc[g][2]), y3 = fmaf(blo(kv.x), dv, acc[g][3]);
    z[g].x = pk2(y0, y1); z[g].y = pk2(y2, y3);
  }
#pragma unroll
  for (int g = 0; g < 8; g += 2) {
    const auto s0 = __builtin_amdgcn_permlane16_swap(z[g].x, z[g + 1].x, false, false);
    const auto s1 = __builtin_amdgcn_permlane16_swap(z[g].y, z[g + 1].y, false, false);
    const u32x4 o = {s0[0], s1[0], s0[1], s1[1]};
    const int gg = g + (kg & 1);
    __builtin_amdgcn_raw_buffer_store_b128(o, rz, c * 4096u + (256 * gg + 16 * r + 4 * (kg & 2)) * 2, 0, 16);
  }
}
struct ConvHalf { u32x4 La[4], Lb[4], U[2]; unsigned dv; };
DI void convh_load_h(const Params& p, ConvHalf& in, int c, int half, int lane) {
  const i32x4 rh = make_srd(p.h, D_ * S_ * 2), rd = make_srd(p.dv, D_ * 2);
  const unsigned vo = lane * 16 + half * 4, so = c * 4096u;
  asm volatile("buffer_load_ushort %0, %1, %2, 0 offen" : "=v"(in.dv) : "v"(c * 2u), "s"(rd) : "memory");
#pragma unroll
  for (int i = 0; i < 4; ++i) { ld16<NT_H>(in.La[i], rh, vo, so + i * 1024); ld16(in.Lb[i], rh, vo + 4, so + i * 1024); }
}
DI void convh_load_u(const Params& p, ConvHalf& in, int c, int half, int lane) {
  const i32x4 ru = make_srd(p.ws + WS_KVT, D_ * S_ * 2);
#pragma unroll
  for (int i = 0; i < 2; ++i) ld16_sc1(in.U[i], ru, lane * 16, c * 4096u + (half * 2 + i) * 1024);
}
DI void convh_build(ConvHalf& in, LAS char* cb, int half, int lane) {
#pragma unroll
  for (int i = 0; i < 4; ++i) { PIN(in.La[i]); PIN(in.Lb[i]); }
  PIN(in.U[0]); PIN(in.U[1]); PIN(in.dv);
  LAS char* hb = cb + 2 * half * CV_H;
#pragma unroll
  for (int i = 0; i < 4; ++i) {
    const unsigned o = (i * 64 + lane + 2) * 16;
    u32x4 c0;
#pragma unroll
    for (int e = 0; e < 4; ++e) c0[e] = __builtin_amdgcn_alignbit(in.Lb[i][e], in.La[i][e], 16);
    *(LAS u32x4*)(hb + o) = c0;
    *(LAS u32x4*)(hb + CV_H + o) = in.Lb[i];
  }
#pragma unroll
  for (int i = 0; i < 2; ++i) *(LAS u32x4*)(cb + CV_U + ((half * 2 + i) * 64 + lane) * 16) = in.U[i];
  if (half == 0) {
    const u32x4 z4 = {0u, 0u, 0u, 0u};
    if (lane < 30) *(LAS u32x4*)(cb + CV_U + 4096 + lane * 16) = z4;
    if (lane == 0) {
      const unsigned w0 = in.La[0].x, w1 = in.La[0].y;
#pragma unroll
      for (int cc = 0; cc < 4; ++cc) *(LAS u32x4*)(cb + cc * CV_H) = z4;
      *(LAS u32x4*)(cb + 0 * CV_H + 16) = (u32x4){0u, 0u, 0u, w0 << 16};
      *(LAS u32x4*)(cb + 1 * CV_H + 16) = (u32x4){0u, 0u, 0u, w0};
      *(LAS u32x4*)(cb + 2 * CV_H + 16) = (u32x4){0u, 0u, w0 << 16, __builtin_amdgcn_alignbit(w1, w0, 16)};
      *(LAS u32x4*)(cb + 3 * CV_H + 16) = (u32x4){0u, 0u, w0, w1};
    }
  }
}

DI void g2_issue_w(const i32x4& srdD, const unsigned (&vd)[5], unsigned smb, int nt, int w, int j) {
  const unsigned sb = nt * 160u + (j < 13 ? w : (w < 2 ? w : 1)) * 40960u, lb = smb + L_WR + w * 5120;
#pragma unroll
  for (int i = 0; i < 5; ++i) dma16i<NT_WD>(srdD, vd[i], sb, j * 163840, lb, (j & 3) * G2_WSLOT + i * 1024);
}
DI void g2_wd_offsets(unsigned (&vd)[5], int lane) {
#pragma unroll
  for (int i = 0; i < 5; ++i) { const int idx = i * 64 + lane, rho = idx / 10, ch = idx - rho * 10; vd[i] = rho * 1280 + ch * 16; }
}
template <bool CONV>
DI void g1_loader(KP kp, Params p, unsigned long long pc0, LAS char* sm, unsigned smb, int rb, int ft, int mt, int nt, int hw, int lane, unsigned& tag, int cch, unsigned& dvb, u32x4 (&wy)[3][5]) {
  const i32x4 srdQ = make_srd(p.q, S_ * D_ * 2), srdW = make_srd((hw >> 1) ? p.wu : p.wg, D_ * F_ * 2), srdC = make_srd(p.ws, WS_KVT);
  const unsigned vq0 = (lane >> 3) * 1280 + (((lane & 7) ^ (lane >> 4)) * 16);
  const unsigned vq1 = (lane >> 3) * 1280 + (((lane & 7) ^ (4 + (lane >> 4))) * 16);
  const unsigned vw0 = (lane >> 3) * 3456 + (((lane & 7) ^ (2 * ((lane >> 4) & 1))) * 16);
  const unsigned vw1 = (lane >> 3) * 3456 + (((lane & 7) ^ (2 * (((lane >> 4) & 1) | 2))) * 16);
  const unsigned qb = (unsigned)(rb * 256 + 64 * hw) * 1280u, wb = (unsigned)(32 * (hw & 1)) * 3456u + ft * 128u;
  const unsigned lq = smb + hw * 8192, lw = smb + 32768 + (hw >> 1) * 8192 + (hw & 1) * 4096;
  auto issue = [&](int s, int slot) {
#pragma unroll
    for (int i = 0; i < 4; ++i) dma16i<NT_G1>(srdW, (i & 1) ? vw1 : vw0, wb, (s * 64 + 8 * i) * 3456, lw, slot * G1_SLOT + i * 1024);
#pragma unroll
    for (int i = 0; i < 8; ++i) dma16i<NT_G1>(srdQ, (i & 1) ? vq1 : vq0, qb, i * 10240 + s * 128, lq, slot * G1_SLOT + i * 1024);
  };
  const int xq = blockIdx.x & 7, wv = (blockIdx.x >> 3) * 4 + hw;
  const unsigned junk = smb + L_END - 256;
  const i32x4 srdD = make_srd(p.wd, F_ * D_ * 2);
  auto touch_slot = [&](int sl) {
    const int g = wv >> 5, sub = wv & 31, gsel = (sl + 1) % 3, st = sl + 3;
    unsigned fo = (((27 * (xq & 3)) >> 2) + (lane & 7)) * 128u; fo = fo < 3456u ? fo : 3328u;
    const unsigned vt = (unsigned)((xq >> 2) * 1024 + (sub & 15) * 64 + lane) * 1280u + st * 128u;
    const unsigned vw = (unsigned)(st * 64 + ((sub & 15) >> 1) * 8 + (lane >> 3)) * 3456u + fo;
    const bool stage = TOUCH && g == gsel;
    const void* base = (!stage || sub < 16) ? (const void*)p.q : ((sub & 1) ? (const void*)p.wu : (const void*)p.wg);
    touchg(base, stage ? (sub < 16 ? vt : vw) : 0u, junk);
  };
  unsigned ep; ld4_sc1(ep, srdC, CW_EPOCH);
  BAR();
  ConvHalf in;
  issue(0, 0); issue(1, 1);
  if (CONV) convh_load_h(p, in, cch, hw & 1, lane);
  touch_slot(-1);
  if (CONV) WAITV(22); else WAITV(13);
  PIN(ep); tag = __builtin_amdgcn_readfirstlane(ep) + 1u;
  if ((hw & 1) == 0 && lane == 0) *(LAS unsigned*)(sm + L_PF + (hw >> 1) * 4) = ~tag;
  BAR();
  if (hw == 0) flag_set(p.ws, CW_ARRIVE + blockIdx.x * 4, tag, lane);
  issue(2, 2); touch_slot(0);
  unsigned vd[5]; g2_wd_offsets(vd, lane);
  unsigned f1 = 0, f2 = 0; u32x2 st, st2;
  const unsigned f1off = lane < 16 ? CW_ST + (rb * 64 + hw * 16 + lane) * 16 : CW_ST + (mt * 16 + (lane & 15)) * 16;
  const unsigned f2off = CW_KV + ((CONV ? (cch >> 6) : 0) * 32 + (lane & 31)) * 16;
#pragma unroll
  for (int s = 1; s < 10; ++s) {
    const int nslot = (s + 2) % 3;
    if (s == 1) { if (CONV) WAITV(23); else WAITV(14); }
    else if (s == 6) WAITV(15);
    else if (s == 8) { if (CONV) WAITV(16); else WAITV(15); }
    else if (s == 9) WAITV(10);
    else WAITV(14);
    BAR();
    if (s == 4 || s == 7) p = reload(kp);
    if (s == 5) ld4_sc1(f1, srdC, f1off);
    if (s == 7) {
      PIN(f1);
      if (!__all(f1 == tag)) poll_all(p.ws, f1off, tag);
      ld8_sc1(st, srdC, WS_STATS + (rb * 256 + hw * 64 + lane) * 8, 0);
      ld8_sc1(st2, srdC, WS_STATS + (mt * 64 + lane) * 8, 0);
      if (CONV) ld4_sc1(f2, srdC, f2off);
    }
    if (s <= 7) issue(s + 2, nslot);
    if (s == 8) { g2_issue_w(srdD, vd, smb, nt, hw, 0); g2_issue_w(srdD, vd, smb, nt, hw, 1); }
    if (s <= 6) touch_slot(s);
    if (s == 9) {
      if (CONV) {
        PIN(f2);
        if (!__all(f2 == tag)) poll_all(p.ws, f2off, tag);
        convh_load_u(p, in, cch, hw & 1, lane);
      }
      PIN(st); PIN(st2);
      *(LAS u32x2*)(sm + L_ST + (hw * 64 + lane) * 8) = st;
      if (hw == 0) *(LAS u32x2*)(sm + L_S2 + lane * 8) = st2;
    }
  }
  BAR();
#pragma unroll
  for (int j = 0; j < 3; ++j)
#pragma unroll
    for (int i = 0; i < 5; ++i) ld16i<NT_WD>(wy[j][i], srdD, vd[i], nt * 160u + hw * 40960u, (j + 2) * 163840);
  if (CONV) { WAITV(15); convh_build(in, sm + L_CVG + (hw >> 1) * CV_SZ, hw & 1, lane); dvb = in.dv; }
  WAITV(0);
#pragma unroll
  for (int j = 0; j < 3; ++j)
#pragma unroll
    for (int i = 0; i < 5; ++i) PIN(wy[j][i]);
}

template <bool SWAP>
DI void g1_epilogue(const Params& p, LAS char* sm, int rb, int ft, int wr, int wc, int lane, const f32x4 (&a1)[2][8], const f32x4 (&a2)[2][8], const f32x4 (&csg)[2], const f32x4 (&csu)[2], const f32x2 (&st)[8]) {
  const __amdgpu_buffer_rsrc_t rh = __builtin_amdgcn_make_buffer_rsrc((void*)(p.ws + WS_HM), 0, 54 * 128 * 1024, 0x00020000);
  const unsigned hb = (unsigned)((ft * 2 + wc) * 128 + rb * 16 + wr * 8) * 1024u + lane * 16;
#pragma unroll
  for (int m = 0; m < 8; ++m) {
    asm volatile("; gated epilogue, operand order %0" :: "i"(SWAP ? 1 : 0));
    const float nm = -st[m].x, rs = st[m].y, rsl = rs * -1.44269504089f, rs2 = rs * rs;
    f32x2 tg[4], tu[4], e[4];
#pragma unroll
    for (int h = 0; h < 4; ++h) {
      const int a = h >> 1, i = (h & 1) * 2;
      const f32x4 ga = SWAP ? a2[a][m] : a1[a][m], ua = SWAP ? a1[a][m] : a2[a][m];
      tg[h] = (f32x2){csg[a][i], csg[a][i + 1]} * nm + (f32x2){ga[i], ga[i + 1]};
      tu[h] = (f32x2){csu[a][i], csu[a][i + 1]} * nm + (f32x2){ua[i], ua[i + 1]};
    }
#pragma unroll
    for (int h = 0; h < 4; ++h) { const f32x2 gl = tg[h] * rsl; e[h].x = __builtin_amdgcn_exp2f(gl.x); e[h].y = __builtin_amdgcn_exp2f(gl.y); }
#pragma unroll
    for (int h = 0; h < 4; ++h) { tu[h] = (tg[h] * tu[h]) * rs2; e[h] = e[h] + 1.0f; }
#pragma unroll
    for (int h = 0; h < 4; ++h) { e[h].x = __builtin_amdgcn_rcpf(e[h].x); e[h].y = __builtin_amdgcn_rcpf(e[h].y); }
    u32x4 w;
#pragma unroll
    for (int h = 0; h < 4; ++h) { const f32x2 hv = tu[h] * e[h]; w[h] = pk2(hv.x, hv.y); }
    __builtin_amdgcn_raw_buffer_store_b128(w, rh, hb + m * 1024, 0, 16);
  }
}
DI void g1_math(const Params& p, LAS char* sm, int rb, int ft, int wid, int lane, unsigned tag, unsigned taskflag) {
  const int wr = wid >> 1, wc = wid & 1;
  const int n = lane & 15, kg = lane >> 4, q4 = (lane & 15) >> 2, pp = lane & 3;
  f32x4 a1[2][8], a2[2][8], cs[2];
#pragma unroll
  for (int a = 0; a < 2; ++a) {
    cs[a] = (f32x4){0.f, 0.f, 0.f, 0.f};
#pragma unroll
    for (int m = 0; m < 8; ++m) { a1[a][m] = (f32x4){0.f, 0.f, 0.f, 0.f}; a2[a][m] = (f32x4){0.f, 0.f, 0.f, 0.f}; }
  }
  u32x4 ones_ = {0x3f803f80u, 0x3f803f80u, 0x3f803f80u, 0x3f803f80u};
  PIN(ones_);
  const bf16x8 ones = __builtin_bit_cast(bf16x8, ones_);
  const unsigned tbase = (wr * 128 + n) * 128 + ((kg ^ ((n >> 1) & 7)) * 16);
  const int sw = 2 * (((q4 >> 1) & 1) | ((kg & 1) << 1));
  const unsigned wb0 = 32768 + (8 * kg + q4) * 128 + (((4 * wc + 0 + (pp >> 1)) ^ sw) * 16) + 8 * (pp & 1);
  const unsigned wb1 = 32768 + (8 * kg + q4) * 128 + (((4 * wc + 2 + (pp >> 1)) ^ sw) * 16) + 8 * (pp & 1);
  const unsigned o1 = wr * 8192, o2 = 8192 - o1;
  bf16x8 tP[8], tQ[8], gP[2], gQ[2], uP[2], uQ[2];
#define G1_RD(T, G, U, so, ks) do { LAS char* tp_ = sm + (so) + (tbase ^ ((ks) * 64)); \
    LAS char* w10_ = sm + (so) + wb0 + o1 + (ks) * 4096; LAS char* w11_ = sm + (so) + wb1 + o1 + (ks) * 4096; \
    LAS char* w20_ = sm + (so) + wb0 + o2 + (ks) * 4096; LAS char* w21_ = sm + (so) + wb1 + o2 + (ks) * 4096; \
    G[0] = ldtr(w10_, w10_ + 512); T[0] = *(LAS bf16x8*)(tp_); U[0] = ldtr(w20_, w20_ + 512); G[1] = ldtr(w11_, w11_ + 512); U[1] = ldtr(w21_, w21_ + 512); \
    _Pragma("unroll") for (int m = 1; m < 8; ++m) T[m] = *(LAS bf16x8*)(tp_ + 2048 * m); } while (0)
#define G1_MM(T, G, U) do { _Pragma("unroll") for (int m = 0; m < 8; ++m) { _Pragma("unroll") for (int a = 0; a < 2; ++a) { \
    a1[a][m] = __builtin_amdgcn_mfma_f32_16x16x32_bf16(G[a], T[m], a1[a][m], 0, 0, 0); a2[a][m] = __builtin_amdgcn_mfma_f32_16x16x32_bf16(U[a], T[m], a2[a][m], 0, 0, 0); } } } while (0)
#define G1_CS(G) do { cs[0] = __builtin_amdgcn_mfma_f32_16x16x32_bf16(G[0], ones, cs[0], 0, 0, 0); cs[1] = __builtin_amdgcn_mfma_f32_16x16x32_bf16(G[1], ones, cs[1], 0, 0, 0); } while (0)
#define G1_SCHED() do { _Pragma("unroll") for (int i_ = 0; i_ < 16; ++i_) { __builtin_amdgcn_sched_group_barrier(0x8, 1, 0); __builtin_amdgcn_sched_group_barrier(0x100, 1, 0); } __builtin_amdgcn_sched_group_barrier(0x8, 18, 0); } while (0)
  unsigned so = 0;
  BAR();
  STAMP_AT(1); STAMP_CY(14);
  G1_RD(tP, gP, uP, so, 0);
  G1_RD(tQ, gQ, uQ, so, 1); G1_MM(tP, gP, uP); G1_CS(gP); G1_SCHED();
#pragma unroll 1
  for (int s = 1; s < 10; ++s) {
    so = so == 2 * G1_SLOT ? 0 : so + G1_SLOT;
    BAR();
    if (s == 2 && taskflag) { WAITV(0); flag_set(p.ws, taskflag, tag, lane); }
    G1_RD(tP, gP, uP, so, 0); G1_MM(tQ, gQ, uQ); G1_CS(gQ); G1_SCHED();
    G1_RD(tQ, gQ, uQ, so, 1); G1_MM(tP, gP, uP); G1_CS(gP); G1_SCHED();
  }
  G1_CS(gQ);
  if (n == 0) { *(LAS f32x4*)(sm + L_CS + (wr * 64 + wc * 32 + 4 * kg) * 4) = cs[0]; *(LAS f32x4*)(sm + L_CS + (wr * 64 + wc * 32 + 16 + 4 * kg) * 4) = cs[1]; }
  BAR();
  f32x4 csg[2], csu[2];
#pragma unroll
  for (int a = 0; a < 2; ++a) { csg[a] = *(LAS f32x4*)(sm + L_CS + (wc * 32 + 16 * a + 4 * kg) * 4); csu[a] = *(LAS f32x4*)(sm + L_CS + (64 + wc * 32 + 16 * a + 4 * kg) * 4); }
  f32x2 st[8];
#pragma unroll
  for (int m = 0; m < 8; ++m) st[m] = *(LAS f32x2*)(sm + L_ST + (wr * 128 + 16 * m + n) * 8);
  G1_MM(tQ, gQ, uQ);
  STAMP_AT(2); STAMP_CY(15);
  if (wr == 0) g1_epilogue<false>(p, sm, rb, ft, wr, wc, lane, a1, a2, csg, csu, st);
  else g1_epilogue<true>(p, sm, rb, ft, wr, wc, lane, a1, a2, csg, csu, st);
  STAMP_AT(3);
}

template <int NPRE> constexpr int g2_w_after(int j) { return (j >= 1 && j + 3 <= 13 && j + 3 >= NPRE) ? 5 : 0; }
template <int NPRE> constexpr int g2_ops_after(int j) { return (j == 5 ? 1 : 0) + (j == 7 ? 3 : 0) + ((j >= 1 && j + 2 <= 13) ? 4 : 0) + g2_w_after<NPRE>(j); }
template <int NPRE> constexpr int g2_wait(int j) {
  return j == 0 ? 4 : (j == 1 ? g2_ops_after<NPRE>(1) : g2_w_after<NPRE>(j - 1) + g2_ops_after<NPRE>(j));
}
template <bool ISP>
DI void g2_loader(const Params& p, LAS char* sm, unsigned smb, int mt, int nt, int w, int lane, unsigned tag, unsigned hmflag) {
  const i32x4 srdD = make_srd(p.wd, F_ * D_ * 2), srdH = make_srd(p.ws + WS_HM, 54 * 128 * 1024), srdC = make_srd(p.ws, WS_KVT);
  unsigned vd[5]; g2_wd_offsets(vd, lane);
  const unsigned vh = lane * 16;
  auto fbof = [&](int j) { const int fb = 4 * j + w; return fb < 54 ? fb : 53; };
  auto issueH = [&](int j) {
    const unsigned sb = mt * 4096u + (j < 13 ? w : (w < 2 ? w : 1)) * 131072u, lb = smb + L_HR + w * 4096;
#pragma unroll
    for (int i = 0; i < 4; ++i) dma16i_sc1(srdH, vh, sb, j * 524288 + i * 1024, lb, (j % 3) * 16384 + i * 1024);
  };
  constexpr int XB = ISP ? 1 : 5, NREG = ISP ? 9 : 7, NPRE = XB + NREG;
  u32x4 wreg[NREG][5];
  if (ISP) g2_issue_w(srdD, vd, smb, nt, w, 0);
  else BAR();
#pragma unroll
  for (int j = 0; j < NREG; ++j) {
#pragma unroll
    for (int i = 0; i < 5; ++i) ld16i<NT_WD>(wreg[j][i], srdD, vd[i], nt * 160u + w * 40960u, (j + XB) * 163840);
    if (!ISP && j == 0) {
      WAITV(5);
      flag_set(p.ws, hmflag, tag, lane);
      STAMP_AT(4);
    }
  }
  {
    const unsigned junk = smb + L_END - 256;
#pragma unroll
    for (int r = 0; r < (14 - NPRE + 3) / 4; ++r) {
      const int idx = r * 64 + lane;
      int j = NPRE + (idx >> 4); j = j < 14 ? j : 13;
      touch4(srdD, (unsigned)(fbof(j) * 32 + (mt & 3) * 8 + ((idx & 15) >> 1)) * 1280u + nt * 160u + (idx & 1) * 128u, 0, junk);
    }
  }
  if (ISP && w == 0) {
    poll_all(p.ws, CW_ST + (mt * 16 + (lane & 15)) * 16, tag);
    u32x2 sv; ld8_sc1(sv, srdC, WS_STATS + (mt * 64 + lane) * 8, 0);
    WAITV(0); PIN(sv);
    *(LAS u32x2*)(sm + L_S2 + lane * 8) = sv;
  }
  if (ISP && blockIdx.x == 255 && w == 3) {
    poll_all(p.ws, CW_ARRIVE + lane * 4, tag); poll_all(p.ws, CW_ARRIVE + 256 + lane * 4, tag);
    poll_all(p.ws, CW_ARRIVE + 512 + lane * 4, tag); poll_all(p.ws, CW_ARRIVE + 768 + lane * 4, tag);
    flag_set(p.ws, CW_EPOCH, tag, lane);
  }
  { const int j = lane < 14 ? lane : 0; poll_all(p.ws, CW_HM + (((mt >> 2) * 2 + ((mt >> 1) & 1)) * 54 + fbof(j)) * 16, tag); }
  STAMP_AT(5);
  { const int j = (lane >> 2) < 14 ? (lane >> 2) : 13;
    touch4_sc1(srdH, (unsigned)(fbof(j) * 128 + mt * 4 + (lane & 3)) * 1024u + nt * 128u, 0, smb + L_END - 256); }
  issueH(0); issueH(1); issueH(2);
  WAITV(8);
#pragma unroll
  for (int j = 0; j < NREG; ++j)
#pragma unroll
    for (int i = 0; i < 5; ++i) PIN(wreg[j][i]);
  BAR();
  STAMP_AT(6);
  unsigned zf = 0; u32x4 zr[3];
  const unsigned zoff = CW_Z + (nt * 80 + w * 20 + (lane < 20 ? lane : 0)) * 16;
  auto put_w = [&](int j) {
#pragma unroll
    for (int i = 0; i < 5; ++i) *(LAS u32x4*)(sm + L_WR + (j & 3) * G2_WSLOT + w * 5120 + i * 1024 + lane * 16) = wreg[j - XB][i];
  };
#pragma unroll
  for (int j = 0; j < 14; ++j) {
    if (j == 0) {
#pragma unroll
      for (int jj = XB; jj < 4; ++jj) put_w(jj);
    } else if (j + 3 >= XB && j + 3 < NPRE) put_w(j + 3);
    if (j == 5) ld4_sc1(zf, srdC, zoff);
    if (j == 7) {
      PIN(zf);
      if (!__all(zf == tag)) poll_all(p.ws, zoff, tag);
      const i32x4 srdZ = make_srd(p.ws + WS_ZT, D_ * S_ * 2);
#pragma unroll
      for (int r = 0; r < 3; ++r) {
        int task = r * 64 + lane; task = task < 160 ? task : 159;
        ld16_sc1(zr[r], srdZ, (unsigned)(nt * 80 + w * 20 + (task >> 3)) * 4096u + (mt * 64 + (task & 7) * 8) * 2, 0);
      }
    }
    if (j >= 1 && j + 2 <= 13) issueH(j + 2);
    if (g2_w_after<NPRE>(j)) g2_issue_w(srdD, vd, smb, nt, w, j + 3);
    if (j == 9) {
#pragma unroll
      for (int r = 0; r < 3; ++r) PIN(zr[r]);
#pragma unroll
      for (int r = 0; r < 3; ++r) { const int task = r * 64 + lane; if (task < 160) *(LAS u32x4*)(sm + L_ZS + (w * 20 + (task >> 3)) * ZS_RS + (task & 7) * 16) = zr[r]; }
    }
    if (j < 13) WAITV(g2_wait<NPRE>(j));
    BAR();
  }
}

template <int NY>
DI void g2_math(const Params& p, LAS char* sm, int w, int lane, unsigned tag, int zc, const u32x4 (&wy)[3][5]) {
  f32x4 acc[5][4];
#pragma unroll
  for (int a = 0; a < 5; ++a)
#pragma unroll
    for (int b = 0; b < 4; ++b) acc[a][b] = (f32x4){0.f, 0.f, 0.f, 0.f};
  const int kg = lane >> 4, q4 = (lane & 15) >> 2, pp = lane & 3;
  LAS char* hrd = sm + L_HR + w * 4096 + lane * 16;
  LAS char* wrd = sm + L_WR + w * 5120 + (4 * kg + q4) * 160 + pp * 8;
  bf16x8 hP[4], wP[5], hQ[4], wQ[5];
#define G2_RD(H, W, j) do { LAS char* hp_ = hrd + ((j) % 3) * 16384; LAS char* wp_ = wrd + ((j) & 3) * G2_WSLOT; \
    W[0] = ldtr(wp_, wp_ + 2560); H[0] = *(LAS bf16x8*)(hp_); H[1] = *(LAS bf16x8*)(hp_ + 1024); H[2] = *(LAS bf16x8*)(hp_ + 2048); H[3] = *(LAS bf16x8*)(hp_ + 3072); \
    _Pragma("unroll") for (int a = 1; a < 5; ++a) W[a] = ldtr(wp_ + a * 32, wp_ + a * 32 + 2560); } while (0)
#define G2_MM(H, W) do { _Pragma("unroll") for (int a = 0; a < 5; ++a) _Pragma("unroll") for (int b = 0; b < 4; ++b) \
    acc[a][b] = __builtin_amdgcn_mfma_f32_16x16x32_bf16(W[a], H[b], acc[a][b], 0, 0, 0); } while (0)
#define G2_SCHED() do { _Pragma("unroll") for (int i_ = 0; i_ < 14; ++i_) { __builtin_amdgcn_sched_group_barrier(0x8, 1, 0); __builtin_amdgcn_sched_group_barrier(0x100, 1, 0); } __builtin_amdgcn_sched_group_barrier(0x8, 6, 0); } while (0)
  if (zc >= 0) WAITV(4); else WAITV(0);
  BAR();
  STAMP_AT(9);
  G2_RD(hP, wP, 0);
  auto put_y = [&](int j) {
#pragma unroll
    for (int i = 0; i < 5; ++i) *(LAS u32x4*)(sm + L_WR + (j & 3) * G2_WSLOT + w * 5120 + i * 1024 + lane * 16) = wy[j - 2][i];
  };
  if (NY) { put_y(2); put_y(3); }
  if (zc >= 0) {
    WAITV(0);
    flag_set(p.ws, CW_Z + zc * 16, tag, lane);
  }
  BAR();
  G2_RD(hQ, wQ, 1); G2_MM(hP, wP); G2_SCHED();
  if (NY) put_y(4);
#pragma unroll
  for (int j = 2; j < 14; j += 2) {
    BAR();
    G2_RD(hP, wP, j); G2_MM(hQ, wQ); G2_SCHED();
    BAR();
    G2_RD(hQ, wQ, j + 1); G2_MM(hP, wP); G2_SCHED();
  }
  BAR();
  if (w < 2) G2_MM(hQ, wQ);
  STAMP_AT(10);
#pragma unroll
  for (int a = 0; a < 5; ++a)
#pragma unroll
    for (int b = 0; b < 4; ++b) *(LAS f32x4*)(sm + w * G2_PART + (16 * b + (lane & 15)) * G2_RS + a * 64 + kg * 16) = acc[a][b];
}
DI void g2_final_task(const Params& p, LAS char* sm, int mt, int nt, int gt, int lane, const u32x4& x) {
  const int b = gt / 10, ch = gt - b * 10, tk = b * 16 + (lane & 15);
  f32x4 a0 = (f32x4){0.f, 0.f, 0.f, 0.f}, a1 = a0;
#pragma unroll
  for (int w = 0; w < 4; ++w) { a0 += *(LAS f32x4*)(sm + w * G2_PART + tk * G2_RS + ch * 32); a1 += *(LAS f32x4*)(sm + w * G2_PART + tk * G2_RS + ch * 32 + 16); }
  LAS char* zp = sm + L_ZS + (ch * 8 + ((lane & 15) >> 2)) * ZS_RS + b * 32 + (lane & 3) * 8;
  const u32x2 z0 = __builtin_bit_cast(u32x2, __builtin_amdgcn_ds_read_tr16_b64_v4i16((LAS s16x4*)zp));
  const u32x2 z1 = __builtin_bit_cast(u32x2, __builtin_amdgcn_ds_read_tr16_b64_v4i16((LAS s16x4*)(zp + 4 * ZS_RS)));
  const f32x2 st = *(LAS f32x2*)(sm + L_S2 + tk * 8);
  const float rs = st.y, nmr = -st.x * rs;
  float o[8];
#pragma unroll
  for (int e = 0; e < 8; ++e) {
    const float xv = (e & 1) ? bhi(x[e >> 1]) : blo(x[e >> 1]);
    const unsigned zw = e < 4 ? z0[(e & 3) >> 1] : z1[(e & 3) >> 1];
    const float z = (e & 1) ? bhi(zw) : blo(zw);
    const float xn = fmaf(xv, rs, nmr);
    o[e] = fmaf(xn, z, xv) + (e < 4 ? a0[e] : a1[e - 4]);
  }
  const u32x4 r = {pk2(o[0], o[1]), pk2(o[2], o[3]), pk2(o[4], o[5]), pk2(o[6], o[7])};
  *(GAS u32x4*)(p.out + (mt * 64 + tk) * 640 + nt * 80 + ch * 8) = r;
}
DI u32x4 final_x(const Params& p, int mt, int nt, int gt, int lane) {
  const int b = gt / 10, ch = gt - b * 10;
  return *(const GAS u32x4*)(p.q + (mt * 64 + b * 16 + (lane & 15)) * 640 + nt * 80 + ch * 8);
}

DI void pair_send(LAS char* pb, LAS unsigned* pf, unsigned tag, int lane, const f32x4 (&acc)[8]) {
#pragma unroll
  for (int g = 0; g < 8; ++g) *(LAS f32x4*)(pb + g * 1024 + lane * 16) = acc[g];
  asm volatile("s_waitcnt lgkmcnt(0)" ::: "memory");
  if (lane == 0) *(volatile LAS unsigned*)pf = tag;
}
DI void pair_recv(LAS char* pb, LAS unsigned* pf, unsigned tag, int lane, f32x4 (&acc)[8]) {
#pragma unroll 1
  for (unsigned it = 0; it < (1u << 22); ++it) { if (*(volatile LAS unsigned*)pf == tag) break; __builtin_amdgcn_s_sleep(1); }
  asm volatile("" ::: "memory");
#pragma unroll
  for (int g = 0; g < 8; ++g) acc[g] += *(LAS f32x4*)(pb + g * 1024 + lane * 16);
}

__global__ __launch_bounds__(512, 2) void fused_kernel(Params p_arg) {
  unsigned long long pc0; asm volatile("s_getpc_b64 %0" : "=s"(pc0));
  const KP kp = (KP)__builtin_amdgcn_kernarg_segment_ptr();
  Params p = reload(kp);
  extern __shared__ __attribute__((aligned(16))) char smem_raw[];
  LAS char* sm = (LAS char*)smem_raw;
  const unsigned smb = (unsigned)(size_t)sm;
  const int lane = threadIdx.x & 63, wid = __builtin_amdgcn_readfirstlane(threadIdx.x >> 6);
  const int b = blockIdx.x, x = b & 7, i = b >> 3;
  const int mt = x * 4 + (i & 3), nt = i >> 2;
  STAMP_AT(0);
  if (i < 27) {
    const int mm = 27 * (x & 3) + i, ft = mm >> 2, rb = 4 * (x >> 2) + (mm & 3);
    if (wid < 4) {
      const unsigned ep = __hip_atomic_load((gu32*)(p.ws + CW_EPOCH), __ATOMIC_RELAXED, __HIP_MEMORY_SCOPE_AGENT);
      unsigned taskflag = 0;
      const int id = wid < 2 ? 2 * b + wid : 432 + b;
      if (wid < 3 && id < 512) { task_stats<true>(p, id, lane); taskflag = CW_ST + id * 16; }
      else BAR();
      const unsigned tag = __builtin_amdgcn_readfirstlane(ep) + 1u;
      asm volatile("" :: "s"(tag) : "memory");
      STAMP_AT(13);
      g1_math(p, sm, rb, ft, wid, lane, tag, taskflag);
      p = reload(kp);
      g2_loader<false>(p, sm, smb, mt, nt, wid, lane, tag, CW_HM + ((rb * 2 + (wid >> 1)) * 54 + ft * 2 + (wid & 1)) * 16);
    } else {
      const int hw = wid - 4;
      const bool conv = 2 * b < NCH_G;
      const int cch = 2 * b + (hw >> 1);
      unsigned tag, dvb = 0; int zc = -1;
      if (conv) {
        u32x4 wy[3][5];
        g1_loader<true>(kp, p, pc0, sm, smb, rb, ft, mt, nt, hw, lane, tag, cch, dvb, wy);
        p = reload(kp);
        BAR();
        STAMP_AT(7);
        LAS char* cb = sm + L_CVG + (hw >> 1) * CV_SZ;
        LAS char* pb = sm + L_CP + (hw >> 1) * 8192;
        LAS unsigned* pf = (LAS unsigned*)(sm + L_PF + (hw >> 1) * 4);
        f32x4 acc[8];
        conv_mfma<4>(cb, (hw & 1) * 4, lane, acc);
        if (hw & 1) pair_send(pb, pf, tag, lane, acc);
        else { pair_recv(pb, pf, tag, lane, acc); conv_store(p, cb, cch, lane, dvb, acc); zc = cch; }
        STAMP_AT(8);
        g2_math<3>(p, sm, hw, lane, tag, zc, wy);
      } else {
        u32x4 wy[3][5];
        g1_loader<false>(kp, p, pc0, sm, smb, rb, ft, mt, nt, hw, lane, tag, cch, dvb, wy);
        p = reload(kp);
        BAR();
        g2_math<3>(p, sm, hw, lane, tag, zc, wy);
      }
    }
  } else {
    const int cg = i - 27, pb = x * 5 + cg;
    const unsigned ep = __hip_atomic_load((gu32*)(p.ws + CW_EPOCH), __ATOMIC_RELAXED, __HIP_MEMORY_SCOPE_AGENT);
    const int hw = wid - 4, c0 = NCH_G + pb * NCH_P;
    if (wid < 4) {
      task_kv<0>(p, 2 * cg + (wid & 1), 4 * x + (wid >> 1), lane, [] {});
      const unsigned tag = __builtin_amdgcn_readfirstlane(ep) + 1u;
      asm volatile("" :: "s"(tag) : "memory");
      WAITV(0);
      flag_set(p.ws, CW_KV + ((2 * cg + (wid & 1)) * 32 + 4 * x + (wid >> 1)) * 16, tag, lane);
      BAR();
      if (wid == 0) flag_set(p.ws, CW_ARRIVE + b * 4, tag, lane);
      STAMP_AT(1);
      BAR();
      p = reload(kp);
      touchg(p.wd, (unsigned)((pb * 4 + wid) * 64 + lane) * 128u, smb + L_END - 256);
      if (pb * 4 + wid + 160 < 270) touchg(p.wd, (unsigned)((pb * 4 + wid + 160) * 64 + lane) * 128u, smb + L_END - 256);
      g2_loader<true>(p, sm, smb, mt, nt, wid, lane, tag, 0);
    } else {
      LAS char* cb = sm + L_CVP + hw * CV_SZ;
      const int cA = c0 + hw, cB = c0 + 4 + (hw >> 1);
      ConvIn in, in2;
      task_kv<13>(p, 2 * cg + (wid & 1), 4 * x + (wid >> 1), lane, [&] { conv_load_h(p, in, cA, lane); });
      conv_load_h(p, in2, cB, lane);
      const unsigned tag = __builtin_amdgcn_readfirstlane(ep) + 1u;
      asm volatile("" :: "s"(tag) : "memory");
      if ((hw & 1) == 0 && lane == 0) *(LAS unsigned*)(sm + L_PFP + (hw >> 1) * 4) = ~tag;
      WAITV(13);
      flag_set(p.ws, CW_KV + ((2 * cg + (wid & 1)) * 32 + 4 * x + (wid >> 1)) * 16, tag, lane);
      BAR();
      STAMP_AT(1);
      poll_all(p.ws, CW_KV + (((lane < 32 ? cA : cB) >> 6) * 32 + (lane & 31)) * 16, tag);
      conv_load_u(p, in, cA, lane);
      conv_load_u(p, in2, cB, lane);
      BAR();
      WAITV(4);
      conv_pin(in);
      conv_build(in, cb, lane);
      unsigned dvb = in.dv;
      STAMP_AT(2);
      f32x4 acc[8];
      conv_mfma<8>(cb, 0, lane, acc);
      conv_store(p, cb, cA, lane, dvb, acc);
      STAMP_AT(3);
      WAITV(4);
      conv_pin(in2);
      conv_build(in2, cb, lane);
      dvb = in2.dv;
      STAMP_AT(7);
      conv_mfma<4>(cb, (hw & 1) * 4, lane, acc);
      WAITV(0);
      flag_set(p.ws, CW_Z + cA * 16, tag, lane);
      LAS unsigned* pf = (LAS unsigned*)(sm + L_PFP + (hw >> 1) * 4);
      int zc = -1;
      if (hw & 1) pair_send(cb, pf, tag, lane, acc);
      else { pair_recv(cb + CV_SZ, pf, tag, lane, acc); conv_store(p, cb, cB, lane, dvb, acc); zc = cB; }
      STAMP_AT(8);
      u32x4 wy[3][5];
      g2_math<0>(p, sm, hw, lane, tag, zc, wy);
    }
  }
  p = reload(kp);
  const int gt0 = wid * 4 + (lane >> 4), gt1 = 32 + gt0;
  const u32x4 x0 = final_x(p, mt, nt, gt0, lane);
  u32x4 x1 = x0;
  if (wid < 2) x1 = final_x(p, mt, nt, gt1, lane);
  BAR();
  g2_final_task(p, sm, mt, nt, gt0, lane, x0);
  if (wid < 2) g2_final_task(p, sm, mt, nt, gt1, lane, x1);
  STAMP_AT(11);
}

extern "C" void kernel_launch(void* const* d_in, const int* in_sizes, int n_in, void* d_out, int out_size, void* d_ws, size_t ws_size, hipStream_t stream) {
  Params p;
  p.q = (const u16*)d_in[0]; p.k = (const u16*)d_in[1]; p.v = (const u16*)d_in[2]; p.sk = (const u16*)d_in[3]; p.h = (const u16*)d_in[4]; p.dv = (const u16*)d_in[5];
  p.wg = (const u16*)d_in[7]; p.wu = (const u16*)d_in[8]; p.wd = (const u16*)d_in[9]; p.out = (u16*)d_out; p.ws = (char*)d_ws;
  (void)hipFuncSetAttribute((const void*)fused_kernel, hipFuncAttributeMaxDynamicSharedMemorySize, L_END);
  fused_kernel<<<256, 512, L_END, stream>>>(p);
}
#define HAVE_INTER 1
#define HM_K16 1
```

```cpp
#include <hip/hip_runtime.h>
#include <stdint.h>
typedef unsigned short u16;
typedef __bf16 bf16x8 __attribute__((ext_vector_type(8)));
typedef __bf16 bf16x2 __attribute__((ext_vector_type(2)));
typedef short s16x4 __attribute__((ext_vector_type(4)));
typedef float f32x2 __attribute__((ext_vector_type(2)));
typedef float f32x4 __attribute__((ext_vector_type(4)));
typedef unsigned u32x2 __attribute__((ext_vector_type(2)));
typedef unsigned u32x4 __attribute__((ext_vector_type(4)));
typedef int i32x4 __attribute__((ext_vector_type(4)));
#define LAS __attribute__((address_space(3)))
#define GAS __attribute__((address_space(1)))
#define DI __device__ __forceinline__
typedef GAS unsigned gu32;

#define TOUCH 1
#define TOUCH_CODE 0
#define CODE_BACK 0
#define NT_G1 0
#define NT_KV 1
#define NT_H 1
#define NT_WD 0
#define TLBW 0
constexpr int S_ = 2048, D_ = 640, F_ = 1728;
constexpr unsigned CW_EPOCH = 0;
constexpr unsigned CW_ARRIVE = 1024;
constexpr unsigned CW_KV = 4096;
constexpr unsigned CW_ST = 16384;
constexpr unsigned CW_HM = 24576;
constexpr unsigned CW_Z = 40960;
constexpr unsigned WS_STATS = 65536;
constexpr unsigned WS_KVT = 131072;
constexpr unsigned WS_ZT = WS_KVT + D_ * S_ * 2;
constexpr unsigned WS_HM = WS_ZT + D_ * S_ * 2;
constexpr unsigned WS_END = WS_HM + 54 * 128 * 1024;
constexpr unsigned WS_DBG = 32u << 20;
constexpr int G1_SLOT = 49152;
constexpr int L_CS = 153856, L_ST = L_CS + 512, L_S2 = L_ST + 2048, L_END = L_S2 + 512 + 64 + 256;
constexpr int L_ALLOC = L_END;
constexpr int L_HR = 0, L_WR = 49152, G2_WSLOT = 20480;
constexpr int L_CVG = 90112, L_CVP = 69632;
constexpr int L_ZS = 131072, ZS_RS = 144;
constexpr int L_CP = L_CVG + 2 * 21216, L_PF = L_CS - 64;
constexpr int L_PFP = L_S2 + 512 - 0;
constexpr int G2_RS = 336, G2_PART = 64 * G2_RS;
constexpr int CV_H = 4160, CV_U = 4 * CV_H, CV_SZ = CV_U + 4576;
static_assert(L_ZS + 80 * ZS_RS <= L_PF, "lds");
static_assert(L_CP + 2 * 8192 <= L_PF, "lds");
static_assert(L_CVP + 4 * CV_SZ + 64 <= L_S2, "lds");
constexpr int NCH_P = 6;
constexpr int NCH_G = D_ - 40 * NCH_P;
static_assert(NCH_G <= 2 * 216 && NCH_G % 2 == 0, "conv split");
constexpr unsigned SPIN_MAX = 1u << 16;

struct Params {
  const u16 *q, *k, *v, *sk, *h, *dv, *wg, *wu, *wd;
  u16* out; char* ws;
};

typedef const __attribute__((address_space(4))) Params* KP;
DI Params reload(KP kp) {
  asm volatile("" : "+s"(kp));
  Params r; r.q = kp->q; r.k = kp->k; r.v = kp->v; r.sk = kp->sk; r.h = kp->h; r.dv = kp->dv; r.wg = kp->wg; r.wu = kp->wu; r.wd = kp->wd; r.out = kp->out; r.ws = kp->ws;
  return r;
}
DI i32x4 make_srd(const void* p, unsigned bytes) {
  i32x4 r; unsigned long long a = (unsigned long long)p;
  r.x = (int)(unsigned)a;
  r.y = (int)((unsigned)(a >> 32) & 0xffffu);
  r.z = (int)bytes; r.w = 0x00020000; return r;
}
DI void dma16(const i32x4& srd, unsigned voff, unsigned soff, unsigned lds) {
  asm volatile("s_mov_b32 m0, %3\n\ts_nop 0\n\tbuffer_load_dwordx4 %0, %1, %2 offen lds" :: "v"(voff), "s"(srd), "s"(soff), "s"(lds) : "memory");
}
DI void dma16_sc1(const i32x4& srd, unsigned voff, unsigned soff, unsigned lds) {
  asm volatile("s_mov_b32 m0, %3\n\ts_nop 0\n\tbuffer_load_dwordx4 %0, %1, %2 offen sc1 lds" :: "v"(voff), "s"(srd), "s"(soff), "s"(lds) : "memory");
}
template <int NT = 0>
DI void dma16i(const i32x4& srd, unsigned voff, unsigned sbase, int simm, unsigned lbase, int limm) {
  unsigned t;
  if (NT) asm volatile("s_add_u32 m0, %4, %6\n\ts_add_u32 %0, %3, %5\n\tbuffer_load_dwordx4 %1, %2, %0 offen nt lds" : "=&s"(t) : "v"(voff), "s"(srd), "s"(sbase), "s"(lbase), "i"(simm), "i"(limm) : "memory", "scc");
  else asm volatile("s_add_u32 m0, %4, %6\n\ts_add_u32 %0, %3, %5\n\tbuffer_load_dwordx4 %1, %2, %0 offen lds" : "=&s"(t) : "v"(voff), "s"(srd), "s"(sbase), "s"(lbase), "i"(simm), "i"(limm) : "memory", "scc");
}
DI void dma16i_sc1(const i32x4& srd, unsigned voff, unsigned sbase, int simm, unsigned lbase, int limm) {
  unsigned t;
  asm volatile("s_add_u32 m0, %4, %6\n\ts_add_u32 %0, %3, %5\n\tbuffer_load_dwordx4 %1, %2, %0 offen sc1 lds" : "=&s"(t) : "v"(voff), "s"(srd), "s"(sbase), "s"(lbase), "i"(simm), "i"(limm) : "memory", "scc");
}
template <int NT = 0>
DI void ld16i(u32x4& d, const i32x4& srd, unsigned voff, unsigned sbase, int simm) {
  unsigned t;
  if (NT) asm volatile("s_add_u32 %1, %4, %5\n\tbuffer_load_dwordx4 %0, %2, %3, %1 offen nt" : "=v"(d), "=&s"(t) : "v"(voff), "s"(srd), "s"(sbase), "i"(simm) : "memory", "scc");
  else asm volatile("s_add_u32 %1, %4, %5\n\tbuffer_load_dwordx4 %0, %2, %3, %1 offen" : "=v"(d), "=&s"(t) : "v"(voff), "s"(srd), "s"(sbase), "i"(simm) : "memory", "scc");
}
template <int NT = 0>
DI void ld16(u32x4& d, const i32x4& srd, unsigned voff, unsigned soff) {
  if (NT) asm volatile("buffer_load_dwordx4 %0, %1, %2, %3 offen nt" : "=v"(d) : "v"(voff), "s"(srd), "s"(soff) : "memory");
  else asm volatile("buffer_load_dwordx4 %0, %1, %2, %3 offen" : "=v"(d) : "v"(voff), "s"(srd), "s"(soff) : "memory");
}
DI void ld16_sc1(u32x4& d, const i32x4& srd, unsigned voff, unsigned soff) { asm volatile("buffer_load_dwordx4 %0, %1, %2, %3 offen sc1" : "=v"(d) : "v"(voff), "s"(srd), "s"(soff) : "memory"); }
DI void ld8_sc1(u32x2& d, const i32x4& srd, unsigned voff, unsigned soff) { asm volatile("buffer_load_dwordx2 %0, %1, %2, %3 offen sc1" : "=v"(d) : "v"(voff), "s"(srd), "s"(soff) : "memory"); }
DI void ld8(u32x2& d, const i32x4& srd, unsigned voff, unsigned soff) { asm volatile("buffer_load_dwordx2 %0, %1, %2, %3 offen" : "=v"(d) : "v"(voff), "s"(srd), "s"(soff) : "memory"); }
DI void ld4_sc1(unsigned& d, const i32x4& srd, unsigned voff) { asm volatile("buffer_load_dword %0, %1, %2, 0 offen sc1" : "=v"(d) : "v"(voff), "s"(srd) : "memory"); }
DI void touchg(const void* base, unsigned voff, unsigned lds) {
  asm volatile("s_mov_b32 m0, %2\n\ts_nop 0\n\tglobal_load_lds_dword %0, %1" :: "v"(voff), "s"(base), "s"(lds) : "memory");
}
DI void touchg_nt(const void* base, unsigned voff, unsigned lds) {
  asm volatile("s_mov_b32 m0, %2\n\ts_nop 0\n\tglobal_load_lds_dword %0, %1 nt" :: "v"(voff), "s"(base), "s"(lds) : "memory");
}
DI void touch4(const i32x4& srd, unsigned voff, unsigned soff, unsigned lds) {
  asm volatile("s_mov_b32 m0, %3\n\ts_nop 0\n\tbuffer_load_dword %0, %1, %2 offen lds" :: "v"(voff), "s"(srd), "s"(soff), "s"(lds) : "memory");
}
DI void touch4_sc1(const i32x4& srd, unsigned voff, unsigned soff, unsigned lds) {
  asm volatile("s_mov_b32 m0, %3\n\ts_nop 0\n\tbuffer_load_dword %0, %1, %2 offen sc1 lds" :: "v"(voff), "s"(srd), "s"(soff), "s"(lds) : "memory");
}
#define WAITV(n) asm volatile("s_waitcnt vmcnt(%0)" :: "i"(n) : "memory")
#define BAR() asm volatile("s_waitcnt lgkmcnt(0)\n\ts_barrier" ::: "memory")
#define PIN(x) asm volatile("" : "+v"(x))
DI float b2f(u16 x) { return __uint_as_float(((unsigned)x) << 16); }
DI float blo(unsigned x) { return __uint_as_float(x << 16); }
DI float bhi(unsigned x) { return __uint_as_float(x & 0xffff0000u); }
DI unsigned pk2(float lo, float hi) { bf16x2 r = {(__bf16)lo, (__bf16)hi}; return __builtin_bit_cast(unsigned, r); }
DI float rbf(float x) { return (float)(__bf16)x; }
DI float dot2(unsigned a, unsigned b, float c) { return __builtin_amdgcn_fdot2_f32_bf16(__builtin_bit_cast(bf16x2, a), __builtin_bit_cast(bf16x2, b), c, false); }
template <int CTRL> DI float dpp_add(float x) { return x + __int_as_float(__builtin_amdgcn_update_dpp(0, __float_as_int(x), CTRL, 0xf, 0xf, true)); }
DI float row16_sum(float x) { x = dpp_add<0x128>(x); x = dpp_add<0x124>(x); x = dpp_add<0x122>(x); x = dpp_add<0x121>(x); return x; }
DI bf16x8 ldtr(LAS char* p0, LAS char* p1) {
  s16x4 a = __builtin_amdgcn_ds_read_tr16_b64_v4i16((LAS s16x4*)p0), b = __builtin_amdgcn_ds_read_tr16_b64_v4i16((LAS s16x4*)p1);
  return __builtin_bit_cast(bf16x8, __builtin_shufflevector(a, b, 0, 1, 2, 3, 4, 5, 6, 7));
}
DI void poll_all(const char* ws, unsigned off, unsigned tag) {
  gu32* a = (gu32*)(ws + off);
#pragma unroll 1
  for (unsigned it = 0; it < SPIN_MAX; ++it) {
    const unsigned v = __hip_atomic_load(a, __ATOMIC_RELAXED, __HIP_MEMORY_SCOPE_AGENT);
    if (__all(v == tag)) break;
    __builtin_amdgcn_s_sleep(2);
  }
  asm volatile("" ::: "memory");
}
DI void flag_set(const char* ws, unsigned off, unsigned tag, int lane) {
  if (lane == 0) __hip_atomic_store((gu32*)(ws + off), tag, __ATOMIC_RELAXED, __HIP_MEMORY_SCOPE_AGENT);
}
#define STAMP_CY(idx) do {} while (0)
#define STAMP_AT(idx) do {} while (0)
#define STAMP_INIT() do {} while (0)
#define STAMP_FLUSH() do {} while (0)

template <int NX, class Hook>
DI void task_kv(const Params& p, int cg, int tb, int lane, Hook hook) {
  const int c0 = cg * 64 + 8 * (lane & 7), t0 = tb * 64 + 8 * (lane >> 3);
  const i32x4 rk = make_srd(p.k, S_ * D_ * 2), rv = make_srd(p.v, S_ * D_ * 2), rsk = make_srd(p.sk, D_ * 8);
  u32x4 kk[11], vv[8], ss[4];
#pragma unroll
  for (int j = 0; j < 4; ++j) ld16(ss[j], rsk, (unsigned)(c0 * 8), j * 16);
#pragma unroll
  for (int j = 0; j < 11; ++j) { const int t = t0 - 3 + j; ld16<NT_KV>(kk[j], rk, t >= 0 ? (unsigned)(t * 1280 + c0 * 2) : 0xfffffff0u, 0); }
#pragma unroll
  for (int j = 0; j < 8; ++j) ld16<NT_KV>(vv[j], rv, (unsigned)(t0 * 1280 + c0 * 2), j * 1280);
  hook();
  const __amdgpu_buffer_rsrc_t ro = __builtin_amdgcn_make_buffer_rsrc((void*)(p.ws + WS_KVT), 0, D_ * S_ * 2, 0x00020000);
  WAITV(NX);
  STAMP_AT(12);
#pragma unroll
  for (int j = 0; j < 4; ++j) PIN(ss[j]);
#pragma unroll
  for (int j = 0; j < 11; ++j) PIN(kk[j]);
#pragma unroll
  for (int j = 0; j < 8; ++j) PIN(vv[j]);
#pragma unroll
  for (int cp = 0; cp < 4; ++cp) {
    const f32x2 s0 = {blo(ss[cp].x), blo(ss[cp].z)}, s1 = {bhi(ss[cp].x), bhi(ss[cp].z)}, s2 = {blo(ss[cp].y), blo(ss[cp].w)}, s3 = {bhi(ss[cp].y), bhi(ss[cp].w)};
    f32x2 kx[11];
#pragma unroll
    for (int j = 0; j < 11; ++j) { const unsigned w = kk[j][cp]; kx[j] = (f32x2){blo(w), bhi(w)}; }
    f32x2 o[8];
    __builtin_amdgcn_sched_barrier(0);
#pragma unroll
    for (int j = 0; j < 8; ++j) o[j] = kx[j] * s3;
    __builtin_amdgcn_sched_barrier(0);
#pragma unroll
    for (int j = 0; j < 8; ++j) o[j] = kx[j + 1] * s2 + o[j];
    __builtin_amdgcn_sched_barrier(0);
#pragma unroll
    for (int j = 0; j < 8; ++j) o[j] = kx[j + 2] * s1 + o[j];
    __builtin_amdgcn_sched_barrier(0);
#pragma unroll
    for (int j = 0; j < 8; ++j) o[j] = kx[j + 3] * s0 + o[j];
    __builtin_amdgcn_sched_barrier(0);
    f32x2 vx[8];
#pragma unroll
    for (int j = 0; j < 8; ++j) { const unsigned w = vv[j][cp]; vx[j] = (f32x2){blo(w), bhi(w)}; }
    __builtin_amdgcn_sched_barrier(0);
#pragma unroll
    for (int j = 0; j < 8; ++j) o[j] = o[j] * vx[j];
    __builtin_amdgcn_sched_barrier(0);
    const u32x4 r0 = {pk2(o[7].x, o[6].x), pk2(o[5].x, o[4].x), pk2(o[3].x, o[2].x), pk2(o[1].x, o[0].x)};
    const u32x4 r1 = {pk2(o[7].y, o[6].y), pk2(o[5].y, o[4].y), pk2(o[3].y, o[2].y), pk2(o[1].y, o[0].y)};
    __builtin_amdgcn_raw_buffer_store_b128(r0, ro, (unsigned)((c0 + 2 * cp) * 4096 + (2040 - t0) * 2), 0, 16);
    __builtin_amdgcn_raw_buffer_store_b128(r1, ro, (unsigned)((c0 + 2 * cp + 1) * 4096 + (2040 - t0) * 2), 0, 16);
  }
}
template <bool PRE>
DI void task_stats(const Params& p, int id, int lane) {
  const i32x4 rq = make_srd(p.q, S_ * D_ * 2);
  const int row = id * 4 + (lane >> 4), l16 = lane & 15;
  u32x4 x[5];
#pragma unroll
  for (int j = 0; j < 5; ++j) ld16<NT_G1>(x[j], rq, (unsigned)row * 1280u + l16 * 16, j * 256);
  const __amdgpu_buffer_rsrc_t rs = __builtin_amdgcn_make_buffer_rsrc((void*)(p.ws + WS_STATS), 0, S_ * 8, 0x00020000);
  if (PRE) BAR();
  WAITV(0);
  STAMP_AT(12);
#pragma unroll
  for (int j = 0; j < 5; ++j) PIN(x[j]);
  float s4[4] = {0.f, 0.f, 0.f, 0.f};
#pragma unroll
  for (int j = 0; j < 5; ++j)
#pragma unroll
    for (int e = 0; e < 4; ++e) s4[e] = dot2(x[j][e], 0x3f803f80u, s4[e]);
  const float mean = row16_sum((s4[0] + s4[1]) + (s4[2] + s4[3])) * (1.0f / 640.0f);
  f32x2 q2[2] = {{0.f, 0.f}, {0.f, 0.f}};
#pragma unroll
  for (int j = 0; j < 5; ++j)
#pragma unroll
    for (int e = 0; e < 4; ++e) { const unsigned w = x[j][e]; const f32x2 d = (f32x2){blo(w), bhi(w)} - mean; q2[e & 1] = d * d + q2[e & 1]; }
  const float var = row16_sum((q2[0].x + q2[0].y) + (q2[1].x + q2[1].y)) * (1.0f / 640.0f);
  const float mb = rbf(mean), sb = rbf(__builtin_amdgcn_sqrtf(rbf(rbf(var) + 1e-5f)));
  u32x2 o; o.x = __float_as_uint(mb); o.y = __float_as_uint(1.0f / sb);
  __builtin_amdgcn_raw_buffer_store_b64(o, rs, l16 == 0 ? (unsigned)row * 8u : 0xfffffff0u, 0, 16);
}

struct ConvIn { u32x4 L0[4], L1[4], L2[4], U[4]; unsigned dv; };
DI void conv_load_h(const Params& p, ConvIn& in, int c, int lane) {
  const i32x4 rh = make_srd(p.h, D_ * S_ * 2), rd = make_srd(p.dv, D_ * 2);
  const unsigned vo = lane * 16, so = c * 4096u;
  asm volatile("buffer_load_ushort %0, %1, %2, 0 offen" : "=v"(in.dv) : "v"(c * 2u), "s"(rd) : "memory");
#pragma unroll
  for (int i = 0; i < 4; ++i) { ld16<NT_H>(in.L0[i], rh, vo, so + i * 1024); ld16(in.L1[i], rh, vo + 4, so + i * 1024); ld16(in.L2[i], rh, vo + 8, so + i * 1024); }
}
DI void conv_load_u(const Params& p, ConvIn& in, int c, int lane) {
  const i32x4 ru = make_srd(p.ws + WS_KVT, D_ * S_ * 2);
#pragma unroll
  for (int i = 0; i < 4; ++i) ld16_sc1(in.U[i], ru, lane * 16, c * 4096u + i * 1024);
}
DI void conv_pin(ConvIn& in) {
#pragma unroll
  for (int i = 0; i < 4; ++i) { PIN(in.L0[i]); PIN(in.L1[i]); PIN(in.L2[i]); PIN(in.U[i]); }
  PIN(in.dv);
}
DI void conv_build(const ConvIn& in, LAS char* cb, int lane) {
#pragma unroll
  for (int i = 0; i < 4; ++i) {
    const unsigned o = (i * 64 + lane + 2) * 16;
    u32x4 c0, c2;
#pragma unroll
    for (int e = 0; e < 4; ++e) { c0[e] = __builtin_amdgcn_alignbit(in.L1[i][e], in.L0[i][e], 16); c2[e] = __builtin_amdgcn_alignbit(in.L2[i][e], in.L1[i][e], 16); }
    *(LAS u32x4*)(cb + 0 * CV_H + o) = c0;
    *(LAS u32x4*)(cb + 1 * CV_H + o) = in.L1[i];
    *(LAS u32x4*)(cb + 2 * CV_H + o) = c2;
    *(LAS u32x4*)(cb + 3 * CV_H + o) = in.L2[i];
    *(LAS u32x4*)(cb + CV_U + (i * 64 + lane) * 16) = in.U[i];
  }
  const u32x4 z4 = {0u, 0u, 0u, 0u};
  if (lane < 30) *(LAS u32x4*)(cb + CV_U + 4096 + lane * 16) = z4;
  if (lane == 0) {
    const unsigned w0 = in.L0[0].x, w1 = in.L0[0].y;
#pragma unroll
    for (int cc = 0; cc < 4; ++cc) *(LAS u32x4*)(cb + cc * CV_H) = z4;
    *(LAS u32x4*)(cb + 0 * CV_H + 16) = (u32x4){0u, 0u, 0u, w0 << 16};
    *(LAS u32x4*)(cb + 1 * CV_H + 16) = (u32x4){0u, 0u, 0u, w0};
    *(LAS u32x4*)(cb + 2 * CV_H + 16) = (u32x4){0u, 0u, w0 << 16, __builtin_amdgcn_alignbit(w1, w0, 16)};
    *(LAS u32x4*)(cb + 3 * CV_H + 16) = (u32x4){0u, 0u, w0, w1};
  }
}
template <int TRIPS>
DI void conv_mfma(LAS char* cb, int d0, int lane, f32x4 (&acc)[8]) {
  const int r = lane & 15, kg = lane >> 4, kap = kg >> 1, bp = kg & 1;
  unsigned a0 = (unsigned)(size_t)cb + (r & 3) * CV_H + 32 * kap + 16 * bp + 2 * (r & 12) + d0 * 64;
  unsigned a1 = a0 + 8;
  unsigned ub = (unsigned)(size_t)cb + CV_U + (4064 - 3584) - 32 * r + 32 * kap + 16 * bp + d0 * 64;
  PIN(a0); PIN(a1); PIN(ub);
#pragma unroll
  for (int g = 0; g < 8; ++g) acc[g] = (f32x4){0.f, 0.f, 0.f, 0.f};
  bf16x8 A[8], B[8];
#define CV_LDA(g) do { const s16x4 x0_ = *(LAS s16x4*)(size_t)(a0 + (g) * 512), x1_ = *(LAS s16x4*)(size_t)(a1 + (g) * 512); \
    A[g] = __builtin_bit_cast(bf16x8, __builtin_shufflevector(x0_, x1_, 0, 1, 2, 3, 4, 5, 6, 7)); } while (0)
#define CV_LDB(g) do { B[g] = *(LAS bf16x8*)(size_t)(ub + (7 - (g)) * 512); } while (0)
#pragma unroll
  for (int g = 0; g < 8; ++g) { CV_LDA(g); CV_LDB(7 - g); }
#pragma unroll 1
  for (int d = 0; d < TRIPS; ++d) {
    a0 += 64; a1 += 64; ub += 64; PIN(a0); PIN(a1); PIN(ub);
#pragma unroll
    for (int gm = 0; gm < 8; ++gm) {
#pragma unroll
      for (int gp = 7 - gm; gp >= 0; --gp) acc[gm + gp] = __builtin_amdgcn_mfma_f32_16x16x32_bf16(A[gm], B[gp], acc[gm + gp], 0, 0, 0);
      __builtin_amdgcn_sched_barrier(0);
      CV_LDA(gm); CV_LDB(7 - gm);
      __builtin_amdgcn_sched_barrier(0);
    }
  }
}
DI void conv_store(const Params& p, LAS char* cb, int c, int lane, unsigned dvb, const f32x4 (&acc)[8]) {
  const int r = lane & 15, kg = lane >> 4;
  const float dv = __uint_as_float(dvb << 16);
  const __amdgpu_buffer_rsrc_t rz = __builtin_amdgcn_make_buffer_rsrc((void*)(p.ws + WS_ZT), 0, D_ * S_ * 2, 0x00020000);
  u32x2 z[8];
#pragma unroll
  for (int g = 0; g < 8; ++g) {
    const int t = 256 * g + 16 * r + 4 * kg;
    const u32x2 kv = *(LAS u32x2*)(cb + CV_U + (2044 - t) * 2);
    const float y0 = fmaf(bhi(kv.y), dv, acc[g][0]), y1 = fmaf(blo(kv.y), dv, acc[g][1]), y2 = fmaf(bhi(kv.x), dv, acc[g][2]), y3 = fmaf(blo(kv.x), dv, acc[g][3]);
    z[g].x = pk2(y0, y1); z[g].y = pk2(y2, y3);
  }
#pragma unroll
  for (int g = 0; g < 8; g += 2) {
    const auto s0 = __builtin_amdgcn_permlane16_swap(z[g].x, z[g + 1].x, false, false);
    const auto s1 = __builtin_amdgcn_permlane16_swap(z[g].y, z[g + 1].y, false, false);
    const u32x4 o = {s0[0], s1[0], s0[1], s1[1]};
    const int gg = g + (kg & 1);
    __builtin_amdgcn_raw_buffer_store_b128(o, rz, c * 4096u + (256 * gg + 16 * r + 4 * (kg & 2)) * 2, 0, 16);
  }
}
struct ConvHalf { u32x4 La[4], Lb[4], U[2]; unsigned dv; };
DI void convh_load_h(const Params& p, ConvHalf& in, int c, int half, int lane) {
  const i32x4 rh = make_srd(p.h, D_ * S_ * 2), rd = make_srd(p.dv, D_ * 2);
  const unsigned vo = lane * 16 + half * 4, so = c * 4096u;
  asm volatile("buffer_load_ushort %0, %1, %2, 0 offen" : "=v"(in.dv) : "v"(c * 2u), "s"(rd) : "memory");
#pragma unroll
  for (int i = 0; i < 4; ++i) { ld16<NT_H>(in.La[i], rh, vo, so + i * 1024); ld16(in.Lb[i], rh, vo + 4, so + i * 1024); }
}
DI void convh_load_u(const Params& p, ConvHalf& in, int c, int half, int lane) {
  const i32x4 ru = make_srd(p.ws + WS_KVT, D_ * S_ * 2);
#pragma unroll
  for (int i = 0; i < 2; ++i) ld16_sc1(in.U[i], ru, lane * 16, c * 4096u + (half * 2 + i) * 1024);
}
DI void convh_build(ConvHalf& in, LAS char* cb, int half, int lane) {
#pragma unroll
  for (int i = 0; i < 4; ++i) { PIN(in.La[i]); PIN(in.Lb[i]); }
  PIN(in.U[0]); PIN(in.U[1]); PIN(in.dv);
  LAS char* hb = cb + 2 * half * CV_H;
#pragma unroll
  for (int i = 0; i < 4; ++i) {
    const unsigned o = (i * 64 + lane + 2) * 16;
    u32x4 c0;
#pragma unroll
    for (int e = 0; e < 4; ++e) c0[e] = __builtin_amdgcn_alignbit(in.Lb[i][e], in.La[i][e], 16);
    *(LAS u32x4*)(hb + o) = c0;
    *(LAS u32x4*)(hb + CV_H + o) = in.Lb[i];
  }
#pragma unroll
  for (int i = 0; i < 2; ++i) *(LAS u32x4*)(cb + CV_U + ((half * 2 + i) * 64 + lane) * 16) = in.U[i];
  if (half == 0) {
    const u32x4 z4 = {0u, 0u, 0u, 0u};
    if (lane < 30) *(LAS u32x4*)(cb + CV_U + 4096 + lane * 16) = z4;
    if (lane == 0) {
      const unsigned w0 = in.La[0].x, w1 = in.La[0].y;
#pragma unroll
      for (int cc = 0; cc < 4; ++cc) *(LAS u32x4*)(cb + cc * CV_H) = z4;
      *(LAS u32x4*)(cb + 0 * CV_H + 16) = (u32x4){0u, 0u, 0u, w0 << 16};
      *(LAS u32x4*)(cb + 1 * CV_H + 16) = (u32x4){0u, 0u, 0u, w0};
      *(LAS u32x4*)(cb + 2 * CV_H + 16) = (u32x4){0u, 0u, w0 << 16, __builtin_amdgcn_alignbit(w1, w0, 16)};
      *(LAS u32x4*)(cb + 3 * CV_H + 16) = (u32x4){0u, 0u, w0, w1};
    }
  }
}

DI void g2_issue_w(const i32x4& srdD, const unsigned (&vd)[5], unsigned smb, int nt, int w, int j) {
  const unsigned sb = nt * 160u + (j < 13 ? w : (w < 2 ? w : 1)) * 40960u, lb = smb + L_WR + w * 5120;
#pragma unroll
  for (int i = 0; i < 5; ++i) dma16i<NT_WD>(srdD, vd[i], sb, j * 163840, lb, (j & 3) * G2_WSLOT + i * 1024);
}
DI void g2_wd_offsets(unsigned (&vd)[5], int lane) {
#pragma unroll
  for (int i = 0; i < 5; ++i) { const int idx = i * 64 + lane, rho = idx / 10, ch = idx - rho * 10; vd[i] = rho * 1280 + ch * 16; }
}
template <bool CONV>
DI void g1_loader(KP kp, Params p, unsigned long long pc0, LAS char* sm, unsigned smb, int rb, int ft, int mt, int nt, int hw, int lane, unsigned& tag, int cch, unsigned& dvb, u32x4 (&wy)[3][5]) {
  const i32x4 srdQ = make_srd(p.q, S_ * D_ * 2), srdW = make_srd((hw >> 1) ? p.wu : p.wg, D_ * F_ * 2), srdC = make_srd(p.ws, WS_KVT);
  const unsigned vq0 = (lane >> 3) * 1280 + (((lane & 7) ^ (lane >> 4)) * 16);
  const unsigned vq1 = (lane >> 3) * 1280 + (((lane & 7) ^ (4 + (lane >> 4))) * 16);
  const unsigned vw0 = (lane >> 3) * 3456 + (((lane & 7) ^ (2 * ((lane >> 4) & 1))) * 16);
  const unsigned vw1 = (lane >> 3) * 3456 + (((lane & 7) ^ (2 * (((lane >> 4) & 1) | 2))) * 16);
  const unsigned qb = (unsigned)(rb * 256 + 64 * hw) * 1280u, wb = (unsigned)(32 * (hw & 1)) * 3456u + ft * 128u;
  const unsigned lq = smb + hw * 8192, lw = smb + 32768 + (hw >> 1) * 8192 + (hw & 1) * 4096;
  auto issue = [&](int s, int slot) {
#pragma unroll
    for (int i = 0; i < 4; ++i) dma16i<NT_G1>(srdW, (i & 1) ? vw1 : vw0, wb, (s * 64 + 8 * i) * 3456, lw, slot * G1_SLOT + i * 1024);
#pragma unroll
    for (int i = 0; i < 8; ++i) dma16i<NT_G1>(srdQ, (i & 1) ? vq1 : vq0, qb, i * 10240 + s * 128, lq, slot * G1_SLOT + i * 1024);
  };
  const int xq = blockIdx.x & 7, wv = (blockIdx.x >> 3) * 4 + hw;
  const unsigned junk = smb + L_END - 256;
  const i32x4 srdD = make_srd(p.wd, F_ * D_ * 2);
  auto touch_slot = [&](int sl) {
    const int g = wv >> 5, sub = wv & 31, gsel = (sl + 1) % 3, st = sl + 3;
    unsigned fo = (((27 * (xq & 3)) >> 2) + (lane & 7)) * 128u; fo = fo < 3456u ? fo : 3328u;
    const unsigned vt = (unsigned)((xq >> 2) * 1024 + (sub & 15) * 64 + lane) * 1280u + st * 128u;
    const unsigned vw = (unsigned)(st * 64 + ((sub & 15) >> 1) * 8 + (lane >> 3)) * 3456u + fo;
    const bool stage = TOUCH && g == gsel;
    const void* base = (!stage || sub < 16) ? (const void*)p.q : ((sub & 1) ? (const void*)p.wu : (const void*)p.wg);
    touchg(base, stage ? (sub < 16 ? vt : vw) : 0u, junk);
  };
  unsigned ep; ld4_sc1(ep, srdC, CW_EPOCH);
  BAR();
  ConvHalf in;
  issue(0, 0); issue(1, 1);
  if (CONV) convh_load_h(p, in, cch, hw & 1, lane);
  touch_slot(-1);
  if (CONV) WAITV(22); else WAITV(13);
  PIN(ep); tag = __builtin_amdgcn_readfirstlane(ep) + 1u;
  if ((hw & 1) == 0 && lane == 0) *(LAS unsigned*)(sm + L_PF + (hw >> 1) * 4) = ~tag;
  BAR();
  if (hw == 0) flag_set(p.ws, CW_ARRIVE + blockIdx.x * 4, tag, lane);
  issue(2, 2); touch_slot(0);
  unsigned vd[5]; g2_wd_offsets(vd, lane);
  unsigned f1 = 0, f2 = 0; u32x2 st, st2;
  const unsigned f1off = lane < 16 ? CW_ST + (rb * 64 + hw * 16 + lane) * 16 : CW_ST + (mt * 16 + (lane & 15)) * 16;
  const unsigned f2off = CW_KV + ((CONV ? (cch >> 6) : 0) * 32 + (lane & 31)) * 16;
#pragma unroll
  for (int s = 1; s < 10; ++s) {
    const int nslot = (s + 2) % 3;
    if (s == 1) { if (CONV) WAITV(23); else WAITV(14); }
    else if (s == 6) WAITV(15);
    else if (s == 8) { if (CONV) WAITV(16); else WAITV(15); }
    else if (s == 9) WAITV(10);
    else WAITV(14);
    BAR();
    if (s == 4 || s == 7) p = reload(kp);
    if (s == 5) ld4_sc1(f1, srdC, f1off);
    if (s == 7) {
      PIN(f1);
      if (!__all(f1 == tag)) poll_all(p.ws, f1off, tag);
      ld8_sc1(st, srdC, WS_STATS + (rb * 256 + hw * 64 + lane) * 8, 0);
      ld8_sc1(st2, srdC, WS_STATS + (mt * 64 + lane) * 8, 0);
      if (CONV) ld4_sc1(f2, srdC, f2off);
    }
    if (s <= 7) issue(s + 2, nslot);
    if (s == 8) { g2_issue_w(srdD, vd, smb, nt, hw, 0); g2_issue_w(srdD, vd, smb, nt, hw, 1); }
    if (s <= 6) touch_slot(s);
    if (s == 9) {
      if (CONV) {
        PIN(f2);
        if (!__all(f2 == tag)) poll_all(p.ws, f2off, tag);
        convh_load_u(p, in, cch, hw & 1, lane);
      }
      PIN(st); PIN(st2);
      *(LAS u32x2*)(sm + L_ST + (hw * 64 + lane) * 8) = st;
      if (hw == 0) *(LAS u32x2*)(sm + L_S2 + lane * 8) = st2;
    }
  }
  BAR();
#pragma unroll
  for (int j = 0; j < 3; ++j)
#pragma unroll
    for (int i = 0; i < 5; ++i) ld16i<NT_WD>(wy[j][i], srdD, vd[i], nt * 160u + hw * 40960u, (j + 2) * 163840);
  if (CONV) { WAITV(15); convh_build(in, sm + L_CVG + (hw >> 1) * CV_SZ, hw & 1, lane); dvb = in.dv; }
  WAITV(0);
#pragma unroll
  for (int j = 0; j < 3; ++j)
#pragma unroll
    for (int i = 0; i < 5; ++i) PIN(wy[j][i]);
}

template <bool SWAP>
DI void g1_epilogue(const Params& p, LAS char* sm, int rb, int ft, int wr, int wc, int lane, const f32x4 (&a1)[2][8], const f32x4 (&a2)[2][8], const f32x4 (&csg)[2], const f32x4 (&csu)[2], const f32x2 (&st)[8]) {
  const __amdgpu_buffer_rsrc_t rh = __builtin_amdgcn_make_buffer_rsrc((void*)(p.ws + WS_HM), 0, 54 * 128 * 1024, 0x00020000);
  const unsigned hb = (unsigned)((ft * 2 + wc) * 128 + rb * 16 + wr * 8) * 1024u + lane * 16;
#pragma unroll
  for (int m = 0; m < 8; ++m) {
    asm volatile("; gated epilogue, operand order %0" :: "i"(SWAP ? 1 : 0));
    const float nm = -st[m].x, rs = st[m].y, rsl = rs * -1.44269504089f, rs2 = rs * rs;
    f32x2 tg[4], tu[4], e[4];
#pragma unroll
    for (int h = 0; h < 4; ++h) {
      const int a = h >> 1, i = (h & 1) * 2;
      const f32x4 ga = SWAP ? a2[a][m] : a1[a][m], ua = SWAP ? a1[a][m] : a2[a][m];
      tg[h] = (f32x2){csg[a][i], csg[a][i + 1]} * nm + (f32x2){ga[i], ga[i + 1]};
      tu[h] = (f32x2){csu[a][i], csu[a][i + 1]} * nm + (f32x2){ua[i], ua[i + 1]};
    }
#pragma unroll
    for (int h = 0; h < 4; ++h) { const f32x2 gl = tg[h] * rsl; e[h].x = __builtin_amdgcn_exp2f(gl.x); e[h].y = __builtin_amdgcn_exp2f(gl.y); }
#pragma unroll
    for (int h = 0; h < 4; ++h) { tu[h] = (tg[h] * tu[h]) * rs2; e[h] = e[h] + 1.0f; }
#pragma unroll
    for (int h = 0; h < 4; ++h) { e[h].x = __builtin_amdgcn_rcpf(e[h].x); e[h].y = __builtin_amdgcn_rcpf(e[h].y); }
    u32x4 w;
#pragma unroll
    for (int h = 0; h < 4; ++h) { const f32x2 hv = tu[h] * e[h]; w[h] = pk2(hv.x, hv.y); }
    __builtin_amdgcn_raw_buffer_store_b128(w, rh, hb + m * 1024, 0, 16);
  }
}
DI void g1_math(const Params& p, LAS char* sm, int rb, int ft, int wid, int lane, unsigned tag, unsigned taskflag) {
  const int wr = wid >> 1, wc = wid & 1;
  const int n = lane & 15, kg = lane >> 4, q4 = (lane & 15) >> 2, pp = lane & 3;
  f32x4 a1[2][8], a2[2][8], cs[2];
#pragma unroll
  for (int a = 0; a < 2; ++a) {
    cs[a] = (f32x4){0.f, 0.f, 0.f, 0.f};
#pragma unroll
    for (int m = 0; m < 8; ++m) { a1[a][m] = (f32x4){0.f, 0.f, 0.f, 0.f}; a2[a][m] = (f32x4){0.f, 0.f, 0.f, 0.f}; }
  }
  u32x4 ones_ = {0x3f803f80u, 0x3f803f80u, 0x3f803f80u, 0x3f803f80u};
  PIN(ones_);
  const bf16x8 ones = __builtin_bit_cast(bf16x8, ones_);
  const unsigned tbase = (wr * 128 + n) * 128 + ((kg ^ ((n >> 1) & 7)) * 16);
  const int sw = 2 * (((q4 >> 1) & 1) | ((kg & 1) << 1));
  const unsigned wb0 = 32768 + (8 * kg + q4) * 128 + (((4 * wc + 0 + (pp >> 1)) ^ sw) * 16) + 8 * (pp & 1);
  const unsigned wb1 = 32768 + (8 * kg + q4) * 128 + (((4 * wc + 2 + (pp >> 1)) ^ sw) * 16) + 8 * (pp & 1);
  const unsigned o1 = wr * 8192, o2 = 8192 - o1;
  bf16x8 tP[8], tQ[8], gP[2], gQ[2], uP[2], uQ[2];
#define G1_RD(T, G, U, so, ks) do { LAS char* tp_ = sm + (so) + (tbase ^ ((ks) * 64)); \
    LAS char* w10_ = sm + (so) + wb0 + o1 + (ks) * 4096; LAS char* w11_ = sm + (so) + wb1 + o1 + (ks) * 4096; \
    LAS char* w20_ = sm + (so) + wb0 + o2 + (ks) * 4096; LAS char* w21_ = sm + (so) + wb1 + o2 + (ks) * 4096; \
    G[0] = ldtr(w10_, w10_ + 512); T[0] = *(LAS bf16x8*)(tp_); U[0] = ldtr(w20_, w20_ + 512); G[1] = ldtr(w11_, w11_ + 512); U[1] = ldtr(w21_, w21_ + 512); \
    _Pragma("unroll") for (int m = 1; m < 8; ++m) T[m] = *(LAS bf16x8*)(tp_ + 2048 * m); } while (0)
#define G1_MM(T, G, U) do { _Pragma("unroll") for (int m = 0; m < 8; ++m) { _Pragma("unroll") for (int a = 0; a < 2; ++a) { \
    a1[a][m] = __builtin_amdgcn_mfma_f32_16x16x32_bf16(G[a], T[m], a1[a][m], 0, 0, 0); a2[a][m] = __builtin_amdgcn_mfma_f32_16x16x32_bf16(U[a], T[m], a2[a][m], 0, 0, 0); } } } while (0)
#define G1_CS(G) do { cs[0] = __builtin_amdgcn_mfma_f32_16x16x32_bf16(G[0], ones, cs[0], 0, 0, 0); cs[1] = __builtin_amdgcn_mfma_f32_16x16x32_bf16(G[1], ones, cs[1], 0, 0, 0); } while (0)
#define G1_SCHED() do { _Pragma("unroll") for (int i_ = 0; i_ < 16; ++i_) { __builtin_amdgcn_sched_group_barrier(0x8, 1, 0); __builtin_amdgcn_sched_group_barrier(0x100, 1, 0); } __builtin_amdgcn_sched_group_barrier(0x8, 18, 0); } while (0)
  unsigned so = 0;
  BAR();
  STAMP_AT(1); STAMP_CY(14);
  G1_RD(tP, gP, uP, so, 0);
  G1_RD(tQ, gQ, uQ, so, 1); G1_MM(tP, gP, uP); G1_CS(gP); G1_SCHED();
#pragma unroll 1
  for (int s = 1; s < 10; ++s) {
    so = so == 2 * G1_SLOT ? 0 : so + G1_SLOT;
    BAR();
    if (s == 2 && taskflag) { WAITV(0); flag_set(p.ws, taskflag, tag, lane); }
    G1_RD(tP, gP, uP, so, 0); G1_MM(tQ, gQ, uQ); G1_CS(gQ); G1_SCHED();
    G1_RD(tQ, gQ, uQ, so, 1); G1_MM(tP, gP, uP); G1_CS(gP); G1_SCHED();
  }
  G1_CS(gQ);
  if (n == 0) { *(LAS f32x4*)(sm + L_CS + (wr * 64 + wc * 32 + 4 * kg) * 4) = cs[0]; *(LAS f32x4*)(sm + L_CS + (wr * 64 + wc * 32 + 16 + 4 * kg) * 4) = cs[1]; }
  BAR();
  f32x4 csg[2], csu[2];
#pragma unroll
  for (int a = 0; a < 2; ++a) { csg[a] = *(LAS f32x4*)(sm + L_CS + (wc * 32 + 16 * a + 4 * kg) * 4); csu[a] = *(LAS f32x4*)(sm + L_CS + (64 + wc * 32 + 16 * a + 4 * kg) * 4); }
  f32x2 st[8];
#pragma unroll
  for (int m = 0; m < 8; ++m) st[m] = *(LAS f32x2*)(sm + L_ST + (wr * 128 + 16 * m + n) * 8);
  G1_MM(tQ, gQ, uQ);
  STAMP_AT(2); STAMP_CY(15);
  if (wr == 0) g1_epilogue<false>(p, sm, rb, ft, wr, wc, lane, a1, a2, csg, csu, st);
  else g1_epilogue<true>(p, sm, rb, ft, wr, wc, lane, a1, a2, csg, csu, st);
  STAMP_AT(3);
}

template <int NPRE> constexpr int g2_w_after(int j) { return (j >= 1 && j + 3 <= 13 && j + 3 >= NPRE) ? 5 : 0; }
template <int NPRE> constexpr int g2_ops_after(int j) { return (j == 5 ? 1 : 0) + (j == 7 ? 3 : 0) + ((j >= 1 && j + 2 <= 13) ? 4 : 0) + g2_w_after<NPRE>(j); }
template <int NPRE> constexpr int g2_wait(int j) {
  return j == 0 ? 4 : (j == 1 ? g2_ops_after<NPRE>(1) : g2_w_after<NPRE>(j - 1) + g2_ops_after<NPRE>(j));
}
template <bool ISP>
DI void g2_loader(const Params& p, LAS char* sm, unsigned smb, int mt, int nt, int w, int lane, unsigned tag, unsigned hmflag) {
  const i32x4 srdD = make_srd(p.wd, F_ * D_ * 2), srdH = make_srd(p.ws + WS_HM, 54 * 128 * 1024), srdC = make_srd(p.ws, WS_KVT);
  unsigned vd[5]; g2_wd_offsets(vd, lane);
  const unsigned vh = lane * 16;
  auto fbof = [&](int j) { const int fb = 4 * j + w; return fb < 54 ? fb : 53; };
  auto issueH = [&](int j) {
    const unsigned sb = mt * 4096u + (j < 13 ? w : (w < 2 ? w : 1)) * 131072u, lb = smb + L_HR + w * 4096;
#pragma unroll
    for (int i = 0; i < 4; ++i) dma16i_sc1(srdH, vh, sb, j * 524288 + i * 1024, lb, (j % 3) * 16384 + i * 1024);
  };
  constexpr int XB = ISP ? 1 : 5, NREG = ISP ? 9 : 7, NPRE = XB + NREG;
  u32x4 wreg[NREG][5];
  if (ISP) g2_issue_w(srdD, vd, smb, nt, w, 0);
  else BAR();
#pragma unroll
  for (int j = 0; j < NREG; ++j) {
#pragma unroll
    for (int i = 0; i < 5; ++i) ld16i<NT_WD>(wreg[j][i], srdD, vd[i], nt * 160u + w * 40960u, (j + XB) * 163840);
    if (!ISP && j == 0) {
      WAITV(5);
      flag_set(p.ws, hmflag, tag, lane);
      STAMP_AT(4);
    }
  }
  {
    const unsigned junk = smb + L_END - 256;
#pragma unroll
    for (int r = 0; r < (14 - NPRE + 3) / 4; ++r) {
      const int idx = r * 64 + lane;
      int j = NPRE + (idx >> 4); j = j < 14 ? j : 13;
      touch4(srdD, (unsigned)(fbof(j) * 32 + (mt & 3) * 8 + ((idx & 15) >> 1)) * 1280u + nt * 160u + (idx & 1) * 128u, 0, junk);
    }
  }
  if (ISP && w == 0) {
    poll_all(p.ws, CW_ST + (mt * 16 + (lane & 15)) * 16, tag);
    u32x2 sv; ld8_sc1(sv, srdC, WS_STATS + (mt * 64 + lane) * 8, 0);
    WAITV(0); PIN(sv);
    *(LAS u32x2*)(sm + L_S2 + lane * 8) = sv;
  }
  if (ISP && blockIdx.x == 255 && w == 3) {
    poll_all(p.ws, CW_ARRIVE + lane * 4, tag); poll_all(p.ws, CW_ARRIVE + 256 + lane * 4, tag);
    poll_all(p.ws, CW_ARRIVE + 512 + lane * 4, tag); poll_all(p.ws, CW_ARRIVE + 768 + lane * 4, tag);
    flag_set(p.ws, CW_EPOCH, tag, lane);
  }
  { const int j = lane < 14 ? lane : 0; poll_all(p.ws, CW_HM + (((mt >> 2) * 2 + ((mt >> 1) & 1)) * 54 + fbof(j)) * 16, tag); }
  STAMP_AT(5);
  { const int j = (lane >> 2) < 14 ? (lane >> 2) : 13;
    touch4_sc1(srdH, (unsigned)(fbof(j) * 128 + mt * 4 + (lane & 3)) * 1024u + nt * 128u, 0, smb + L_END - 256); }
  issueH(0); issueH(1); issueH(2);
  WAITV(8);
#pragma unroll
  for (int j = 0; j < NREG; ++j)
#pragma unroll
    for (int i = 0; i < 5; ++i) PIN(wreg[j][i]);
  BAR();
  STAMP_AT(6);
  unsigned zf = 0; u32x4 zr[3];
  const unsigned zoff = CW_Z + (nt * 80 + w * 20 + (lane < 20 ? lane : 0)) * 16;
  auto put_w = [&](int j) {
#pragma unroll
    for (int i = 0; i < 5; ++i) *(LAS u32x4*)(sm + L_WR + (j & 3) * G2_WSLOT + w * 5120 + i * 1024 + lane * 16) = wreg[j - XB][i];
  };
#pragma unroll
  for (int j = 0; j < 14; ++j) {
    if (j == 0) {
#pragma unroll
      for (int jj = XB; jj < 4; ++jj) put_w(jj);
    } else if (j + 3 >= XB && j + 3 < NPRE) put_w(j + 3);
    if (j == 5) ld4_sc1(zf, srdC, zoff);
    if (j == 7) {
      PIN(zf);
      if (!__all(zf == tag)) poll_all(p.ws, zoff, tag);
      const i32x4 srdZ = make_srd(p.ws + WS_ZT, D_ * S_ * 2);
#pragma unroll
      for (int r = 0; r < 3; ++r) {
        int task = r * 64 + lane; task = task < 160 ? task : 159;
        ld16_sc1(zr[r], srdZ, (unsigned)(nt * 80 + w * 20 + (task >> 3)) * 4096u + (mt * 64 + (task & 7) * 8) * 2, 0);
      }
    }
    if (j >= 1 && j + 2 <= 13) issueH(j + 2);
    if (g2_w_after<NPRE>(j)) g2_issue_w(srdD, vd, smb, nt, w, j + 3);
    if (j == 9) {
#pragma unroll
      for (int r = 0; r < 3; ++r) PIN(zr[r]);
#pragma unroll
      for (int r = 0; r < 3; ++r) { const int task = r * 64 + lane; if (task < 160) *(LAS u32x4*)(sm + L_ZS + (w * 20 + (task >> 3)) * ZS_RS + (task & 7) * 16) = zr[r]; }
    }
    if (j < 13) WAITV(g2_wait<NPRE>(j));
    BAR();
  }
}

template <int NY>
DI void g2_math(const Params& p, LAS char* sm, int w, int lane, unsigned tag, int zc, const u32x4 (&wy)[3][5]) {
  f32x4 acc[5][4];
#pragma unroll
  for (int a = 0; a < 5; ++a)
#pragma unroll
    for (int b = 0; b < 4; ++b) acc[a][b] = (f32x4){0.f, 0.f, 0.f, 0.f};
  const int kg = lane >> 4, q4 = (lane & 15) >> 2, pp = lane & 3;
  LAS char* hrd = sm + L_HR + w * 4096 + lane * 16;
  LAS char* wrd = sm + L_WR + w * 5120 + (4 * kg + q4) * 160 + pp * 8;
  bf16x8 hP[4], wP[5], hQ[4], wQ[5];
#define G2_RD(H, W, j) do { LAS char* hp_ = hrd + ((j) % 3) * 16384; LAS char* wp_ = wrd + ((j) & 3) * G2_WSLOT; \
    W[0] = ldtr(wp_, wp_ + 2560); H[0] = *(LAS bf16x8*)(hp_); H[1] = *(LAS bf16x8*)(hp_ + 1024); H[2] = *(LAS bf16x8*)(hp_ + 2048); H[3] = *(LAS bf16x8*)(hp_ + 3072); \
    _Pragma("unroll") for (int a = 1; a < 5; ++a) W[a] = ldtr(wp_ + a * 32, wp_ + a * 32 + 2560); } while (0)
#define G2_MM(H, W) do { _Pragma("unroll") for (int a = 0; a < 5; ++a) _Pragma("unroll") for (int b = 0; b < 4; ++b) \
    acc[a][b] = __builtin_amdgcn_mfma_f32_16x16x32_bf16(W[a], H[b], acc[a][b], 0, 0, 0); } while (0)
#define G2_SCHED() do { _Pragma("unroll") for (int i_ = 0; i_ < 14; ++i_) { __builtin_amdgcn_sched_group_barrier(0x8, 1, 0); __builtin_amdgcn_sched_group_barrier(0x100, 1, 0); } __builtin_amdgcn_sched_group_barrier(0x8, 6, 0); } while (0)
  if (zc >= 0) WAITV(4); else WAITV(0);
  BAR();
  STAMP_AT(9);
  G2_RD(hP, wP, 0);
  auto put_y = [&](int j) {
#pragma unroll
    for (int i = 0; i < 5; ++i) *(LAS u32x4*)(sm + L_WR + (j & 3) * G2_WSLOT + w * 5120 + i * 1024 + lane * 16) = wy[j - 2][i];
  };
  if (NY) { put_y(2); put_y(3); }
  if (zc >= 0) {
    WAITV(0);
    flag_set(p.ws, CW_Z + zc * 16, tag, lane);
  }
  BAR();
  G2_RD(hQ, wQ, 1); G2_MM(hP, wP); G2_SCHED();
  if (NY) put_y(4);
#pragma unroll
  for (int j = 2; j < 14; j += 2) {
    BAR();
    G2_RD(hP, wP, j); G2_MM(hQ, wQ); G2_SCHED();
    BAR();
    G2_RD(hQ, wQ, j + 1); G2_MM(hP, wP); G2_SCHED();
  }
  BAR();
  if (w < 2) G2_MM(hQ, wQ);
  STAMP_AT(10);
#pragma unroll
  for (int a = 0; a < 5; ++a)
#pragma unroll
    for (int b = 0; b < 4; ++b) *(LAS f32x4*)(sm + w * G2_PART + (16 * b + (lane & 15)) * G2_RS + a * 64 + kg * 16) = acc[a][b];
}
DI void g2_final_task(const Params& p, LAS char* sm, int mt, int nt, int gt, int lane, const u32x4& x) {
  const int b = gt / 10, ch = gt - b * 10, tk = b * 16 + (lane & 15);
  f32x4 a0 = (f32x4){0.f, 0.f, 0.f, 0.f}, a1 = a0;
#pragma unroll
  for (int w = 0; w < 4; ++w) { a0 += *(LAS f32x4*)(sm + w * G2_PART + tk * G2_RS + ch * 32); a1 += *(LAS f32x4*)(sm + w * G2_PART + tk * G2_RS + ch * 32 + 16); }
  LAS char* zp = sm + L_ZS + (ch * 8 + ((lane & 15) >> 2)) * ZS_RS + b * 32 + (lane & 3) * 8;
  const u32x2 z0 = __builtin_bit_cast(u32x2, __builtin_amdgcn_ds_read_tr16_b64_v4i16((LAS s16x4*)zp));
  const u32x2 z1 = __builtin_bit_cast(u32x2, __builtin_amdgcn_ds_read_tr16_b64_v4i16((LAS s16x4*)(zp + 4 * ZS_RS)));
  const f32x2 st = *(LAS f32x2*)(sm + L_S2 + tk * 8);
  const float rs = st.y, nmr = -st.x * rs;
  float o[8];
#pragma unroll
  for (int e = 0; e < 8; ++e) {
    const float xv = (e & 1) ? bhi(x[e >> 1]) : blo(x[e >> 1]);
    const unsigned zw = e < 4 ? z0[(e & 3) >> 1] : z1[(e & 3) >> 1];
    const float z = (e & 1) ? bhi(zw) : blo(zw);
    const float xn = fmaf(xv, rs, nmr);
    o[e] = fmaf(xn, z, xv) + (e < 4 ? a0[e] : a1[e - 4]);
  }
  const u32x4 r = {pk2(o[0], o[1]), pk2(o[2], o[3]), pk2(o[4], o[5]), pk2(o[6], o[7])};
  *(GAS u32x4*)(p.out + (mt * 64 + tk) * 640 + nt * 80 + ch * 8) = r;
}
DI u32x4 final_x(const Params& p, int mt, int nt, int gt, int lane) {
  const int b = gt / 10, ch = gt - b * 10;
  return *(const GAS u32x4*)(p.q + (mt * 64 + b * 16 + (lane & 15)) * 640 + nt * 80 + ch * 8);
}

DI void pair_send(LAS char* pb, LAS unsigned* pf, unsigned tag, int lane, const f32x4 (&acc)[8]) {
#pragma unroll
  for (int g = 0; g < 8; ++g) *(LAS f32x4*)(pb + g * 1024 + lane * 16) = acc[g];
  asm volatile("s_waitcnt lgkmcnt(0)" ::: "memory");
  if (lane == 0) *(volatile LAS unsigned*)pf = tag;
}
DI void pair_recv(LAS char* pb, LAS unsigned* pf, unsigned tag, int lane, f32x4 (&acc)[8]) {
#pragma unroll 1
  for (unsigned it = 0; it < (1u << 22); ++it) { if (*(volatile LAS unsigned*)pf == tag) break; __builtin_amdgcn_s_sleep(1); }
  asm volatile("" ::: "memory");
#pragma unroll
  for (int g = 0; g < 8; ++g) acc[g] += *(LAS f32x4*)(pb + g * 1024 + lane * 16);
}

__global__ __launch_bounds__(512, 2) void fused_kernel(Params p_arg) {
  unsigned long long pc0; asm volatile("s_getpc_b64 %0" : "=s"(pc0));
  const KP kp = (KP)__builtin_amdgcn_kernarg_segment_ptr();
  Params p = reload(kp);
  extern __shared__ __attribute__((aligned(16))) char smem_raw[];
  LAS char* sm = (LAS char*)smem_raw;
  const unsigned smb = (unsigned)(size_t)sm;
  const int lane = threadIdx.x & 63, wid = __builtin_amdgcn_readfirstlane(threadIdx.x >> 6);
  const int b = blockIdx.x, x = b & 7, i = b >> 3;
  const int mt = x * 4 + (i & 3), nt = i >> 2;
  STAMP_INIT();
  STAMP_AT(0);
  if (i < 27) {
    const int mm = 27 * (x & 3) + i, ft = mm >> 2, rb = 4 * (x >> 2) + (mm & 3);
    if (wid < 4) {
      const unsigned ep = __hip_atomic_load((gu32*)(p.ws + CW_EPOCH), __ATOMIC_RELAXED, __HIP_MEMORY_SCOPE_AGENT);
      unsigned taskflag = 0;
      const int id = wid < 2 ? 2 * b + wid : 432 + b;
      if (wid < 3 && id < 512) { task_stats<true>(p, id, lane); taskflag = CW_ST + id * 16; }
      else BAR();
      const unsigned tag = __builtin_amdgcn_readfirstlane(ep) + 1u;
      asm volatile("" :: "s"(tag) : "memory");
      STAMP_AT(13);
      g1_math(p, sm, rb, ft, wid, lane, tag, taskflag);
      p = reload(kp);
      g2_loader<false>(p, sm, smb, mt, nt, wid, lane, tag, CW_HM + ((rb * 2 + (wid >> 1)) * 54 + ft * 2 + (wid & 1)) * 16);
    } else {
      const int hw = wid - 4;
      const bool conv = 2 * b < NCH_G;
      const int cch = 2 * b + (hw >> 1);
      unsigned tag, dvb = 0; int zc = -1;
      if (conv) {
        u32x4 wy[3][5];
        g1_loader<true>(kp, p, pc0, sm, smb, rb, ft, mt, nt, hw, lane, tag, cch, dvb, wy);
        p = reload(kp);
        BAR();
        STAMP_AT(7);
        LAS char* cb = sm + L_CVG + (hw >> 1) * CV_SZ;
        LAS char* pb = sm + L_CP + (hw >> 1) * 8192;
        LAS unsigned* pf = (LAS unsigned*)(sm + L_PF + (hw >> 1) * 4);
        f32x4 acc[8];
        conv_mfma<4>(cb, (hw & 1) * 4, lane, acc);
        asm volatile("" :: "v"(acc[0]), "v"(acc[7]));
        STAMP_AT(4);
        if (hw & 1) pair_send(pb, pf, tag, lane, acc);
        else { pair_recv(pb, pf, tag, lane, acc); conv_store(p, cb, cch, lane, dvb, acc); zc = cch; }
        STAMP_AT(8);
        g2_math<3>(p, sm, hw, lane, tag, zc, wy);
      } else {
        u32x4 wy[3][5];
        g1_loader<false>(kp, p, pc0, sm, smb, rb, ft, mt, nt, hw, lane, tag, cch, dvb, wy);
        p = reload(kp);
        BAR();
        g2_math<3>(p, sm, hw, lane, tag, zc, wy);
      }
    }
  } else {
    const int cg = i - 27, pb = x * 5 + cg;
    const unsigned ep = __hip_atomic_load((gu32*)(p.ws + CW_EPOCH), __ATOMIC_RELAXED, __HIP_MEMORY_SCOPE_AGENT);
    const int hw = wid - 4, c0 = NCH_G + pb * NCH_P;
    if (wid < 4) {
      task_kv<0>(p, 2 * cg + (wid & 1), 4 * x + (wid >> 1), lane, [] {});
      const unsigned tag = __builtin_amdgcn_readfirstlane(ep) + 1u;
      asm volatile("" :: "s"(tag) : "memory");
      WAITV(0);
      flag_set(p.ws, CW_KV + ((2 * cg + (wid & 1)) * 32 + 4 * x + (wid >> 1)) * 16, tag, lane);
      BAR();
      if (wid == 0) flag_set(p.ws, CW_ARRIVE + b * 4, tag, lane);
      STAMP_AT(1);
      BAR();
      p = reload(kp);
      touchg(p.wd, (unsigned)((pb * 4 + wid) * 64 + lane) * 128u, smb + L_END - 256);
      if (pb * 4 + wid + 160 < 270) touchg(p.wd, (unsigned)((pb * 4 + wid + 160) * 64 + lane) * 128u, smb + L_END - 256);
      g2_loader<true>(p, sm, smb, mt, nt, wid, lane, tag, 0);
    } else {
      LAS char* cb = sm + L_CVP + hw * CV_SZ;
      const int cA = c0 + hw, cB = c0 + 4 + (hw >> 1);
      ConvIn in, in2;
      task_kv<13>(p, 2 * cg + (wid & 1), 4 * x + (wid >> 1), lane, [&] { conv_load_h(p, in, cA, lane); });
      conv_load_h(p, in2, cB, lane);
      const unsigned tag = __builtin_amdgcn_readfirstlane(ep) + 1u;
      asm volatile("" :: "s"(tag) : "memory");
      if ((hw & 1) == 0 && lane == 0) *(LAS unsigned*)(sm + L_PFP + (hw >> 1) * 4) = ~tag;
      WAITV(13);
      flag_set(p.ws, CW_KV + ((2 * cg + (wid & 1)) * 32 + 4 * x + (wid >> 1)) * 16, tag, lane);
      BAR();
      STAMP_AT(1);
      poll_all(p.ws, CW_KV + (((lane < 32 ? cA : cB) >> 6) * 32 + (lane & 31)) * 16, tag);
      conv_load_u(p, in, cA, lane);
      conv_load_u(p, in2, cB, lane);
      BAR();
      WAITV(4);
      conv_pin(in);
      conv_build(in, cb, lane);
      unsigned dvb = in.dv;
      STAMP_AT(2); STAMP_CY(14);
      f32x4 acc[8];
      conv_mfma<8>(cb, 0, lane, acc);
      asm volatile("" :: "v"(acc[0]), "v"(acc[7]));
      STAMP_AT(4); STAMP_CY(15);
      conv_store(p, cb, cA, lane, dvb, acc);
      STAMP_AT(3);
      WAITV(4);
      conv_pin(in2);
      conv_build(in2, cb, lane);
      dvb = in2.dv;
      STAMP_AT(7);
      conv_mfma<4>(cb, (hw & 1) * 4, lane, acc);
      WAITV(0);
      flag_set(p.ws, CW_Z + cA * 16, tag, lane);
      LAS unsigned* pf = (LAS unsigned*)(sm + L_PFP + (hw >> 1) * 4);
      int zc = -1;
      if (hw & 1) pair_send(cb, pf, tag, lane, acc);
      else { pair_recv(cb + CV_SZ, pf, tag, lane, acc); conv_store(p, cb, cB, lane, dvb, acc); zc = cB; }
      STAMP_AT(8);
      u32x4 wy[3][5];
      g2_math<0>(p, sm, hw, lane, tag, zc, wy);
    }
  }
  p = reload(kp);
  const int gt0 = wid * 4 + (lane >> 4), gt1 = 32 + gt0;
  const u32x4 x0 = final_x(p, mt, nt, gt0, lane);
  u32x4 x1 = x0;
  if (wid < 2) x1 = final_x(p, mt, nt, gt1, lane);
  BAR();
  g2_final_task(p, sm, mt, nt, gt0, lane, x0);
  if (wid < 2) g2_final_task(p, sm, mt, nt, gt1, lane, x1);
  STAMP_AT(11);
  STAMP_FLUSH();
}

extern "C" void kernel_launch(void* const* d_in, const int* in_sizes, int n_in, void* d_out, int out_size, void* d_ws, size_t ws_size, hipStream_t stream) {
  Params p;
  p.q = (const u16*)d_in[0]; p.k = (const u16*)d_in[1]; p.v = (const u16*)d_in[2]; p.sk = (const u16*)d_in[3]; p.h = (const u16*)d_in[4]; p.dv = (const u16*)d_in[5];
  p.wg = (const u16*)d_in[7]; p.wu = (const u16*)d_in[8]; p.wd = (const u16*)d_in[9]; p.out = (u16*)d_out; p.ws = (char*)d_ws;
  (void)hipFuncSetAttribute((const void*)fused_kernel, hipFuncAttributeMaxDynamicSharedMemorySize, L_ALLOC);
  fused_kernel<<<256, 512, L_ALLOC, stream>>>(p);
}
#define HAVE_INTER 1
#define HM_K16 1
```

```cpp
#define STAMP 1
#define DIAG_B 100
#define DIAG_W 0
#define DIAG_I 13
#include <hip/hip_runtime.h>
#include <stdint.h>
typedef unsigned short u16;
typedef __bf16 bf16x8 __attribute__((ext_vector_type(8)));
typedef __bf16 bf16x2 __attribute__((ext_vector_type(2)));
typedef short s16x4 __attribute__((ext_vector_type(4)));
typedef float f32x2 __attribute__((ext_vector_type(2)));
typedef float f32x4 __attribute__((ext_vector_type(4)));
typedef unsigned u32x2 __attribute__((ext_vector_type(2)));
typedef unsigned u32x4 __attribute__((ext_vector_type(4)));
typedef int i32x4 __attribute__((ext_vector_type(4)));
#define LAS __attribute__((address_space(3)))
#define GAS __attribute__((address_space(1)))
#define DI __device__ __forceinline__
typedef GAS unsigned gu32;

#define TOUCH 1
#define TOUCH_CODE 0
#define CODE_BACK 0
#define NT_G1 0
#define NT_KV 1
#define NT_H 1
#define NT_WD 0
#define TLBW 0
constexpr int S_ = 2048, D_ = 640, F_ = 1728;
constexpr unsigned CW_EPOCH = 0;
constexpr unsigned CW_ARRIVE = 1024;
constexpr unsigned CW_KV = 4096;
constexpr unsigned CW_ST = 16384;
constexpr unsigned CW_HM = 24576;
constexpr unsigned CW_Z = 40960;
constexpr unsigned WS_STATS = 65536;
constexpr unsigned WS_KVT = 131072;
constexpr unsigned WS_ZT = WS_KVT + D_ * S_ * 2;
constexpr unsigned WS_HM = WS_ZT + D_ * S_ * 2;
constexpr unsigned WS_END = WS_HM + 54 * 128 * 1024;
constexpr unsigned WS_DBG = 32u << 20;
constexpr int G1_SLOT = 49152;
constexpr int L_CS = 153856, L_ST = L_CS + 512, L_S2 = L_ST + 2048, L_END = L_S2 + 512 + 64 + 256;
constexpr int L_DBG = L_END, L_ALLOC = L_END + 2048;
constexpr int L_HR = 0, L_WR = 49152, G2_WSLOT = 20480;
constexpr int L_CVG = 90112, L_CVP = 69632;
constexpr int L_ZS = 131072, ZS_RS = 144;
constexpr int L_CP = L_CVG + 2 * 21216, L_PF = L_CS - 64;
constexpr int L_PFP = L_S2 + 512 - 0;
constexpr int G2_RS = 336, G2_PART = 64 * G2_RS;
constexpr int CV_H = 4160, CV_U = 4 * CV_H, CV_SZ = CV_U + 4576;
static_assert(L_ZS + 80 * ZS_RS <= L_PF, "lds");
static_assert(L_CP + 2 * 8192 <= L_PF, "lds");
static_assert(L_CVP + 4 * CV_SZ + 64 <= L_S2, "lds");
constexpr int NCH_P = 6;
constexpr int NCH_G = D_ - 40 * NCH_P;
static_assert(NCH_G <= 2 * 216 && NCH_G % 2 == 0, "conv split");
constexpr unsigned SPIN_MAX = 1u << 16;

struct Params {
  const u16 *q, *k, *v, *sk, *h, *dv, *wg, *wu, *wd;
  u16* out; char* ws;
};

typedef const __attribute__((address_space(4))) Params* KP;
DI Params reload(KP kp) {
  asm volatile("" : "+s"(kp));
  Params r; r.q = kp->q; r.k = kp->k; r.v = kp->v; r.sk = kp->sk; r.h = kp->h; r.dv = kp->dv; r.wg = kp->wg; r.wu = kp->wu; r.wd = kp->wd; r.out = kp->out; r.ws = kp->ws;
  return r;
}
DI i32x4 make_srd(const void* p, unsigned bytes) {
  i32x4 r; unsigned long long a = (unsigned long long)p;
  r.x = (int)(unsigned)a;
  r.y = (int)((unsigned)(a >> 32) & 0xffffu);
  r.z = (int)bytes; r.w = 0x00020000; return r;
}
DI void dma16(const i32x4& srd, unsigned voff, unsigned soff, unsigned lds) {
  asm volatile("s_mov_b32 m0, %3\n\ts_nop 0\n\tbuffer_load_dwordx4 %0, %1, %2 offen lds" :: "v"(voff), "s"(srd), "s"(soff), "s"(lds) : "memory");
}
DI void dma16_sc1(const i32x4& srd, unsigned voff, unsigned soff, unsigned lds) {
  asm volatile("s_mov_b32 m0, %3\n\ts_nop 0\n\tbuffer_load_dwordx4 %0, %1, %2 offen sc1 lds" :: "v"(voff), "s"(srd), "s"(soff), "s"(lds) : "memory");
}
template <int NT = 0>
DI void dma16i(const i32x4& srd, unsigned voff, unsigned sbase, int simm, unsigned lbase, int limm) {
  unsigned t;
  if (NT) asm volatile("s_add_u32 m0, %4, %6\n\ts_add_u32 %0, %3, %5\n\tbuffer_load_dwordx4 %1, %2, %0 offen nt lds" : "=&s"(t) : "v"(voff), "s"(srd), "s"(sbase), "s"(lbase), "i"(simm), "i"(limm) : "memory", "scc");
  else asm volatile("s_add_u32 m0, %4, %6\n\ts_add_u32 %0, %3, %5\n\tbuffer_load_dwordx4 %1, %2, %0 offen lds" : "=&s"(t) : "v"(voff), "s"(srd), "s"(sbase), "s"(lbase), "i"(simm), "i"(limm) : "memory", "scc");
}
DI void dma16i_sc1(const i32x4& srd, unsigned voff, unsigned sbase, int simm, unsigned lbase, int limm) {
  unsigned t;
  asm volatile("s_add_u32 m0, %4, %6\n\ts_add_u32 %0, %3, %5\n\tbuffer_load_dwordx4 %1, %2, %0 offen sc1 lds" : "=&s"(t) : "v"(voff), "s"(srd), "s"(sbase), "s"(lbase), "i"(simm), "i"(limm) : "memory", "scc");
}
template <int NT = 0>
DI void ld16i(u32x4& d, const i32x4& srd, unsigned voff, unsigned sbase, int simm) {
  unsigned t;
  if (NT) asm volatile("s_add_u32 %1, %4, %5\n\tbuffer_load_dwordx4 %0, %2, %3, %1 offen nt" : "=v"(d), "=&s"(t) : "v"(voff), "s"(srd), "s"(sbase), "i"(simm) : "memory", "scc");
  else asm volatile("s_add_u32 %1, %4, %5\n\tbuffer_load_dwordx4 %0, %2, %3, %1 offen" : "=v"(d), "=&s"(t) : "v"(voff), "s"(srd), "s"(sbase), "i"(simm) : "memory", "scc");
}
template <int NT = 0>
DI void ld16(u32x4& d, const i32x4& srd, unsigned voff, unsigned soff) {
  if (NT) asm volatile("buffer_load_dwordx4 %0, %1, %2, %3 offen nt" : "=v"(d) : "v"(voff), "s"(srd), "s"(soff) : "memory");
  else asm volatile("buffer_load_dwordx4 %0, %1, %2, %3 offen" : "=v"(d) : "v"(voff), "s"(srd), "s"(soff) : "memory");
}
DI void ld16_sc1(u32x4& d, const i32x4& srd, unsigned voff, unsigned soff) { asm volatile("buffer_load_dwordx4 %0, %1, %2, %3 offen sc1" : "=v"(d) : "v"(voff), "s"(srd), "s"(soff) : "memory"); }
DI void ld8_sc1(u32x2& d, const i32x4& srd, unsigned voff, unsigned soff) { asm volatile("buffer_load_dwordx2 %0, %1, %2, %3 offen sc1" : "=v"(d) : "v"(voff), "s"(srd), "s"(soff) : "memory"); }
DI void ld8(u32x2& d, const i32x4& srd, unsigned voff, unsigned soff) { asm volatile("buffer_load_dwordx2 %0, %1, %2, %3 offen" : "=v"(d) : "v"(voff), "s"(srd), "s"(soff) : "memory"); }
DI void ld4_sc1(unsigned& d, const i32x4& srd, unsigned voff) { asm volatile("buffer_load_dword %0, %1, %2, 0 offen sc1" : "=v"(d) : "v"(voff), "s"(srd) : "memory"); }
DI void touchg(const void* base, unsigned voff, unsigned lds) {
  asm volatile("s_mov_b32 m0, %2\n\ts_nop 0\n\tglobal_load_lds_dword %0, %1" :: "v"(voff), "s"(base), "s"(lds) : "memory");
}
DI void touchg_nt(const void* base, unsigned voff, unsigned lds) {
  asm volatile("s_mov_b32 m0, %2\n\ts_nop 0\n\tglobal_load_lds_dword %0, %1 nt" :: "v"(voff), "s"(base), "s"(lds) : "memory");
}
DI void touch4(const i32x4& srd, unsigned voff, unsigned soff, unsigned lds) {
  asm volatile("s_mov_b32 m0, %3\n\ts_nop 0\n\tbuffer_load_dword %0, %1, %2 offen lds" :: "v"(voff), "s"(srd), "s"(soff), "s"(lds) : "memory");
}
DI void touch4_sc1(const i32x4& srd, unsigned voff, unsigned soff, unsigned lds) {
  asm volatile("s_mov_b32 m0, %3\n\ts_nop 0\n\tbuffer_load_dword %0, %1, %2 offen sc1 lds" :: "v"(voff), "s"(srd), "s"(soff), "s"(lds) : "memory");
}
#define WAITV(n) asm volatile("s_waitcnt vmcnt(%0)" :: "i"(n) : "memory")
#define BAR() asm volatile("s_waitcnt lgkmcnt(0)\n\ts_barrier" ::: "memory")
#define PIN(x) asm volatile("" : "+v"(x))
DI float b2f(u16 x) { return __uint_as_float(((unsigned)x) << 16); }
DI float blo(unsigned x) { return __uint_as_float(x << 16); }
DI float bhi(unsigned x) { return __uint_as_float(x & 0xffff0000u); }
DI unsigned pk2(float lo, float hi) { bf16x2 r = {(__bf16)lo, (__bf16)hi}; return __builtin_bit_cast(unsigned, r); }
DI float rbf(float x) { return (float)(__bf16)x; }
DI float dot2(unsigned a, unsigned b, float c) { return __builtin_amdgcn_fdot2_f32_bf16(__builtin_bit_cast(bf16x2, a), __builtin_bit_cast(bf16x2, b), c, false); }
template <int CTRL> DI float dpp_add(float x) { return x + __int_as_float(__builtin_amdgcn_update_dpp(0, __float_as_int(x), CTRL, 0xf, 0xf, true)); }
DI float row16_sum(float x) { x = dpp_add<0x128>(x); x = dpp_add<0x124>(x); x = dpp_add<0x122>(x); x = dpp_add<0x121>(x); return x; }
DI bf16x8 ldtr(LAS char* p0, LAS char* p1) {
  s16x4 a = __builtin_amdgcn_ds_read_tr16_b64_v4i16((LAS s16x4*)p0), b = __builtin_amdgcn_ds_read_tr16_b64_v4i16((LAS s16x4*)p1);
  return __builtin_bit_cast(bf16x8, __builtin_shufflevector(a, b, 0, 1, 2, 3, 4, 5, 6, 7));
}
DI void poll_all(const char* ws, unsigned off, unsigned tag) {
  gu32* a = (gu32*)(ws + off);
#pragma unroll 1
  for (unsigned it = 0; it < SPIN_MAX; ++it) {
    const unsigned v = __hip_atomic_load(a, __ATOMIC_RELAXED, __HIP_MEMORY_SCOPE_AGENT);
    if (__all(v == tag)) break;
    __builtin_amdgcn_s_sleep(2);
  }
  asm volatile("" ::: "memory");
}
DI void flag_set(const char* ws, unsigned off, unsigned tag, int lane) {
  if (lane == 0) __hip_atomic_store((gu32*)(ws + off), tag, __ATOMIC_RELAXED, __HIP_MEMORY_SCOPE_AGENT);
}
extern __shared__ __attribute__((aligned(16))) char smem_dbg[];
#define STAMP_AT(idx) do { const unsigned long long t_ = __builtin_amdgcn_s_memrealtime(), c_ = __builtin_amdgcn_s_memtime(); \
    if (lane == 0) *(LAS u32x4*)((LAS char*)smem_dbg + L_DBG + ((threadIdx.x >> 6) * 16 + (idx)) * 16) = (u32x4){(unsigned)t_, (unsigned)(t_ >> 32), (unsigned)c_, (unsigned)(c_ >> 32)}; } while (0)
#define STAMP_INIT() do { if (lane < 16) *(LAS u32x4*)((LAS char*)smem_dbg + L_DBG + ((threadIdx.x >> 6) * 16 + lane) * 16) = (u32x4){0u, 0u, 0u, 0u}; } while (0)
#define STAMP_FLUSH() do { if (lane < 16) { const u32x4 v_ = *(LAS u32x4*)((LAS char*)smem_dbg + L_DBG + ((threadIdx.x >> 6) * 16 + lane) * 16); \
    ((u32x2*)(p.ws + WS_DBG))[(blockIdx.x * 8 + (threadIdx.x >> 6)) * 16 + lane] = (u32x2){v_.x, v_.y}; \
    ((u32x2*)(p.ws + WS_DBG + (8 << 20)))[(blockIdx.x * 8 + (threadIdx.x >> 6)) * 16 + lane] = (u32x2){v_.z, v_.w}; } } while (0)
#define STAMP_CY(idx) do {} while (0)

template <int NX, class Hook>
DI void task_kv(const Params& p, int cg, int tb, int lane, Hook hook) {
  const int c0 = cg * 64 + 8 * (lane & 7), t0 = tb * 64 + 8 * (lane >> 3);
  const i32x4 rk = make_srd(p.k, S_ * D_ * 2), rv = make_srd(p.v, S_ * D_ * 2), rsk = make_srd(p.sk, D_ * 8);
  u32x4 kk[11], vv[8], ss[4];
#pragma unroll
  for (int j = 0; j < 4; ++j) ld16(ss[j], rsk, (unsigned)(c0 * 8), j * 16);
#pragma unroll
  for (int j = 0; j < 11; ++j) { const int t = t0 - 3 + j; ld16<NT_KV>(kk[j], rk, t >= 0 ? (unsigned)(t * 1280 + c0 * 2) : 0xfffffff0u, 0); }
#pragma unroll
  for (int j = 0; j < 8; ++j) ld16<NT_KV>(vv[j], rv, (unsigned)(t0 * 1280 + c0 * 2), j * 1280);
  hook();
  const __amdgpu_buffer_rsrc_t ro = __builtin_amdgcn_make_buffer_rsrc((void*)(p.ws + WS_KVT), 0, D_ * S_ * 2, 0x00020000);
  WAITV(NX);
  STAMP_AT(12);
#pragma unroll
  for (int j = 0; j < 4; ++j) PIN(ss[j]);
#pragma unroll
  for (int j = 0; j < 11; ++j) PIN(kk[j]);
#pragma unroll
  for (int j = 0; j < 8; ++j) PIN(vv[j]);
#pragma unroll
  for (int cp = 0; cp < 4; ++cp) {
    const f32x2 s0 = {blo(ss[cp].x), blo(ss[cp].z)}, s1 = {bhi(ss[cp].x), bhi(ss[cp].z)}, s2 = {blo(ss[cp].y), blo(ss[cp].w)}, s3 = {bhi(ss[cp].y), bhi(ss[cp].w)};
    f32x2 kx[11];
#pragma unroll
    for (int j = 0; j < 11; ++j) { const unsigned w = kk[j][cp]; kx[j] = (f32x2){blo(w), bhi(w)}; }
    f32x2 o[8];
    __builtin_amdgcn_sched_barrier(0);
#pragma unroll
    for (int j = 0; j < 8; ++j) o[j] = kx[j] * s3;
    __builtin_amdgcn_sched_barrier(0);
#pragma unroll
    for (int j = 0; j < 8; ++j) o[j] = kx[j + 1] * s2 + o[j];
    __builtin_amdgcn_sched_barrier(0);
#pragma unroll
    for (int j = 0; j < 8; ++j) o[j] = kx[j + 2] * s1 + o[j];
    __builtin_amdgcn_sched_barrier(0);
#pragma unroll
    for (int j = 0; j < 8; ++j) o[j] = kx[j + 3] * s0 + o[j];
    __builtin_amdgcn_sched_barrier(0);
    f32x2 vx[8];
#pragma unroll
    for (int j = 0; j < 8; ++j) { const unsigned w = vv[j][cp]; vx[j] = (f32x2){blo(w), bhi(w)}; }
    __builtin_amdgcn_sched_barrier(0);
#pragma unroll
    for (int j = 0; j < 8; ++j) o[j] = o[j] * vx[j];
    __builtin_amdgcn_sched_barrier(0);
    const u32x4 r0 = {pk2(o[7].x, o[6].x), pk2(o[5].x, o[4].x), pk2(o[3].x, o[2].x), pk2(o[1].x, o[0].x)};
    const u32x4 r1 = {pk2(o[7].y, o[6].y), pk2(o[5].y, o[4].y), pk2(o[3].y, o[2].y), pk2(o[1].y, o[0].y)};
    __builtin_amdgcn_raw_buffer_store_b128(r0, ro, (unsigned)((c0 + 2 * cp) * 4096 + (2040 - t0) * 2), 0, 16);
    __builtin_amdgcn_raw_buffer_store_b128(r1, ro, (unsigned)((c0 + 2 * cp + 1) * 4096 + (2040 - t0) * 2), 0, 16);
  }
}
template <bool PRE>
DI void task_stats(const Params& p, int id, int lane) {
  const i32x4 rq = make_srd(p.q, S_ * D_ * 2);
  const int row = id * 4 + (lane >> 4), l16 = lane & 15;
  u32x4 x[5];
#pragma unroll
  for (int j = 0; j < 5; ++j) ld16<NT_G1>(x[j], rq, (unsigned)row * 1280u + l16 * 16, j * 256);
  const __amdgpu_buffer_rsrc_t rs = __builtin_amdgcn_make_buffer_rsrc((void*)(p.ws + WS_STATS), 0, S_ * 8, 0x00020000);
  if (PRE) BAR();
  WAITV(0);
  STAMP_AT(12);
#pragma unroll
  for (int j = 0; j < 5; ++j) PIN(x[j]);
  float s4[4] = {0.f, 0.f, 0.f, 0.f};
#pragma unroll
  for (int j = 0; j < 5; ++j)
#pragma unroll
    for (int e = 0; e < 4; ++e) s4[e] = dot2(x[j][e], 0x3f803f80u, s4[e]);
  const float mean = row16_sum((s4[0] + s4[1]) + (s4[2] + s4[3])) * (1.0f / 640.0f);
  f32x2 q2[2] = {{0.f, 0.f}, {0.f, 0.f}};
#pragma unroll
  for (int j = 0; j < 5; ++j)
#pragma unroll
    for (int e = 0; e < 4; ++e) { const unsigned w = x[j][e]; const f32x2 d = (f32x2){blo(w), bhi(w)} - mean; q2[e & 1] = d * d + q2[e & 1]; }
  const float var = row16_sum((q2[0].x + q2[0].y) + (q2[1].x + q2[1].y)) * (1.0f / 640.0f);
  const float mb = rbf(mean), sb = rbf(__builtin_amdgcn_sqrtf(rbf(rbf(var) + 1e-5f)));
  u32x2 o; o.x = __float_as_uint(mb); o.y = __float_as_uint(1.0f / sb);
  __builtin_amdgcn_raw_buffer_store_b64(o, rs, l16 == 0 ? (unsigned)row * 8u : 0xfffffff0u, 0, 16);
}

struct ConvIn { u32x4 L0[4], L1[4], L2[4], U[4]; unsigned dv; };
DI void conv_load_h(const Params& p, ConvIn& in, int c, int lane) {
  const i32x4 rh = make_srd(p.h, D_ * S_ * 2), rd = make_srd(p.dv, D_ * 2);
  const unsigned vo = lane * 16, so = c * 4096u;
  asm volatile("buffer_load_ushort %0, %1, %2, 0 offen" : "=v"(in.dv) : "v"(c * 2u), "s"(rd) : "memory");
#pragma unroll
  for (int i = 0; i < 4; ++i) { ld16<NT_H>(in.L0[i], rh, vo, so + i * 1024); ld16(in.L1[i], rh, vo + 4, so + i * 1024); ld16(in.L2[i], rh, vo + 8, so + i * 1024); }
}
DI void conv_load_u(const Params& p, ConvIn& in, int c, int lane) {
  const i32x4 ru = make_srd(p.ws + WS_KVT, D_ * S_ * 2);
#pragma unroll
  for (int i = 0; i < 4; ++i) ld16_sc1(in.U[i], ru, lane * 16, c * 4096u + i * 1024);
}
DI void conv_pin(ConvIn& in) {
#pragma unroll
  for (int i = 0; i < 4; ++i) { PIN(in.L0[i]); PIN(in.L1[i]); PIN(in.L2[i]); PIN(in.U[i]); }
  PIN(in.dv);
}
DI void conv_build(const ConvIn& in, LAS char* cb, int lane) {
#pragma unroll
  for (int i = 0; i < 4; ++i) {
    const unsigned o = (i * 64 + lane + 2) * 16;
    u32x4 c0, c2;
#pragma unroll
    for (int e = 0; e < 4; ++e) { c0[e] = __builtin_amdgcn_alignbit(in.L1[i][e], in.L0[i][e], 16); c2[e] = __builtin_amdgcn_alignbit(in.L2[i][e], in.L1[i][e], 16); }
    *(LAS u32x4*)(cb + 0 * CV_H + o) = c0;
    *(LAS u32x4*)(cb + 1 * CV_H + o) = in.L1[i];
    *(LAS u32x4*)(cb + 2 * CV_H + o) = c2;
    *(LAS u32x4*)(cb + 3 * CV_H + o) = in.L2[i];
    *(LAS u32x4*)(cb + CV_U + (i * 64 + lane) * 16) = in.U[i];
  }
  const u32x4 z4 = {0u, 0u, 0u, 0u};
  if (lane < 30) *(LAS u32x4*)(cb + CV_U + 4096 + lane * 16) = z4;
  if (lane == 0) {
    const unsigned w0 = in.L0[0].x, w1 = in.L0[0].y;
#pragma unroll
    for (int cc = 0; cc < 4; ++cc) *(LAS u32x4*)(cb + cc * CV_H) = z4;
    *(LAS u32x4*)(cb + 0 * CV_H + 16) = (u32x4){0u, 0u, 0u, w0 << 16};
    *(LAS u32x4*)(cb + 1 * CV_H + 16) = (u32x4){0u, 0u, 0u, w0};
    *(LAS u32x4*)(cb + 2 * CV_H + 16) = (u32x4){0u, 0u, w0 << 16, __builtin_amdgcn_alignbit(w1, w0, 16)};
    *(LAS u32x4*)(cb + 3 * CV_H + 16) = (u32x4){0u, 0u, w0, w1};
  }
}
template <int TRIPS>
DI void conv_mfma(LAS char* cb, int d0, int lane, f32x4 (&acc)[8]) {
  const int r = lane & 15, kg = lane >> 4, kap = kg >> 1, bp = kg & 1;
  unsigned a0 = (unsigned)(size_t)cb + (r & 3) * CV_H + 32 * kap + 16 * bp + 2 * (r & 12) + d0 * 64;
  unsigned a1 = a0 + 8;
  unsigned ub = (unsigned)(size_t)cb + CV_U + (4064 - 3584) - 32 * r + 32 * kap + 16 * bp + d0 * 64;
  PIN(a0); PIN(a1); PIN(ub);
#pragma unroll
  for (int g = 0; g < 8; ++g) acc[g] = (f32x4){0.f, 0.f, 0.f, 0.f};
  bf16x8 A[8], B[8];
#define CV_LDA(g) do { const s16x4 x0_ = *(LAS s16x4*)(size_t)(a0 + (g) * 512), x1_ = *(LAS s16x4*)(size_t)(a1 + (g) * 512); \
    A[g] = __builtin_bit_cast(bf16x8, __builtin_shufflevector(x0_, x1_, 0, 1, 2, 3, 4, 5, 6, 7)); } while (0)
#define CV_LDB(g) do { B[g] = *(LAS bf16x8*)(size_t)(ub + (7 - (g)) * 512); } while (0)
#pragma unroll
  for (int g = 0; g < 8; ++g) { CV_LDA(g); CV_LDB(7 - g); }
#pragma unroll 1
  for (int d = 0; d < TRIPS; ++d) {
    a0 += 64; a1 += 64; ub += 64; PIN(a0); PIN(a1); PIN(ub);
#pragma unroll
    for (int gm = 0; gm < 8; ++gm) {
#pragma unroll
      for (int gp = 7 - gm; gp >= 0; --gp) acc[gm + gp] = __builtin_amdgcn_mfma_f32_16x16x32_bf16(A[gm], B[gp], acc[gm + gp], 0, 0, 0);
      __builtin_amdgcn_sched_barrier(0);
      CV_LDA(gm); CV_LDB(7 - gm);
      __builtin_amdgcn_sched_barrier(0);
    }
  }
}
DI void conv_store(const Params& p, LAS char* cb, int c, int lane, unsigned dvb, const f32x4 (&acc)[8]) {
  const int r = lane & 15, kg = lane >> 4;
  const float dv = __uint_as_float(dvb << 16);
  const __amdgpu_buffer_rsrc_t rz = __builtin_amdgcn_make_buffer_rsrc((void*)(p.ws + WS_ZT), 0, D_ * S_ * 2, 0x00020000);
  u32x2 z[8];
#pragma unroll
  for (int g = 0; g < 8; ++g) {
    const int t = 256 * g + 16 * r + 4 * kg;
    const u32x2 kv = *(LAS u32x2*)(cb + CV_U + (2044 - t) * 2);
    const float y0 = fmaf(bhi(kv.y), dv, acc[g][0]), y1 = fmaf(blo(kv.y), dv, acc[g][1]), y2 = fmaf(bhi(kv.x), dv, acc[g][2]), y3 = fmaf(blo(kv.x), dv, acc[g][3]);
    z[g].x = pk2(y0, y1); z[g].y = pk2(y2, y3);
  }
#pragma unroll
  for (int g = 0; g < 8; g += 2) {
    const auto s0 = __builtin_amdgcn_permlane16_swap(z[g].x, z[g + 1].x, false, false);
    const auto s1 = __builtin_amdgcn_permlane16_swap(z[g].y, z[g + 1].y, false, false);
    const u32x4 o = {s0[0], s1[0], s0[1], s1[1]};
    const int gg = g + (kg & 1);
    __builtin_amdgcn_raw_buffer_store_b128(o, rz, c * 4096u + (256 * gg + 16 * r + 4 * (kg & 2)) * 2, 0, 16);
  }
}
struct ConvHalf { u32x4 La[4], Lb[4], U[2]; unsigned dv; };
DI void convh_load_h(const Params& p, ConvHalf& in, int c, int half, int lane) {
  const i32x4 rh = make_srd(p.h, D_ * S_ * 2), rd = make_srd(p.dv, D_ * 2);
  const unsigned vo = lane * 16 + half * 4, so = c * 4096u;
  asm volatile("buffer_load_ushort %0, %1, %2, 0 offen" : "=v"(in.dv) : "v"(c * 2u), "s"(rd) : "memory");
#pragma unroll
  for (int i = 0; i < 4; ++i) { ld16<NT_H>(in.La[i], rh, vo, so + i * 1024); ld16(in.Lb[i], rh, vo + 4, so + i * 1024); }
}
DI void convh_load_u(const Params& p, ConvHalf& in, int c, int half, int lane) {
  const i32x4 ru = make_srd(p.ws + WS_KVT, D_ * S_ * 2);
#pragma unroll
  for (int i = 0; i < 2; ++i) ld16_sc1(in.U[i], ru, lane * 16, c * 4096u + (half * 2 + i) * 1024);
}
DI void convh_build(ConvHalf& in, LAS char* cb, int half, int lane) {
#pragma unroll
  for (int i = 0; i < 4; ++i) { PIN(in.La[i]); PIN(in.Lb[i]); }
  PIN(in.U[0]); PIN(in.U[1]); PIN(in.dv);
  LAS char* hb = cb + 2 * half * CV_H;
#pragma unroll
  for (int i = 0; i < 4; ++i) {
    const unsigned o = (i * 64 + lane + 2) * 16;
    u32x4 c0;
#pragma unroll
    for (int e = 0; e < 4; ++e) c0[e] = __builtin_amdgcn_alignbit(in.Lb[i][e], in.La[i][e], 16);
    *(LAS u32x4*)(hb + o) = c0;
    *(LAS u32x4*)(hb + CV_H + o) = in.Lb[i];
  }
#pragma unroll
  for (int i = 0; i < 2; ++i) *(LAS u32x4*)(cb + CV_U + ((half * 2 + i) * 64 + lane) * 16) = in.U[i];
  if (half == 0) {
    const u32x4 z4 = {0u, 0u, 0u, 0u};
    if (lane < 30) *(LAS u32x4*)(cb + CV_U + 4096 + lane * 16) = z4;
    if (lane == 0) {
      const unsigned w0 = in.La[0].x, w1 = in.La[0].y;
#pragma unroll
      for (int cc = 0; cc < 4; ++cc) *(LAS u32x4*)(cb + cc * CV_H) = z4;
      *(LAS u32x4*)(cb + 0 * CV_H + 16) = (u32x4){0u, 0u, 0u, w0 << 16};
      *(LAS u32x4*)(cb + 1 * CV_H + 16) = (u32x4){0u, 0u, 0u, w0};
      *(LAS u32x4*)(cb + 2 * CV_H + 16) = (u32x4){0u, 0u, w0 << 16, __builtin_amdgcn_alignbit(w1, w0, 16)};
      *(LAS u32x4*)(cb + 3 * CV_H + 16) = (u32x4){0u, 0u, w0, w1};
    }
  }
}

DI void g2_issue_w(const i32x4& srdD, const unsigned (&vd)[5], unsigned smb, int nt, int w, int j) {
  const unsigned sb = nt * 160u + (j < 13 ? w : (w < 2 ? w : 1)) * 40960u, lb = smb + L_WR + w * 5120;
#pragma unroll
  for (int i = 0; i < 5; ++i) dma16i<NT_WD>(srdD, vd[i], sb, j * 163840, lb, (j & 3) * G2_WSLOT + i * 1024);
}
DI void g2_wd_offsets(unsigned (&vd)[5], int lane) {
#pragma unroll
  for (int i = 0; i < 5; ++i) { const int idx = i * 64 + lane, rho = idx / 10, ch = idx - rho * 10; vd[i] = rho * 1280 + ch * 16; }
}
template <bool CONV>
DI void g1_loader(KP kp, Params p, unsigned long long pc0, LAS char* sm, unsigned smb, int rb, int ft, int mt, int nt, int hw, int lane, unsigned& tag, int cch, unsigned& dvb, u32x4 (&wy)[3][5]) {
  const i32x4 srdQ = make_srd(p.q, S_ * D_ * 2), srdW = make_srd((hw >> 1) ? p.wu : p.wg, D_ * F_ * 2), srdC = make_srd(p.ws, WS_KVT);
  const unsigned vq0 = (lane >> 3) * 1280 + (((lane & 7) ^ (lane >> 4)) * 16);
  const unsigned vq1 = (lane >> 3) * 1280 + (((lane & 7) ^ (4 + (lane >> 4))) * 16);
  const unsigned vw0 = (lane >> 3) * 3456 + (((lane & 7) ^ (2 * ((lane >> 4) & 1))) * 16);
  const unsigned vw1 = (lane >> 3) * 3456 + (((lane & 7) ^ (2 * (((lane >> 4) & 1) | 2))) * 16);
  const unsigned qb = (unsigned)(rb * 256 + 64 * hw) * 1280u, wb = (unsigned)(32 * (hw & 1)) * 3456u + ft * 128u;
  const unsigned lq = smb + hw * 8192, lw = smb + 32768 + (hw >> 1) * 8192 + (hw & 1) * 4096;
  auto issue = [&](int s, int slot) {
#pragma unroll
    for (int i = 0; i < 4; ++i) dma16i<NT_G1>(srdW, (i & 1) ? vw1 : vw0, wb, (s * 64 + 8 * i) * 3456, lw, slot * G1_SLOT + i * 1024);
#pragma unroll
    for (int i = 0; i < 8; ++i) dma16i<NT_G1>(srdQ, (i & 1) ? vq1 : vq0, qb, i * 10240 + s * 128, lq, slot * G1_SLOT + i * 1024);
  };
  const int xq = blockIdx.x & 7, wv = (blockIdx.x >> 3) * 4 + hw;
  const unsigned junk = smb + L_END - 256;
  const i32x4 srdD = make_srd(p.wd, F_ * D_ * 2);
  auto touch_slot = [&](int sl) {
    const int g = wv >> 5, sub = wv & 31, gsel = (sl + 1) % 3, st = sl + 3;
    unsigned fo = (((27 * (xq & 3)) >> 2) + (lane & 7)) * 128u; fo = fo < 3456u ? fo : 3328u;
    const unsigned vt = (unsigned)((xq >> 2) * 1024 + (sub & 15) * 64 + lane) * 1280u + st * 128u;
    const unsigned vw = (unsigned)(st * 64 + ((sub & 15) >> 1) * 8 + (lane >> 3)) * 3456u + fo;
    const bool stage = TOUCH && g == gsel;
    const void* base = (!stage || sub < 16) ? (const void*)p.q : ((sub & 1) ? (const void*)p.wu : (const void*)p.wg);
    touchg(base, stage ? (sub < 16 ? vt : vw) : 0u, junk);
  };
  unsigned ep; ld4_sc1(ep, srdC, CW_EPOCH);
  BAR();
  ConvHalf in;
  issue(0, 0); issue(1, 1);
  if (CONV) convh_load_h(p, in, cch, hw & 1, lane);
  touch_slot(-1);
  if (CONV) WAITV(22); else WAITV(13);
  PIN(ep); tag = __builtin_amdgcn_readfirstlane(ep) + 1u;
  if ((hw & 1) == 0 && lane == 0) *(LAS unsigned*)(sm + L_PF + (hw >> 1) * 4) = ~tag;
  BAR();
  if (hw == 0) flag_set(p.ws, CW_ARRIVE + blockIdx.x * 4, tag, lane);
  issue(2, 2); touch_slot(0);
  unsigned vd[5]; g2_wd_offsets(vd, lane);
  unsigned f1 = 0, f2 = 0; u32x2 st, st2;
  const unsigned f1off = lane < 16 ? CW_ST + (rb * 64 + hw * 16 + lane) * 16 : CW_ST + (mt * 16 + (lane & 15)) * 16;
  const unsigned f2off = CW_KV + ((CONV ? (cch >> 6) : 0) * 32 + (lane & 31)) * 16;
#pragma unroll
  for (int s = 1; s < 10; ++s) {
    const int nslot = (s + 2) % 3;
    if (s == 1) { if (CONV) WAITV(23); else WAITV(14); }
    else if (s == 6) WAITV(15);
    else if (s == 8) { if (CONV) WAITV(16); else WAITV(15); }
    else if (s == 9) WAITV(10);
    else WAITV(14);
    BAR();
    if (s == 4 || s == 7) p = reload(kp);
    if (s == 5) ld4_sc1(f1, srdC, f1off);
    if (s == 7) {
      PIN(f1);
      if (!__all(f1 == tag)) poll_all(p.ws, f1off, tag);
      ld8_sc1(st, srdC, WS_STATS + (rb * 256 + hw * 64 + lane) * 8, 0);
      ld8_sc1(st2, srdC, WS_STATS + (mt * 64 + lane) * 8, 0);
      if (CONV) ld4_sc1(f2, srdC, f2off);
    }
    if (s <= 7) issue(s + 2, nslot);
    if (s == 8) { g2_issue_w(srdD, vd, smb, nt, hw, 0); g2_issue_w(srdD, vd, smb, nt, hw, 1); }
    if (s <= 6) touch_slot(s);
    if (s == 9) {
      if (CONV) {
        PIN(f2);
        if (!__all(f2 == tag)) poll_all(p.ws, f2off, tag);
        convh_load_u(p, in, cch, hw & 1, lane);
      }
      PIN(st); PIN(st2);
      *(LAS u32x2*)(sm + L_ST + (hw * 64 + lane) * 8) = st;
      if (hw == 0) *(LAS u32x2*)(sm + L_S2 + lane * 8) = st2;
    }
  }
  BAR();
#pragma unroll
  for (int j = 0; j < 3; ++j)
#pragma unroll
    for (int i = 0; i < 5; ++i) ld16i<NT_WD>(wy[j][i], srdD, vd[i], nt * 160u + hw * 40960u, (j + 2) * 163840);
  if (CONV) { WAITV(15); convh_build(in, sm + L_CVG + (hw >> 1) * CV_SZ, hw & 1, lane); dvb = in.dv; }
  WAITV(0);
#pragma unroll
  for (int j = 0; j < 3; ++j)
#pragma unroll
    for (int i = 0; i < 5; ++i) PIN(wy[j][i]);
}

template <bool SWAP>
DI void g1_epilogue(const Params& p, LAS char* sm, int rb, int ft, int wr, int wc, int lane, const f32x4 (&a1)[2][8], const f32x4 (&a2)[2][8], const f32x4 (&csg)[2], const f32x4 (&csu)[2], const f32x2 (&st)[8]) {
  const __amdgpu_buffer_rsrc_t rh = __builtin_amdgcn_make_buffer_rsrc((void*)(p.ws + WS_HM), 0, 54 * 128 * 1024, 0x00020000);
  const unsigned hb = (unsigned)((ft * 2 + wc) * 128 + rb * 16 + wr * 8) * 1024u + lane * 16;
#pragma unroll
  for (int m = 0; m < 8; ++m) {
    asm volatile("; gated epilogue, operand order %0" :: "i"(SWAP ? 1 : 0));
    const float nm = -st[m].x, rs = st[m].y, rsl = rs * -1.44269504089f, rs2 = rs * rs;
    f32x2 tg[4], tu[4], e[4];
#pragma unroll
    for (int h = 0; h < 4; ++h) {
      const int a = h >> 1, i = (h & 1) * 2;
      const f32x4 ga = SWAP ? a2[a][m] : a1[a][m], ua = SWAP ? a1[a][m] : a2[a][m];
      tg[h] = (f32x2){csg[a][i], csg[a][i + 1]} * nm + (f32x2){ga[i], ga[i + 1]};
      tu[h] = (f32x2){csu[a][i], csu[a][i + 1]} * nm + (f32x2){ua[i], ua[i + 1]};
    }
#pragma unroll
    for (int h = 0; h < 4; ++h) { const f32x2 gl = tg[h] * rsl; e[h].x = __builtin_amdgcn_exp2f(gl.x); e[h].y = __builtin_amdgcn_exp2f(gl.y); }
#pragma unroll
    for (int h = 0; h < 4; ++h) { tu[h] = (tg[h] * tu[h]) * rs2; e[h] = e[h] + 1.0f; }
#pragma unroll
    for (int h = 0; h < 4; ++h) { e[h].x = __builtin_amdgcn_rcpf(e[h].x); e[h].y = __builtin_amdgcn_rcpf(e[h].y); }
    u32x4 w;
#pragma unroll
    for (int h = 0; h < 4; ++h) { const f32x2 hv = tu[h] * e[h]; w[h] = pk2(hv.x, hv.y); }
    __builtin_amdgcn_raw_buffer_store_b128(w, rh, hb + m * 1024, 0, 16);
  }
}
DI void g1_math(const Params& p, LAS char* sm, int rb, int ft, int wid, int lane, unsigned tag, unsigned taskflag) {
  const int wr = wid >> 1, wc = wid & 1;
  const int n = lane & 15, kg = lane >> 4, q4 = (lane & 15) >> 2, pp = lane & 3;
  f32x4 a1[2][8], a2[2][8], cs[2];
#pragma unroll
  for (int a = 0; a < 2; ++a) {
    cs[a] = (f32x4){0.f, 0.f, 0.f, 0.f};
#pragma unroll
    for (int m = 0; m < 8; ++m) { a1[a][m] = (f32x4){0.f, 0.f, 0.f, 0.f}; a2[a][m] = (f32x4){0.f, 0.f, 0.f, 0.f}; }
  }
  u32x4 ones_ = {0x3f803f80u, 0x3f803f80u, 0x3f803f80u, 0x3f803f80u};
  PIN(ones_);
  const bf16x8 ones = __builtin_bit_cast(bf16x8, ones_);
  const unsigned tbase = (wr * 128 + n) * 128 + ((kg ^ ((n >> 1) & 7)) * 16);
  const int sw = 2 * (((q4 >> 1) & 1) | ((kg & 1) << 1));
  const unsigned wb0 = 32768 + (8 * kg + q4) * 128 + (((4 * wc + 0 + (pp >> 1)) ^ sw) * 16) + 8 * (pp & 1);
  const unsigned wb1 = 32768 + (8 * kg + q4) * 128 + (((4 * wc + 2 + (pp >> 1)) ^ sw) * 16) + 8 * (pp & 1);
  const unsigned o1 = wr * 8192, o2 = 8192 - o1;
  bf16x8 tP[8], tQ[8], gP[2], gQ[2], uP[2], uQ[2];
#define G1_RD(T, G, U, so, ks) do { LAS char* tp_ = sm + (so) + (tbase ^ ((ks) * 64)); \
    LAS char* w10_ = sm + (so) + wb0 + o1 + (ks) * 4096; LAS char* w11_ = sm + (so) + wb1 + o1 + (ks) * 4096; \
    LAS char* w20_ = sm + (so) + wb0 + o2 + (ks) * 4096; LAS char* w21_ = sm + (so) + wb1 + o2 + (ks) * 4096; \
    G[0] = ldtr(w10_, w10_ + 512); T[0] = *(LAS bf16x8*)(tp_); U[0] = ldtr(w20_, w20_ + 512); G[1] = ldtr(w11_, w11_ + 512); U[1] = ldtr(w21_, w21_ + 512); \
    _Pragma("unroll") for (int m = 1; m < 8; ++m) T[m] = *(LAS bf16x8*)(tp_ + 2048 * m); } while (0)
#define G1_MM(T, G, U) do { _Pragma("unroll") for (int m = 0; m < 8; ++m) { _Pragma("unroll") for (int a = 0; a < 2; ++a) { \
    a1[a][m] = __builtin_amdgcn_mfma_f32_16x16x32_bf16(G[a], T[m], a1[a][m], 0, 0, 0); a2[a][m] = __builtin_amdgcn_mfma_f32_16x16x32_bf16(U[a], T[m], a2[a][m], 0, 0, 0); } } } while (0)
#define G1_CS(G) do { cs[0] = __builtin_amdgcn_mfma_f32_16x16x32_bf16(G[0], ones, cs[0], 0, 0, 0); cs[1] = __builtin_amdgcn_mfma_f32_16x16x32_bf16(G[1], ones, cs[1], 0, 0, 0); } while (0)
#define G1_SCHED() do { _Pragma("unroll") for (int i_ = 0; i_ < 16; ++i_) { __builtin_amdgcn_sched_group_barrier(0x8, 1, 0); __builtin_amdgcn_sched_group_barrier(0x100, 1, 0); } __builtin_amdgcn_sched_group_barrier(0x8, 18, 0); } while (0)
  unsigned so = 0;
  BAR();
  STAMP_AT(1); STAMP_CY(14);
  G1_RD(tP, gP, uP, so, 0);
  G1_RD(tQ, gQ, uQ, so, 1); G1_MM(tP, gP, uP); G1_CS(gP); G1_SCHED();
#pragma unroll 1
  for (int s = 1; s < 10; ++s) {
    so = so == 2 * G1_SLOT ? 0 : so + G1_SLOT;
    BAR();
    if (s == 2 && taskflag) { WAITV(0); flag_set(p.ws, taskflag, tag, lane); }
    G1_RD(tP, gP, uP, so, 0); G1_MM(tQ, gQ, uQ); G1_CS(gQ); G1_SCHED();
    G1_RD(tQ, gQ, uQ, so, 1); G1_MM(tP, gP, uP); G1_CS(gP); G1_SCHED();
  }
  G1_CS(gQ);
  if (n == 0) { *(LAS f32x4*)(sm + L_CS + (wr * 64 + wc * 32 + 4 * kg) * 4) = cs[0]; *(LAS f32x4*)(sm + L_CS + (wr * 64 + wc * 32 + 16 + 4 * kg) * 4) = cs[1]; }
  BAR();
  f32x4 csg[2], csu[2];
#pragma unroll
  for (int a = 0; a < 2; ++a) { csg[a] = *(LAS f32x4*)(sm + L_CS + (wc * 32 + 16 * a + 4 * kg) * 4); csu[a] = *(LAS f32x4*)(sm + L_CS + (64 + wc * 32 + 16 * a + 4 * kg) * 4); }
  f32x2 st[8];
#pragma unroll
  for (int m = 0; m < 8; ++m) st[m] = *(LAS f32x2*)(sm + L_ST + (wr * 128 + 16 * m + n) * 8);
  G1_MM(tQ, gQ, uQ);
  STAMP_AT(2); STAMP_CY(15);
  if (wr == 0) g1_epilogue<false>(p, sm, rb, ft, wr, wc, lane, a1, a2, csg, csu, st);
  else g1_epilogue<true>(p, sm, rb, ft, wr, wc, lane, a1, a2, csg, csu, st);
  STAMP_AT(3);
}

template <int NPRE> constexpr int g2_w_after(int j) { return (j >= 1 && j + 3 <= 13 && j + 3 >= NPRE) ? 5 : 0; }
template <int NPRE> constexpr int g2_ops_after(int j) { return (j == 5 ? 1 : 0) + (j == 7 ? 3 : 0) + ((j >= 1 && j + 2 <= 13) ? 4 : 0) + g2_w_after<NPRE>(j); }
template <int NPRE> constexpr int g2_wait(int j) {
  return j == 0 ? 4 : (j == 1 ? g2_ops_after<NPRE>(1) : g2_w_after<NPRE>(j - 1) + g2_ops_after<NPRE>(j));
}
template <bool ISP>
DI void g2_loader(const Params& p, LAS char* sm, unsigned smb, int mt, int nt, int w, int lane, unsigned tag, unsigned hmflag) {
  const i32x4 srdD = make_srd(p.wd, F_ * D_ * 2), srdH = make_srd(p.ws + WS_HM, 54 * 128 * 1024), srdC = make_srd(p.ws, WS_KVT);
  unsigned vd[5]; g2_wd_offsets(vd, lane);
  const unsigned vh = lane * 16;
  auto fbof = [&](int j) { const int fb = 4 * j + w; return fb < 54 ? fb : 53; };
  auto issueH = [&](int j) {
    const unsigned sb = mt * 4096u + (j < 13 ? w : (w < 2 ? w : 1)) * 131072u, lb = smb + L_HR + w * 4096;
#pragma unroll
    for (int i = 0; i < 4; ++i) dma16i_sc1(srdH, vh, sb, j * 524288 + i * 1024, lb, (j % 3) * 16384 + i * 1024);
  };
  constexpr int XB = ISP ? 1 : 5, NREG = ISP ? 9 : 7, NPRE = XB + NREG;
  u32x4 wreg[NREG][5];
  if (ISP) g2_issue_w(srdD, vd, smb, nt, w, 0);
  else BAR();
#pragma unroll
  for (int j = 0; j < NREG; ++j) {
#pragma unroll
    for (int i = 0; i < 5; ++i) ld16i<NT_WD>(wreg[j][i], srdD, vd[i], nt * 160u + w * 40960u, (j + XB) * 163840);
    if (!ISP && j == 0) {
      WAITV(5);
      flag_set(p.ws, hmflag, tag, lane);
      STAMP_AT(4);
    }
  }
  {
    const unsigned junk = smb + L_END - 256;
#pragma unroll
    for (int r = 0; r < (14 - NPRE + 3) / 4; ++r) {
      const int idx = r * 64 + lane;
      int j = NPRE + (idx >> 4); j = j < 14 ? j : 13;
      touch4(srdD, (unsigned)(fbof(j) * 32 + (mt & 3) * 8 + ((idx & 15) >> 1)) * 1280u + nt * 160u + (idx & 1) * 128u, 0, junk);
    }
  }
  if (ISP && w == 0) {
    poll_all(p.ws, CW_ST + (mt * 16 + (lane & 15)) * 16, tag);
    u32x2 sv; ld8_sc1(sv, srdC, WS_STATS + (mt * 64 + lane) * 8, 0);
    WAITV(0); PIN(sv);
    *(LAS u32x2*)(sm + L_S2 + lane * 8) = sv;
  }
  if (ISP && blockIdx.x == 255 && w == 3) {
    poll_all(p.ws, CW_ARRIVE + lane * 4, tag); poll_all(p.ws, CW_ARRIVE + 256 + lane * 4, tag);
    poll_all(p.ws, CW_ARRIVE + 512 + lane * 4, tag); poll_all(p.ws, CW_ARRIVE + 768 + lane * 4, tag);
    flag_set(p.ws, CW_EPOCH, tag, lane);
  }
  { const int j = lane < 14 ? lane : 0; poll_all(p.ws, CW_HM + (((mt >> 2) * 2 + ((mt >> 1) & 1)) * 54 + fbof(j)) * 16, tag); }
  STAMP_AT(5);
  { const int j = (lane >> 2) < 14 ? (lane >> 2) : 13;
    touch4_sc1(srdH, (unsigned)(fbof(j) * 128 + mt * 4 + (lane & 3)) * 1024u + nt * 128u, 0, smb + L_END - 256); }
  issueH(0); issueH(1); issueH(2);
  WAITV(8);
#pragma unroll
  for (int j = 0; j < NREG; ++j)
#pragma unroll
    for (int i = 0; i < 5; ++i) PIN(wreg[j][i]);
  BAR();
  STAMP_AT(6);
  unsigned zf = 0; u32x4 zr[3];
  const unsigned zoff = CW_Z + (nt * 80 + w * 20 + (lane < 20 ? lane : 0)) * 16;
  auto put_w = [&](int j) {
#pragma unroll
    for (int i = 0; i < 5; ++i) *(LAS u32x4*)(sm + L_WR + (j & 3) * G2_WSLOT + w * 5120 + i * 1024 + lane * 16) = wreg[j - XB][i];
  };
#pragma unroll
  for (int j = 0; j < 14; ++j) {
    if (j == 0) {
#pragma unroll
      for (int jj = XB; jj < 4; ++jj) put_w(jj);
    } else if (j + 3 >= XB && j + 3 < NPRE) put_w(j + 3);
    if (j == 5) ld4_sc1(zf, srdC, zoff);
    if (j == 7) {
      PIN(zf);
      if (!__all(zf == tag)) poll_all(p.ws, zoff, tag);
      const i32x4 srdZ = make_srd(p.ws + WS_ZT, D_ * S_ * 2);
#pragma unroll
      for (int r = 0; r < 3; ++r) {
        int task = r * 64 + lane; task = task < 160 ? task : 159;
        ld16_sc1(zr[r], srdZ, (unsigned)(nt * 80 + w * 20 + (task >> 3)) * 4096u + (mt * 64 + (task & 7) * 8) * 2, 0);
      }
    }
    if (j >= 1 && j + 2 <= 13) issueH(j + 2);
    if (g2_w_after<NPRE>(j)) g2_issue_w(srdD, vd, smb, nt, w, j + 3);
    if (j == 9) {
#pragma unroll
      for (int r = 0; r < 3; ++r) PIN(zr[r]);
#pragma unroll
      for (int r = 0; r < 3; ++r) { const int task = r * 64 + lane; if (task < 160) *(LAS u32x4*)(sm + L_ZS + (w * 20 + (task >> 3)) * ZS_RS + (task & 7) * 16) = zr[r]; }
    }
    if (j < 13) WAITV(g2_wait<NPRE>(j));
    BAR();
  }
}

template <int NY>
DI void g2_math(const Params& p, LAS char* sm, int w, int lane, unsigned tag, int zc, const u32x4 (&wy)[3][5]) {
  f32x4 acc[5][4];
#pragma unroll
  for (int a = 0; a < 5; ++a)
#pragma unroll
    for (int b = 0; b < 4; ++b) acc[a][b] = (f32x4){0.f, 0.f, 0.f, 0.f};
  const int kg = lane >> 4, q4 = (lane & 15) >> 2, pp = lane & 3;
  LAS char* hrd = sm + L_HR + w * 4096 + lane * 16;
  LAS char* wrd = sm + L_WR + w * 5120 + (4 * kg + q4) * 160 + pp * 8;
  bf16x8 hP[4], wP[5], hQ[4], wQ[5];
#define G2_RD(H, W, j) do { LAS char* hp_ = hrd + ((j) % 3) * 16384; LAS char* wp_ = wrd + ((j) & 3) * G2_WSLOT; \
    W[0] = ldtr(wp_, wp_ + 2560); H[0] = *(LAS bf16x8*)(hp_); H[1] = *(LAS bf16x8*)(hp_ + 1024); H[2] = *(LAS bf16x8*)(hp_ + 2048); H[3] = *(LAS bf16x8*)(hp_ + 3072); \
    _Pragma("unroll") for (int a = 1; a < 5; ++a) W[a] = ldtr(wp_ + a * 32, wp_ + a * 32 + 2560); } while (0)
#define G2_MM(H, W) do { _Pragma("unroll") for (int a = 0; a < 5; ++a) _Pragma("unroll") for (int b = 0; b < 4; ++b) \
    acc[a][b] = __builtin_amdgcn_mfma_f32_16x16x32_bf16(W[a], H[b], acc[a][b], 0, 0, 0); } while (0)
#define G2_SCHED() do { _Pragma("unroll") for (int i_ = 0; i_ < 14; ++i_) { __builtin_amdgcn_sched_group_barrier(0x8, 1, 0); __builtin_amdgcn_sched_group_barrier(0x100, 1, 0); } __builtin_amdgcn_sched_group_barrier(0x8, 6, 0); } while (0)
  if (zc >= 0) WAITV(4); else WAITV(0);
  BAR();
  STAMP_AT(9);
  G2_RD(hP, wP, 0);
  auto put_y = [&](int j) {
#pragma unroll
    for (int i = 0; i < 5; ++i) *(LAS u32x4*)(sm + L_WR + (j & 3) * G2_WSLOT + w * 5120 + i * 1024 + lane * 16) = wy[j - 2][i];
  };
  if (NY) { put_y(2); put_y(3); }
  if (zc >= 0) {
    WAITV(0);
    flag_set(p.ws, CW_Z + zc * 16, tag, lane);
  }
  BAR();
  G2_RD(hQ, wQ, 1); G2_MM(hP, wP); G2_SCHED();
  if (NY) put_y(4);
#pragma unroll
  for (int j = 2; j < 14; j += 2) {
    BAR();
    G2_RD(hP, wP, j); G2_MM(hQ, wQ); G2_SCHED();
    BAR();
    G2_RD(hQ, wQ, j + 1); G2_MM(hP, wP); G2_SCHED();
  }
  BAR();
  if (w < 2) G2_MM(hQ, wQ);
  STAMP_AT(10);
#pragma unroll
  for (int a = 0; a < 5; ++a)
#pragma unroll
    for (int b = 0; b < 4; ++b) *(LAS f32x4*)(sm + w * G2_PART + (16 * b + (lane & 15)) * G2_RS + a * 64 + kg * 16) = acc[a][b];
}
DI void g2_final_task(const Params& p, LAS char* sm, int mt, int nt, int gt, int lane, const u32x4& x) {
  const int b = gt / 10, ch = gt - b * 10, tk = b * 16 + (lane & 15);
  f32x4 a0 = (f32x4){0.f, 0.f, 0.f, 0.f}, a1 = a0;
#pragma unroll
  for (int w = 0; w < 4; ++w) { a0 += *(LAS f32x4*)(sm + w * G2_PART + tk * G2_RS + ch * 32); a1 += *(LAS f32x4*)(sm + w * G2_PART + tk * G2_RS + ch * 32 + 16); }
  LAS char* zp = sm + L_ZS + (ch * 8 + ((lane & 15) >> 2)) * ZS_RS + b * 32 + (lane & 3) * 8;
  const u32x2 z0 = __builtin_bit_cast(u32x2, __builtin_amdgcn_ds_read_tr16_b64_v4i16((LAS s16x4*)zp));
  const u32x2 z1 = __builtin_bit_cast(u32x2, __builtin_amdgcn_ds_read_tr16_b64_v4i16((LAS s16x4*)(zp + 4 * ZS_RS)));
  const f32x2 st = *(LAS f32x2*)(sm + L_S2 + tk * 8);
  const float rs = st.y, nmr = -st.x * rs;
  float o[8];
#pragma unroll
  for (int e = 0; e < 8; ++e) {
    const float xv = (e & 1) ? bhi(x[e >> 1]) : blo(x[e >> 1]);
    const unsigned zw = e < 4 ? z0[(e & 3) >> 1] : z1[(e & 3) >> 1];
    const float z = (e & 1) ? bhi(zw) : blo(zw);
    const float xn = fmaf(xv, rs, nmr);
    o[e] = fmaf(xn, z, xv) + (e < 4 ? a0[e] : a1[e - 4]);
  }
  const u32x4 r = {pk2(o[0], o[1]), pk2(o[2], o[3]), pk2(o[4], o[5]), pk2(o[6], o[7])};
  *(GAS u32x4*)(p.out + (mt * 64 + tk) * 640 + nt * 80 + ch * 8) = r;
}
DI u32x4 final_x(const Params& p, int mt, int nt, int gt, int lane) {
  const int b = gt / 10, ch = gt - b * 10;
  return *(const GAS u32x4*)(p.q + (mt * 64 + b * 16 + (lane & 15)) * 640 + nt * 80 + ch * 8);
}

DI void pair_send(LAS char* pb, LAS unsigned* pf, unsigned tag, int lane, const f32x4 (&acc)[8]) {
#pragma unroll
  for (int g = 0; g < 8; ++g) *(LAS f32x4*)(pb + g * 1024 + lane * 16) = acc[g];
  asm volatile("s_waitcnt lgkmcnt(0)" ::: "memory");
  if (lane == 0) *(volatile LAS unsigned*)pf = tag;
}
DI void pair_recv(LAS char* pb, LAS unsigned* pf, unsigned tag, int lane, f32x4 (&acc)[8]) {
#pragma unroll 1
  for (unsigned it = 0; it < (1u << 22); ++it) { if (*(volatile LAS unsigned*)pf == tag) break; __builtin_amdgcn_s_sleep(1); }
  asm volatile("" ::: "memory");
#pragma unroll
  for (int g = 0; g < 8; ++g) acc[g] += *(LAS f32x4*)(pb + g * 1024 + lane * 16);
}

__global__ __launch_bounds__(512, 2) void fused_kernel(Params p_arg) {
  unsigned long long pc0; asm volatile("s_getpc_b64 %0" : "=s"(pc0));
  const KP kp = (KP)__builtin_amdgcn_kernarg_segment_ptr();
  Params p = reload(kp);
  extern __shared__ __attribute__((aligned(16))) char smem_raw[];
  LAS char* sm = (LAS char*)smem_raw;
  const unsigned smb = (unsigned)(size_t)sm;
  const int lane = threadIdx.x & 63, wid = __builtin_amdgcn_readfirstlane(threadIdx.x >> 6);
  const int b = blockIdx.x, x = b & 7, i = b >> 3;
  const int mt = x * 4 + (i & 3), nt = i >> 2;
  STAMP_INIT();
  STAMP_AT(0);
  if (i < 27) {
    const int mm = 27 * (x & 3) + i, ft = mm >> 2, rb = 4 * (x >> 2) + (mm & 3);
    if (wid < 4) {
      const unsigned ep = __hip_atomic_load((gu32*)(p.ws + CW_EPOCH), __ATOMIC_RELAXED, __HIP_MEMORY_SCOPE_AGENT);
      unsigned taskflag = 0;
      const int id = wid < 2 ? 2 * b + wid : 432 + b;
      if (wid < 3 && id < 512) { task_stats<true>(p, id, lane); taskflag = CW_ST + id * 16; }
      else BAR();
      const unsigned tag = __builtin_amdgcn_readfirstlane(ep) + 1u;
      asm volatile("" :: "s"(tag) : "memory");
      STAMP_AT(13);
      g1_math(p, sm, rb, ft, wid, lane, tag, taskflag);
      p = reload(kp);
      g2_loader<false>(p, sm, smb, mt, nt, wid, lane, tag, CW_HM + ((rb * 2 + (wid >> 1)) * 54 + ft * 2 + (wid & 1)) * 16);
    } else {
      const int hw = wid - 4;
      const bool conv = 2 * b < NCH_G;
      const int cch = 2 * b + (hw >> 1);
      unsigned tag, dvb = 0; int zc = -1;
      if (conv) {
        u32x4 wy[3][5];
        g1_loader<true>(kp, p, pc0, sm, smb, rb, ft, mt, nt, hw, lane, tag, cch, dvb, wy);
        p = reload(kp);
        BAR();
        STAMP_AT(7);
        LAS char* cb = sm + L_CVG + (hw >> 1) * CV_SZ;
        LAS char* pb = sm + L_CP + (hw >> 1) * 8192;
        LAS unsigned* pf = (LAS unsigned*)(sm + L_PF + (hw >> 1) * 4);
        f32x4 acc[8];
        conv_mfma<4>(cb, (hw & 1) * 4, lane, acc);
        asm volatile("" :: "v"(acc[0]), "v"(acc[7]));
        STAMP_AT(4);
        if (hw & 1) pair_send(pb, pf, tag, lane, acc);
        else { pair_recv(pb, pf, tag, lane, acc); conv_store(p, cb, cch, lane, dvb, acc); zc = cch; }
        STAMP_AT(8);
        g2_math<3>(p, sm, hw, lane, tag, zc, wy);
      } else {
        u32x4 wy[3][5];
        g1_loader<false>(kp, p, pc0, sm, smb, rb, ft, mt, nt, hw, lane, tag, cch, dvb, wy);
        p = reload(kp);
        BAR();
        g2_math<3>(p, sm, hw, lane, tag, zc, wy);
      }
    }
  } else {
    const int cg = i - 27, pb = x * 5 + cg;
    const unsigned ep = __hip_atomic_load((gu32*)(p.ws + CW_EPOCH), __ATOMIC_RELAXED, __HIP_MEMORY_SCOPE_AGENT);
    const int hw = wid - 4, c0 = NCH_G + pb * NCH_P;
    if (wid < 4) {
      task_kv<0>(p, 2 * cg + (wid & 1), 4 * x + (wid >> 1), lane, [] {});
      const unsigned tag = __builtin_amdgcn_readfirstlane(ep) + 1u;
      asm volatile("" :: "s"(tag) : "memory");
      WAITV(0);
      flag_set(p.ws, CW_KV + ((2 * cg + (wid & 1)) * 32 + 4 * x + (wid >> 1)) * 16, tag, lane);
      BAR();
      if (wid == 0) flag_set(p.ws, CW_ARRIVE + b * 4, tag, lane);
      STAMP_AT(1);
      BAR();
      p = reload(kp);
      touchg(p.wd, (unsigned)((pb * 4 + wid) * 64 + lane) * 128u, smb + L_END - 256);
      if (pb * 4 + wid + 160 < 270) touchg(p.wd, (unsigned)((pb * 4 + wid + 160) * 64 + lane) * 128u, smb + L_END - 256);
      g2_loader<true>(p, sm, smb, mt, nt, wid, lane, tag, 0);
    } else {
      LAS char* cb = sm + L_CVP + hw * CV_SZ;
      const int cA = c0 + hw, cB = c0 + 4 + (hw >> 1);
      ConvIn in, in2;
      task_kv<13>(p, 2 * cg + (wid & 1), 4 * x + (wid >> 1), lane, [&] { conv_load_h(p, in, cA, lane); });
      conv_load_h(p, in2, cB, lane);
      const unsigned tag = __builtin_amdgcn_readfirstlane(ep) + 1u;
      asm volatile("" :: "s"(tag) : "memory");
      if ((hw & 1) == 0 && lane == 0) *(LAS unsigned*)(sm + L_PFP + (hw >> 1) * 4) = ~tag;
      WAITV(13);
      flag_set(p.ws, CW_KV + ((2 * cg + (wid & 1)) * 32 + 4 * x + (wid >> 1)) * 16, tag, lane);
      BAR();
      STAMP_AT(1);
      poll_all(p.ws, CW_KV + (((lane < 32 ? cA : cB) >> 6) * 32 + (lane & 31)) * 16, tag);
      conv_load_u(p, in, cA, lane);
      conv_load_u(p, in2, cB, lane);
      BAR();
      WAITV(4);
      conv_pin(in);
      conv_build(in, cb, lane);
      unsigned dvb = in.dv;
      STAMP_AT(2); STAMP_CY(14);
      f32x4 acc[8];
      conv_mfma<8>(cb, 0, lane, acc);
      asm volatile("" :: "v"(acc[0]), "v"(acc[7]));
      STAMP_AT(4); STAMP_CY(15);
      conv_store(p, cb, cA, lane, dvb, acc);
      STAMP_AT(3);
      WAITV(4);
      conv_pin(in2);
      conv_build(in2, cb, lane);
      dvb = in2.dv;
      STAMP_AT(7);
      conv_mfma<4>(cb, (hw & 1) * 4, lane, acc);
      WAITV(0);
      flag_set(p.ws, CW_Z + cA * 16, tag, lane);
      LAS unsigned* pf = (LAS unsigned*)(sm + L_PFP + (hw >> 1) * 4);
      int zc = -1;
      if (hw & 1) pair_send(cb, pf, tag, lane, acc);
      else { pair_recv(cb + CV_SZ, pf, tag, lane, acc); conv_store(p, cb, cB, lane, dvb, acc); zc = cB; }
      STAMP_AT(8);
      u32x4 wy[3][5];
      g2_math<0>(p, sm, hw, lane, tag, zc, wy);
    }
  }
  p = reload(kp);
  const int gt0 = wid * 4 + (lane >> 4), gt1 = 32 + gt0;
  const u32x4 x0 = final_x(p, mt, nt, gt0, lane);
  u32x4 x1 = x0;
  if (wid < 2) x1 = final_x(p, mt, nt, gt1, lane);
  BAR();
  g2_final_task(p, sm, mt, nt, gt0, lane, x0);
  if (wid < 2) g2_final_task(p, sm, mt, nt, gt1, lane, x1);
  STAMP_AT(11);
  if (blockIdx.x == DIAG_B && wid == DIAG_W) {
    const u32x4 a_ = *(LAS u32x4*)((LAS char*)smem_dbg + L_DBG + (DIAG_W * 16 + 0) * 16), b_ = *(LAS u32x4*)((LAS char*)smem_dbg + L_DBG + (DIAG_W * 16 + DIAG_I) * 16);
    const unsigned d_ = __builtin_amdgcn_readfirstlane(b_.x - a_.x) * 4u;
    const unsigned long long t0_ = __builtin_amdgcn_s_memrealtime();
    for (unsigned it = 0; it < (1u << 20); ++it) { if (__builtin_amdgcn_s_memrealtime() - t0_ >= d_ || d_ > 100000u) break; __builtin_amdgcn_s_sleep(1); }
  }
}

extern "C" void kernel_launch(void* const* d_in, const int* in_sizes, int n_in, void* d_out, int out_size, void* d_ws, size_t ws_size, hipStream_t stream) {
  Params p;
  p.q = (const u16*)d_in[0]; p.k = (const u16*)d_in[1]; p.v = (const u16*)d_in[2]; p.sk = (const u16*)d_in[3]; p.h = (const u16*)d_in[4]; p.dv = (const u16*)d_in[5];
  p.wg = (const u16*)d_in[7]; p.wu = (const u16*)d_in[8]; p.wd = (const u16*)d_in[9]; p.out = (u16*)d_out; p.ws = (char*)d_ws;
  (void)hipFuncSetAttribute((const void*)fused_kernel, hipFuncAttributeMaxDynamicSharedMemorySize, L_ALLOC);
  fused_kernel<<<256, 512, L_ALLOC, stream>>>(p);
}
#define NCLS 4
static inline int stamp_class(int b, int w) { return ((b >> 3) < 27 ? 0 : 2) + (w < 4 ? 0 : 1); }
static inline const char* cls_name(int c) { return c == 0 ? "GX" : c == 1 ? "GY" : c == 2 ? "PX" : "PY"; }
#define HAVE_INTER 1
#define HM_K16 1
```

```cpp
#define OUT_POL 2
#include <hip/hip_runtime.h>
#include <stdint.h>
typedef unsigned short u16;
typedef __bf16 bf16x8 __attribute__((ext_vector_type(8)));
typedef __bf16 bf16x2 __attribute__((ext_vector_type(2)));
typedef short s16x4 __attribute__((ext_vector_type(4)));
typedef float f32x2 __attribute__((ext_vector_type(2)));
typedef float f32x4 __attribute__((ext_vector_type(4)));
typedef unsigned u32x2 __attribute__((ext_vector_type(2)));
typedef unsigned u32x4 __attribute__((ext_vector_type(4)));
typedef int i32x4 __attribute__((ext_vector_type(4)));
#define LAS __attribute__((address_space(3)))
#define GAS __attribute__((address_space(1)))
#define DI __device__ __forceinline__
typedef GAS unsigned gu32;

#define TOUCH 1
#define TOUCH_CODE 0
#define CODE_BACK 0
#define NT_G1 0
#define NT_KV 1
#define NT_H 1
#define NT_WD 0
#define TLBW 0
constexpr int S_ = 2048, D_ = 640, F_ = 1728;
constexpr unsigned CW_EPOCH = 0;
constexpr unsigned CW_ARRIVE = 1024;
constexpr unsigned CW_KV = 4096;
constexpr unsigned CW_ST = 16384;
constexpr unsigned CW_HM = 24576;
constexpr unsigned CW_Z = 40960;
constexpr unsigned WS_STATS = 65536;
constexpr unsigned WS_KVT = 131072;
constexpr unsigned WS_ZT = WS_KVT + D_ * S_ * 2;
constexpr unsigned WS_HM = WS_ZT + D_ * S_ * 2;
constexpr unsigned WS_END = WS_HM + 54 * 128 * 1024;
constexpr unsigned WS_DBG = 32u << 20;
constexpr int G1_SLOT = 49152;
constexpr int L_CS = 153856, L_ST = L_CS + 512, L_S2 = L_ST + 2048, L_END = L_S2 + 512 + 64 + 256;
constexpr int L_ALLOC = L_END;
constexpr int L_HR = 0, L_WR = 49152, G2_WSLOT = 20480;
constexpr int L_CVG = 90112, L_CVP = 69632;
constexpr int L_ZS = 131072, ZS_RS = 144;
constexpr int L_CP = L_CVG + 2 * 21216, L_PF = L_CS - 64;
constexpr int L_PFP = L_S2 + 512 - 0;
constexpr int G2_RS = 336, G2_PART = 64 * G2_RS;
constexpr int CV_H = 4160, CV_U = 4 * CV_H, CV_SZ = CV_U + 4576;
static_assert(L_ZS + 80 * ZS_RS <= L_PF, "lds");
static_assert(L_CP + 2 * 8192 <= L_PF, "lds");
static_assert(L_CVP + 4 * CV_SZ + 64 <= L_S2, "lds");
constexpr int NCH_P = 6;
constexpr int NCH_G = D_ - 40 * NCH_P;
static_assert(NCH_G <= 2 * 216 && NCH_G % 2 == 0, "conv split");
constexpr unsigned SPIN_MAX = 1u << 16;

struct Params {
  const u16 *q, *k, *v, *sk, *h, *dv, *wg, *wu, *wd;
  u16* out; char* ws;
};

typedef const __attribute__((address_space(4))) Params* KP;
DI Params reload(KP kp) {
  asm volatile("" : "+s"(kp));
  Params r; r.q = kp->q; r.k = kp->k; r.v = kp->v; r.sk = kp->sk; r.h = kp->h; r.dv = kp->dv; r.wg = kp->wg; r.wu = kp->wu; r.wd = kp->wd; r.out = kp->out; r.ws = kp->ws;
  return r;
}
DI i32x4 make_srd(const void* p, unsigned bytes) {
  i32x4 r; unsigned long long a = (unsigned long long)p;
  r.x = (int)(unsigned)a;
  r.y = (int)((unsigned)(a >> 32) & 0xffffu);
  r.z = (int)bytes; r.w = 0x00020000; return r;
}
DI void dma16(const i32x4& srd, unsigned voff, unsigned soff, unsigned lds) {
  asm volatile("s_mov_b32 m0, %3\n\ts_nop 0\n\tbuffer_load_dwordx4 %0, %1, %2 offen lds" :: "v"(voff), "s"(srd), "s"(soff), "s"(lds) : "memory");
}
DI void dma16_sc1(const i32x4& srd, unsigned voff, unsigned soff, unsigned lds) {
  asm volatile("s_mov_b32 m0, %3\n\ts_nop 0\n\tbuffer_load_dwordx4 %0, %1, %2 offen sc1 lds" :: "v"(voff), "s"(srd), "s"(soff), "s"(lds) : "memory");
}
template <int NT = 0>
DI void dma16i(const i32x4& srd, unsigned voff, unsigned sbase, int simm, unsigned lbase, int limm) {
  unsigned t;
  if (NT) asm volatile("s_add_u32 m0, %4, %6\n\ts_add_u32 %0, %3, %5\n\tbuffer_load_dwordx4 %1, %2, %0 offen nt lds" : "=&s"(t) : "v"(voff), "s"(srd), "s"(sbase), "s"(lbase), "i"(simm), "i"(limm) : "memory", "scc");
  else asm volatile("s_add_u32 m0, %4, %6\n\ts_add_u32 %0, %3, %5\n\tbuffer_load_dwordx4 %1, %2, %0 offen lds" : "=&s"(t) : "v"(voff), "s"(srd), "s"(sbase), "s"(lbase), "i"(simm), "i"(limm) : "memory", "scc");
}
DI void dma16i_sc1(const i32x4& srd, unsigned voff, unsigned sbase, int simm, unsigned lbase, int limm) {
  unsigned t;
  asm volatile("s_add_u32 m0, %4, %6\n\ts_add_u32 %0, %3, %5\n\tbuffer_load_dwordx4 %1, %2, %0 offen sc1 lds" : "=&s"(t) : "v"(voff), "s"(srd), "s"(sbase), "s"(lbase), "i"(simm), "i"(limm) : "memory", "scc");
}
template <int NT = 0>
DI void ld16i(u32x4& d, const i32x4& srd, unsigned voff, unsigned sbase, int simm) {
  unsigned t;
  if (NT) asm volatile("s_add_u32 %1, %4, %5\n\tbuffer_load_dwordx4 %0, %2, %3, %1 offen nt" : "=v"(d), "=&s"(t) : "v"(voff), "s"(srd), "s"(sbase), "i"(simm) : "memory", "scc");
  else asm volatile("s_add_u32 %1, %4, %5\n\tbuffer_load_dwordx4 %0, %2, %3, %1 offen" : "=v"(d), "=&s"(t) : "v"(voff), "s"(srd), "s"(sbase), "i"(simm) : "memory", "scc");
}
template <int NT = 0>
DI void ld16(u32x4& d, const i32x4& srd, unsigned voff, unsigned soff) {
  if (NT) asm volatile("buffer_load_dwordx4 %0, %1, %2, %3 offen nt" : "=v"(d) : "v"(voff), "s"(srd), "s"(soff) : "memory");
  else asm volatile("buffer_load_dwordx4 %0, %1, %2, %3 offen" : "=v"(d) : "v"(voff), "s"(srd), "s"(soff) : "memory");
}
DI void ld16_sc1(u32x4& d, const i32x4& srd, unsigned voff, unsigned soff) { asm volatile("buffer_load_dwordx4 %0, %1, %2, %3 offen sc1" : "=v"(d) : "v"(voff), "s"(srd), "s"(soff) : "memory"); }
DI void ld8_sc1(u32x2& d, const i32x4& srd, unsigned voff, unsigned soff) { asm volatile("buffer_load_dwordx2 %0, %1, %2, %3 offen sc1" : "=v"(d) : "v"(voff), "s"(srd), "s"(soff) : "memory"); }
DI void ld8(u32x2& d, const i32x4& srd, unsigned voff, unsigned soff) { asm volatile("buffer_load_dwordx2 %0, %1, %2, %3 offen" : "=v"(d) : "v"(voff), "s"(srd), "s"(soff) : "memory"); }
DI void ld4_sc1(unsigned& d, const i32x4& srd, unsigned voff) { asm volatile("buffer_load_dword %0, %1, %2, 0 offen sc1" : "=v"(d) : "v"(voff), "s"(srd) : "memory"); }
DI void touchg(const void* base, unsigned voff, unsigned lds) {
  asm volatile("s_mov_b32 m0, %2\n\ts_nop 0\n\tglobal_load_lds_dword %0, %1" :: "v"(voff), "s"(base), "s"(lds) : "memory");
}
DI void touchg_nt(const void* base, unsigned voff, unsigned lds) {
  asm volatile("s_mov_b32 m0, %2\n\ts_nop 0\n\tglobal_load_lds_dword %0, %1 nt" :: "v"(voff), "s"(base), "s"(lds) : "memory");
}
DI void touch4(const i32x4& srd, unsigned voff, unsigned soff, unsigned lds) {
  asm volatile("s_mov_b32 m0, %3\n\ts_nop 0\n\tbuffer_load_dword %0, %1, %2 offen lds" :: "v"(voff), "s"(srd), "s"(soff), "s"(lds) : "memory");
}
DI void touch4_sc1(const i32x4& srd, unsigned voff, unsigned soff, unsigned lds) {
  asm volatile("s_mov_b32 m0, %3\n\ts_nop 0\n\tbuffer_load_dword %0, %1, %2 offen sc1 lds" :: "v"(voff), "s"(srd), "s"(soff), "s"(lds) : "memory");
}
#define WAITV(n) asm volatile("s_waitcnt vmcnt(%0)" :: "i"(n) : "memory")
#define BAR() asm volatile("s_waitcnt lgkmcnt(0)\n\ts_barrier" ::: "memory")
#define PIN(x) asm volatile("" : "+v"(x))
DI float b2f(u16 x) { return __uint_as_float(((unsigned)x) << 16); }
DI float blo(unsigned x) { return __uint_as_float(x << 16); }
DI float bhi(unsigned x) { return __uint_as_float(x & 0xffff0000u); }
DI unsigned pk2(float lo, float hi) { bf16x2 r = {(__bf16)lo, (__bf16)hi}; return __builtin_bit_cast(unsigned, r); }
DI float rbf(float x) { return (float)(__bf16)x; }
DI float dot2(unsigned a, unsigned b, float c) { return __builtin_amdgcn_fdot2_f32_bf16(__builtin_bit_cast(bf16x2, a), __builtin_bit_cast(bf16x2, b), c, false); }
template <int CTRL> DI float dpp_add(float x) { return x + __int_as_float(__builtin_amdgcn_update_dpp(0, __float_as_int(x), CTRL, 0xf, 0xf, true)); }
DI float row16_sum(float x) { x = dpp_add<0x128>(x); x = dpp_add<0x124>(x); x = dpp_add<0x122>(x); x = dpp_add<0x121>(x); return x; }
DI bf16x8 ldtr(LAS char* p0, LAS char* p1) {
  s16x4 a = __builtin_amdgcn_ds_read_tr16_b64_v4i16((LAS s16x4*)p0), b = __builtin_amdgcn_ds_read_tr16_b64_v4i16((LAS s16x4*)p1);
  return __builtin_bit_cast(bf16x8, __builtin_shufflevector(a, b, 0, 1, 2, 3, 4, 5, 6, 7));
}
DI void poll_all(const char* ws, unsigned off, unsigned tag) {
  gu32* a = (gu32*)(ws + off);
#pragma unroll 1
  for (unsigned it = 0; it < SPIN_MAX; ++it) {
    const unsigned v = __hip_atomic_load(a, __ATOMIC_RELAXED, __HIP_MEMORY_SCOPE_AGENT);
    if (__all(v == tag)) break;
    __builtin_amdgcn_s_sleep(2);
  }
  asm volatile("" ::: "memory");
}
DI void flag_set(const char* ws, unsigned off, unsigned tag, int lane) {
  if (lane == 0) __hip_atomic_store((gu32*)(ws + off), tag, __ATOMIC_RELAXED, __HIP_MEMORY_SCOPE_AGENT);
}
#define STAMP_CY(idx) do {} while (0)
#define STAMP_AT(idx) do {} while (0)
#define STAMP_INIT() do {} while (0)
#define STAMP_FLUSH() do {} while (0)

template <int NX, class Hook>
DI void task_kv(const Params& p, int cg, int tb, int lane, Hook hook) {
  const int c0 = cg * 64 + 8 * (lane & 7), t0 = tb * 64 + 8 * (lane >> 3);
  const i32x4 rk = make_srd(p.k, S_ * D_ * 2), rv = make_srd(p.v, S_ * D_ * 2), rsk = make_srd(p.sk, D_ * 8);
  u32x4 kk[11], vv[8], ss[4];
#pragma unroll
  for (int j = 0; j < 4; ++j) ld16(ss[j], rsk, (unsigned)(c0 * 8), j * 16);
#pragma unroll
  for (int j = 0; j < 11; ++j) { const int t = t0 - 3 + j; ld16<NT_KV>(kk[j], rk, t >= 0 ? (unsigned)(t * 1280 + c0 * 2) : 0xfffffff0u, 0); }
#pragma unroll
  for (int j = 0; j < 8; ++j) ld16<NT_KV>(vv[j], rv, (unsigned)(t0 * 1280 + c0 * 2), j * 1280);
  hook();
  const __amdgpu_buffer_rsrc_t ro = __builtin_amdgcn_make_buffer_rsrc((void*)(p.ws + WS_KVT), 0, D_ * S_ * 2, 0x00020000);
  WAITV(NX);
  STAMP_AT(12);
#pragma unroll
  for (int j = 0; j < 4; ++j) PIN(ss[j]);
#pragma unroll
  for (int j = 0; j < 11; ++j) PIN(kk[j]);
#pragma unroll
  for (int j = 0; j < 8; ++j) PIN(vv[j]);
#pragma unroll
  for (int cp = 0; cp < 4; ++cp) {
    const f32x2 s0 = {blo(ss[cp].x), blo(ss[cp].z)}, s1 = {bhi(ss[cp].x), bhi(ss[cp].z)}, s2 = {blo(ss[cp].y), blo(ss[cp].w)}, s3 = {bhi(ss[cp].y), bhi(ss[cp].w)};
    f32x2 kx[11];
#pragma unroll
    for (int j = 0; j < 11; ++j) { const unsigned w = kk[j][cp]; kx[j] = (f32x2){blo(w), bhi(w)}; }
    f32x2 o[8];
    __builtin_amdgcn_sched_barrier(0);
#pragma unroll
    for (int j = 0; j < 8; ++j) o[j] = kx[j] * s3;
    __builtin_amdgcn_sched_barrier(0);
#pragma unroll
    for (int j = 0; j < 8; ++j) o[j] = kx[j + 1] * s2 + o[j];
    __builtin_amdgcn_sched_barrier(0);
#pragma unroll
    for (int j = 0; j < 8; ++j) o[j] = kx[j + 2] * s1 + o[j];
    __builtin_amdgcn_sched_barrier(0);
#pragma unroll
    for (int j = 0; j < 8; ++j) o[j] = kx[j + 3] * s0 + o[j];
    __builtin_amdgcn_sched_barrier(0);
    f32x2 vx[8];
#pragma unroll
    for (int j = 0; j < 8; ++j) { const unsigned w = vv[j][cp]; vx[j] = (f32x2){blo(w), bhi(w)}; }
    __builtin_amdgcn_sched_barrier(0);
#pragma unroll
    for (int j = 0; j < 8; ++j) o[j] = o[j] * vx[j];
    __builtin_amdgcn_sched_barrier(0);
    const u32x4 r0 = {pk2(o[7].x, o[6].x), pk2(o[5].x, o[4].x), pk2(o[3].x, o[2].x), pk2(o[1].x, o[0].x)};
    const u32x4 r1 = {pk2(o[7].y, o[6].y), pk2(o[5].y, o[4].y), pk2(o[3].y, o[2].y), pk2(o[1].y, o[0].y)};
    __builtin_amdgcn_raw_buffer_store_b128(r0, ro, (unsigned)((c0 + 2 * cp) * 4096 + (2040 - t0) * 2), 0, 16);
    __builtin_amdgcn_raw_buffer_store_b128(r1, ro, (unsigned)((c0 + 2 * cp + 1) * 4096 + (2040 - t0) * 2), 0, 16);
  }
}
template <bool PRE>
DI void task_stats(const Params& p, int id, int lane) {
  const i32x4 rq = make_srd(p.q, S_ * D_ * 2);
  const int row = id * 4 + (lane >> 4), l16 = lane & 15;
  u32x4 x[5];
#pragma unroll
  for (int j = 0; j < 5; ++j) ld16<NT_G1>(x[j], rq, (unsigned)row * 1280u + l16 * 16, j * 256);
  const __amdgpu_buffer_rsrc_t rs = __builtin_amdgcn_make_buffer_rsrc((void*)(p.ws + WS_STATS), 0, S_ * 8, 0x00020000);
  if (PRE) BAR();
  WAITV(0);
  STAMP_AT(12);
#pragma unroll
  for (int j = 0; j < 5; ++j) PIN(x[j]);
  float s4[4] = {0.f, 0.f, 0.f, 0.f};
#pragma unroll
  for (int j = 0; j < 5; ++j)
#pragma unroll
    for (int e = 0; e < 4; ++e) s4[e] = dot2(x[j][e], 0x3f803f80u, s4[e]);
  const float mean = row16_sum((s4[0] + s4[1]) + (s4[2] + s4[3])) * (1.0f / 640.0f);
  f32x2 q2[2] = {{0.f, 0.f}, {0.f, 0.f}};
#pragma unroll
  for (int j = 0; j < 5; ++j)
#pragma unroll
    for (int e = 0; e < 4; ++e) { const unsigned w = x[j][e]; const f32x2 d = (f32x2){blo(w), bhi(w)} - mean; q2[e & 1] = d * d + q2[e & 1]; }
  const float var = row16_sum((q2[0].x + q2[0].y) + (q2[1].x + q2[1].y)) * (1.0f / 640.0f);
  const float mb = rbf(mean), sb = rbf(__builtin_amdgcn_sqrtf(rbf(rbf(var) + 1e-5f)));
  u32x2 o; o.x = __float_as_uint(mb); o.y = __float_as_uint(1.0f / sb);
  __builtin_amdgcn_raw_buffer_store_b64(o, rs, l16 == 0 ? (unsigned)row * 8u : 0xfffffff0u, 0, 16);
}

struct ConvIn { u32x4 L0[4], L1[4], L2[4], U[4]; unsigned dv; };
DI void conv_load_h(const Params& p, ConvIn& in, int c, int lane) {
  const i32x4 rh = make_srd(p.h, D_ * S_ * 2), rd = make_srd(p.dv, D_ * 2);
  const unsigned vo = lane * 16, so = c * 4096u;
  asm volatile("buffer_load_ushort %0, %1, %2, 0 offen" : "=v"(in.dv) : "v"(c * 2u), "s"(rd) : "memory");
#pragma unroll
  for (int i = 0; i < 4; ++i) { ld16<NT_H>(in.L0[i], rh, vo, so + i * 1024); ld16(in.L1[i], rh, vo + 4, so + i * 1024); ld16(in.L2[i], rh, vo + 8, so + i * 1024); }
}
DI void conv_load_u(const Params& p, ConvIn& in, int c, int lane) {
  const i32x4 ru = make_srd(p.ws + WS_KVT, D_ * S_ * 2);
#pragma unroll
  for (int i = 0; i < 4; ++i) ld16_sc1(in.U[i], ru, lane * 16, c * 4096u + i * 1024);
}
DI void conv_pin(ConvIn& in) {
#pragma unroll
  for (int i = 0; i < 4; ++i) { PIN(in.L0[i]); PIN(in.L1[i]); PIN(in.L2[i]); PIN(in.U[i]); }
  PIN(in.dv);
}
DI void conv_build(const ConvIn& in, LAS char* cb, int lane) {
#pragma unroll
  for (int i = 0; i < 4; ++i) {
    const unsigned o = (i * 64 + lane + 2) * 16;
    u32x4 c0, c2;
#pragma unroll
    for (int e = 0; e < 4; ++e) { c0[e] = __builtin_amdgcn_alignbit(in.L1[i][e], in.L0[i][e], 16); c2[e] = __builtin_amdgcn_alignbit(in.L2[i][e], in.L1[i][e], 16); }
    *(LAS u32x4*)(cb + 0 * CV_H + o) = c0;
    *(LAS u32x4*)(cb + 1 * CV_H + o) = in.L1[i];
    *(LAS u32x4*)(cb + 2 * CV_H + o) = c2;
    *(LAS u32x4*)(cb + 3 * CV_H + o) = in.L2[i];
    *(LAS u32x4*)(cb + CV_U + (i * 64 + lane) * 16) = in.U[i];
  }
  const u32x4 z4 = {0u, 0u, 0u, 0u};
  if (lane < 30) *(LAS u32x4*)(cb + CV_U + 4096 + lane * 16) = z4;
  if (lane == 0) {
    const unsigned w0 = in.L0[0].x, w1 = in.L0[0].y;
#pragma unroll
    for (int cc = 0; cc < 4; ++cc) *(LAS u32x4*)(cb + cc * CV_H) = z4;
    *(LAS u32x4*)(cb + 0 * CV_H + 16) = (u32x4){0u, 0u, 0u, w0 << 16};
    *(LAS u32x4*)(cb + 1 * CV_H + 16) = (u32x4){0u, 0u, 0u, w0};
    *(LAS u32x4*)(cb + 2 * CV_H + 16) = (u32x4){0u, 0u, w0 << 16, __builtin_amdgcn_alignbit(w1, w0, 16)};
    *(LAS u32x4*)(cb + 3 * CV_H + 16) = (u32x4){0u, 0u, w0, w1};
  }
}
template <int TRIPS>
DI void conv_mfma(LAS char* cb, int d0, int lane, f32x4 (&acc)[8]) {
  const int r = lane & 15, kg = lane >> 4, kap = kg >> 1, bp = kg & 1;
  unsigned a0 = (unsigned)(size_t)cb + (r & 3) * CV_H + 32 * kap + 16 * bp + 2 * (r & 12) + d0 * 64;
  unsigned a1 = a0 + 8;
  unsigned ub = (unsigned)(size_t)cb + CV_U + (4064 - 3584) - 32 * r + 32 * kap + 16 * bp + d0 * 64;
  PIN(a0); PIN(a1); PIN(ub);
#pragma unroll
  for (int g = 0; g < 8; ++g) acc[g] = (f32x4){0.f, 0.f, 0.f, 0.f};
  bf16x8 A[8], B[8];
#define CV_LDA(g) do { const s16x4 x0_ = *(LAS s16x4*)(size_t)(a0 + (g) * 512), x1_ = *(LAS s16x4*)(size_t)(a1 + (g) * 512); \
    A[g] = __builtin_bit_cast(bf16x8, __builtin_shufflevector(x0_, x1_, 0, 1, 2, 3, 4, 5, 6, 7)); } while (0)
#define CV_LDB(g) do { B[g] = *(LAS bf16x8*)(size_t)(ub + (7 - (g)) * 512); } while (0)
#pragma unroll
  for (int g = 0; g < 8; ++g) { CV_LDA(g); CV_LDB(7 - g); }
#pragma unroll 1
  for (int d = 0; d < TRIPS; ++d) {
    a0 += 64; a1 += 64; ub += 64; PIN(a0); PIN(a1); PIN(ub);
#pragma unroll
    for (int gm = 0; gm < 8; ++gm) {
#pragma unroll
      for (int gp = 7 - gm; gp >= 0; --gp) acc[gm + gp] = __builtin_amdgcn_mfma_f32_16x16x32_bf16(A[gm], B[gp], acc[gm + gp], 0, 0, 0);
      __builtin_amdgcn_sched_barrier(0);
      CV_LDA(gm); CV_LDB(7 - gm);
      __builtin_amdgcn_sched_barrier(0);
    }
  }
}
DI void conv_store(const Params& p, LAS char* cb, int c, int lane, unsigned dvb, const f32x4 (&acc)[8]) {
  const int r = lane & 15, kg = lane >> 4;
  const float dv = __uint_as_float(dvb << 16);
  const __amdgpu_buffer_rsrc_t rz = __builtin_amdgcn_make_buffer_rsrc((void*)(p.ws + WS_ZT), 0, D_ * S_ * 2, 0x00020000);
  u32x2 z[8];
#pragma unroll
  for (int g = 0; g < 8; ++g) {
    const int t = 256 * g + 16 * r + 4 * kg;
    const u32x2 kv = *(LAS u32x2*)(cb + CV_U + (2044 - t) * 2);
    const float y0 = fmaf(bhi(kv.y), dv, acc[g][0]), y1 = fmaf(blo(kv.y), dv, acc[g][1]), y2 = fmaf(bhi(kv.x), dv, acc[g][2]), y3 = fmaf(blo(kv.x), dv, acc[g][3]);
    z[g].x = pk2(y0, y1); z[g].y = pk2(y2, y3);
  }
#pragma unroll
  for (int g = 0; g < 8; g += 2) {
    const auto s0 = __builtin_amdgcn_permlane16_swap(z[g].x, z[g + 1].x, false, false);
    const auto s1 = __builtin_amdgcn_permlane16_swap(z[g].y, z[g + 1].y, false, false);
    const u32x4 o = {s0[0], s1[0], s0[1], s1[1]};
    const int gg = g + (kg & 1);
    __builtin_amdgcn_raw_buffer_store_b128(o, rz, c * 4096u + (256 * gg + 16 * r + 4 * (kg & 2)) * 2, 0, 16);
  }
}
struct ConvHalf { u32x4 La[4], Lb[4], U[2]; unsigned dv; };
DI void convh_load_h(const Params& p, ConvHalf& in, int c, int half, int lane) {
  const i32x4 rh = make_srd(p.h, D_ * S_ * 2), rd = make_srd(p.dv, D_ * 2);
  const unsigned vo = lane * 16 + half * 4, so = c * 4096u;
  asm volatile("buffer_load_ushort %0, %1, %2, 0 offen" : "=v"(in.dv) : "v"(c * 2u), "s"(rd) : "memory");
#pragma unroll
  for (int i = 0; i < 4; ++i) { ld16<NT_H>(in.La[i], rh, vo, so + i * 1024); ld16(in.Lb[i], rh, vo + 4, so + i * 1024); }
}
DI void convh_load_u(const Params& p, ConvHalf& in, int c, int half, int lane) {
  const i32x4 ru = make_srd(p.ws + WS_KVT, D_ * S_ * 2);
#pragma unroll
  for (int i = 0; i < 2; ++i) ld16_sc1(in.U[i], ru, lane * 16, c * 4096u + (half * 2 + i) * 1024);
}
DI void convh_build(ConvHalf& in, LAS char* cb, int half, int lane) {
#pragma unroll
  for (int i = 0; i < 4; ++i) { PIN(in.La[i]); PIN(in.Lb[i]); }
  PIN(in.U[0]); PIN(in.U[1]); PIN(in.dv);
  LAS char* hb = cb + 2 * half * CV_H;
#pragma unroll
  for (int i = 0; i < 4; ++i) {
    const unsigned o = (i * 64 + lane + 2) * 16;
    u32x4 c0;
#pragma unroll
    for (int e = 0; e < 4; ++e) c0[e] = __builtin_amdgcn_alignbit(in.Lb[i][e], in.La[i][e], 16);
    *(LAS u32x4*)(hb + o) = c0;
    *(LAS u32x4*)(hb + CV_H + o) = in.Lb[i];
  }
#pragma unroll
  for (int i = 0; i < 2; ++i) *(LAS u32x4*)(cb + CV_U + ((half * 2 + i) * 64 + lane) * 16) = in.U[i];
  if (half == 0) {
    const u32x4 z4 = {0u, 0u, 0u, 0u};
    if (lane < 30) *(LAS u32x4*)(cb + CV_U + 4096 + lane * 16) = z4;
    if (lane == 0) {
      const unsigned w0 = in.La[0].x, w1 = in.La[0].y;
#pragma unroll
      for (int cc = 0; cc < 4; ++cc) *(LAS u32x4*)(cb + cc * CV_H) = z4;
      *(LAS u32x4*)(cb + 0 * CV_H + 16) = (u32x4){0u, 0u, 0u, w0 << 16};
      *(LAS u32x4*)(cb + 1 * CV_H + 16) = (u32x4){0u, 0u, 0u, w0};
      *(LAS u32x4*)(cb + 2 * CV_H + 16) = (u32x4){0u, 0u, w0 << 16, __builtin_amdgcn_alignbit(w1, w0, 16)};
      *(LAS u32x4*)(cb + 3 * CV_H + 16) = (u32x4){0u, 0u, w0, w1};
    }
  }
}

DI void g2_issue_w(const i32x4& srdD, const unsigned (&vd)[5], unsigned smb, int nt, int w, int j) {
  const unsigned sb = nt * 160u + (j < 13 ? w : (w < 2 ? w : 1)) * 40960u, lb = smb + L_WR + w * 5120;
#pragma unroll
  for (int i = 0; i < 5; ++i) dma16i<NT_WD>(srdD, vd[i], sb, j * 163840, lb, (j & 3) * G2_WSLOT + i * 1024);
}
DI void g2_wd_offsets(unsigned (&vd)[5], int lane) {
#pragma unroll
  for (int i = 0; i < 5; ++i) { const int idx = i * 64 + lane, rho = idx / 10, ch = idx - rho * 10; vd[i] = rho * 1280 + ch * 16; }
}
template <bool CONV>
DI void g1_loader(KP kp, Params p, unsigned long long pc0, LAS char* sm, unsigned smb, int rb, int ft, int mt, int nt, int hw, int lane, unsigned& tag, int cch, unsigned& dvb, u32x4 (&wy)[3][5]) {
  const i32x4 srdQ = make_srd(p.q, S_ * D_ * 2), srdW = make_srd((hw >> 1) ? p.wu : p.wg, D_ * F_ * 2), srdC = make_srd(p.ws, WS_KVT);
  const unsigned vq0 = (lane >> 3) * 1280 + (((lane & 7) ^ (lane >> 4)) * 16);
  const unsigned vq1 = (lane >> 3) * 1280 + (((lane & 7) ^ (4 + (lane >> 4))) * 16);
  const unsigned vw0 = (lane >> 3) * 3456 + (((lane & 7) ^ (2 * ((lane >> 4) & 1))) * 16);
  const unsigned vw1 = (lane >> 3) * 3456 + (((lane & 7) ^ (2 * (((lane >> 4) & 1) | 2))) * 16);
  const unsigned qb = (unsigned)(rb * 256 + 64 * hw) * 1280u, wb = (unsigned)(32 * (hw & 1)) * 3456u + ft * 128u;
  const unsigned lq = smb + hw * 8192, lw = smb + 32768 + (hw >> 1) * 8192 + (hw & 1) * 4096;
  auto issue = [&](int s, int slot) {
#pragma unroll
    for (int i = 0; i < 4; ++i) dma16i<NT_G1>(srdW, (i & 1) ? vw1 : vw0, wb, (s * 64 + 8 * i) * 3456, lw, slot * G1_SLOT + i * 1024);
#pragma unroll
    for (int i = 0; i < 8; ++i) dma16i<NT_G1>(srdQ, (i & 1) ? vq1 : vq0, qb, i * 10240 + s * 128, lq, slot * G1_SLOT + i * 1024);
  };
  const int xq = blockIdx.x & 7, wv = (blockIdx.x >> 3) * 4 + hw;
  const unsigned junk = smb + L_END - 256;
  const i32x4 srdD = make_srd(p.wd, F_ * D_ * 2);
  auto touch_slot = [&](int sl) {
    const int g = wv >> 5, sub = wv & 31, gsel = (sl + 1) % 3, st = sl + 3;
    unsigned fo = (((27 * (xq & 3)) >> 2) + (lane & 7)) * 128u; fo = fo < 3456u ? fo : 3328u;
    const unsigned vt = (unsigned)((xq >> 2) * 1024 + (sub & 15) * 64 + lane) * 1280u + st * 128u;
    const unsigned vw = (unsigned)(st * 64 + ((sub & 15) >> 1) * 8 + (lane >> 3)) * 3456u + fo;
    const bool stage = TOUCH && g == gsel;
    const void* base = (!stage || sub < 16) ? (const void*)p.q : ((sub & 1) ? (const void*)p.wu : (const void*)p.wg);
    touchg(base, stage ? (sub < 16 ? vt : vw) : 0u, junk);
  };
  unsigned ep; ld4_sc1(ep, srdC, CW_EPOCH);
  BAR();
  ConvHalf in;
  issue(0, 0); issue(1, 1);
  if (CONV) convh_load_h(p, in, cch, hw & 1, lane);
  touch_slot(-1);
  if (CONV) WAITV(22); else WAITV(13);
  PIN(ep); tag = __builtin_amdgcn_readfirstlane(ep) + 1u;
  if ((hw & 1) == 0 && lane == 0) *(LAS unsigned*)(sm + L_PF + (hw >> 1) * 4) = ~tag;
  BAR();
  if (hw == 0) flag_set(p.ws, CW_ARRIVE + blockIdx.x * 4, tag, lane);
  issue(2, 2); touch_slot(0);
  unsigned vd[5]; g2_wd_offsets(vd, lane);
  unsigned f1 = 0, f2 = 0; u32x2 st, st2;
  const unsigned f1off = lane < 16 ? CW_ST + (rb * 64 + hw * 16 + lane) * 16 : CW_ST + (mt * 16 + (lane & 15)) * 16;
  const unsigned f2off = CW_KV + ((CONV ? (cch >> 6) : 0) * 32 + (lane & 31)) * 16;
#pragma unroll
  for (int s = 1; s < 10; ++s) {
    const int nslot = (s + 2) % 3;
    if (s == 1) { if (CONV) WAITV(23); else WAITV(14); }
    else if (s == 6) WAITV(15);
    else if (s == 8) { if (CONV) WAITV(16); else WAITV(15); }
    else if (s == 9) WAITV(10);
    else WAITV(14);
    BAR();
    if (s == 4 || s == 7) p = reload(kp);
    if (s == 5) ld4_sc1(f1, srdC, f1off);
    if (s == 7) {
      PIN(f1);
      if (!__all(f1 == tag)) poll_all(p.ws, f1off, tag);
      ld8_sc1(st, srdC, WS_STATS + (rb * 256 + hw * 64 + lane) * 8, 0);
      ld8_sc1(st2, srdC, WS_STATS + (mt * 64 + lane) * 8, 0);
      if (CONV) ld4_sc1(f2, srdC, f2off);
    }
    if (s <= 7) issue(s + 2, nslot);
    if (s == 8) { g2_issue_w(srdD, vd, smb, nt, hw, 0); g2_issue_w(srdD, vd, smb, nt, hw, 1); }
    if (s <= 6) touch_slot(s);
    if (s == 9) {
      if (CONV) {
        PIN(f2);
        if (!__all(f2 == tag)) poll_all(p.ws, f2off, tag);
        convh_load_u(p, in, cch, hw & 1, lane);
      }
      PIN(st); PIN(st2);
      *(LAS u32x2*)(sm + L_ST + (hw * 64 + lane) * 8) = st;
      if (hw == 0) *(LAS u32x2*)(sm + L_S2 + lane * 8) = st2;
    }
  }
  BAR();
#pragma unroll
  for (int j = 0; j < 3; ++j)
#pragma unroll
    for (int i = 0; i < 5; ++i) ld16i<NT_WD>(wy[j][i], srdD, vd[i], nt * 160u + hw * 40960u, (j + 2) * 163840);
  if (CONV) { WAITV(15); convh_build(in, sm + L_CVG + (hw >> 1) * CV_SZ, hw & 1, lane); dvb = in.dv; }
  WAITV(0);
#pragma unroll
  for (int j = 0; j < 3; ++j)
#pragma unroll
    for (int i = 0; i < 5; ++i) PIN(wy[j][i]);
}

template <bool SWAP>
DI void g1_epilogue(const Params& p, LAS char* sm, int rb, int ft, int wr, int wc, int lane, const f32x4 (&a1)[2][8], const f32x4 (&a2)[2][8], const f32x4 (&csg)[2], const f32x4 (&csu)[2], const f32x2 (&st)[8]) {
  const __amdgpu_buffer_rsrc_t rh = __builtin_amdgcn_make_buffer_rsrc((void*)(p.ws + WS_HM), 0, 54 * 128 * 1024, 0x00020000);
  const unsigned hb = (unsigned)((ft * 2 + wc) * 128 + rb * 16 + wr * 8) * 1024u + lane * 16;
#pragma unroll
  for (int m = 0; m < 8; ++m) {
    asm volatile("; gated epilogue, operand order %0" :: "i"(SWAP ? 1 : 0));
    const float nm = -st[m].x, rs = st[m].y, rsl = rs * -1.44269504089f, rs2 = rs * rs;
    f32x2 tg[4], tu[4], e[4];
#pragma unroll
    for (int h = 0; h < 4; ++h) {
      const int a = h >> 1, i = (h & 1) * 2;
      const f32x4 ga = SWAP ? a2[a][m] : a1[a][m], ua = SWAP ? a1[a][m] : a2[a][m];
      tg[h] = (f32x2){csg[a][i], csg[a][i + 1]} * nm + (f32x2){ga[i], ga[i + 1]};
      tu[h] = (f32x2){csu[a][i], csu[a][i + 1]} * nm + (f32x2){ua[i], ua[i + 1]};
    }
#pragma unroll
    for (int h = 0; h < 4; ++h) { const f32x2 gl = tg[h] * rsl; e[h].x = __builtin_amdgcn_exp2f(gl.x); e[h].y = __builtin_amdgcn_exp2f(gl.y); }
#pragma unroll
    for (int h = 0; h < 4; ++h) { tu[h] = (tg[h] * tu[h]) * rs2; e[h] = e[h] + 1.0f; }
#pragma unroll
    for (int h = 0; h < 4; ++h) { e[h].x = __builtin_amdgcn_rcpf(e[h].x); e[h].y = __builtin_amdgcn_rcpf(e[h].y); }
    u32x4 w;
#pragma unroll
    for (int h = 0; h < 4; ++h) { const f32x2 hv = tu[h] * e[h]; w[h] = pk2(hv.x, hv.y); }
    __builtin_amdgcn_raw_buffer_store_b128(w, rh, hb + m * 1024, 0, 16);
  }
}
DI void g1_math(const Params& p, LAS char* sm, int rb, int ft, int wid, int lane, unsigned tag, unsigned taskflag) {
  const int wr = wid >> 1, wc = wid & 1;
  const int n = lane & 15, kg = lane >> 4, q4 = (lane & 15) >> 2, pp = lane & 3;
  f32x4 a1[2][8], a2[2][8], cs[2];
#pragma unroll
  for (int a = 0; a < 2; ++a) {
    cs[a] = (f32x4){0.f, 0.f, 0.f, 0.f};
#pragma unroll
    for (int m = 0; m < 8; ++m) { a1[a][m] = (f32x4){0.f, 0.f, 0.f, 0.f}; a2[a][m] = (f32x4){0.f, 0.f, 0.f, 0.f}; }
  }
  u32x4 ones_ = {0x3f803f80u, 0x3f803f80u, 0x3f803f80u, 0x3f803f80u};
  PIN(ones_);
  const bf16x8 ones = __builtin_bit_cast(bf16x8, ones_);
  const unsigned tbase = (wr * 128 + n) * 128 + ((kg ^ ((n >> 1) & 7)) * 16);
  const int sw = 2 * (((q4 >> 1) & 1) | ((kg & 1) << 1));
  const unsigned wb0 = 32768 + (8 * kg + q4) * 128 + (((4 * wc + 0 + (pp >> 1)) ^ sw) * 16) + 8 * (pp & 1);
  const unsigned wb1 = 32768 + (8 * kg + q4) * 128 + (((4 * wc + 2 + (pp >> 1)) ^ sw) * 16) + 8 * (pp & 1);
  const unsigned o1 = wr * 8192, o2 = 8192 - o1;
  bf16x8 tP[8], tQ[8], gP[2], gQ[2], uP[2], uQ[2];
#define G1_RD(T, G, U, so, ks) do { LAS char* tp_ = sm + (so) + (tbase ^ ((ks) * 64)); \
    LAS char* w10_ = sm + (so) + wb0 + o1 + (ks) * 4096; LAS char* w11_ = sm + (so) + wb1 + o1 + (ks) * 4096; \
    LAS char* w20_ = sm + (so) + wb0 + o2 + (ks) * 4096; LAS char* w21_ = sm + (so) + wb1 + o2 + (ks) * 4096; \
    G[0] = ldtr(w10_, w10_ + 512); T[0] = *(LAS bf16x8*)(tp_); U[0] = ldtr(w20_, w20_ + 512); G[1] = ldtr(w11_, w11_ + 512); U[1] = ldtr(w21_, w21_ + 512); \
    _Pragma("unroll") for (int m = 1; m < 8; ++m) T[m] = *(LAS bf16x8*)(tp_ + 2048 * m); } while (0)
#define G1_MM(T, G, U) do { _Pragma("unroll") for (int m = 0; m < 8; ++m) { _Pragma("unroll") for (int a = 0; a < 2; ++a) { \
    a1[a][m] = __builtin_amdgcn_mfma_f32_16x16x32_bf16(G[a], T[m], a1[a][m], 0, 0, 0); a2[a][m] = __builtin_amdgcn_mfma_f32_16x16x32_bf16(U[a], T[m], a2[a][m], 0, 0, 0); } } } while (0)
#define G1_CS(G) do { cs[0] = __builtin_amdgcn_mfma_f32_16x16x32_bf16(G[0], ones, cs[0], 0, 0, 0); cs[1] = __builtin_amdgcn_mfma_f32_16x16x32_bf16(G[1], ones, cs[1], 0, 0, 0); } while (0)
#define G1_SCHED() do { _Pragma("unroll") for (int i_ = 0; i_ < 16; ++i_) { __builtin_amdgcn_sched_group_barrier(0x8, 1, 0); __builtin_amdgcn_sched_group_barrier(0x100, 1, 0); } __builtin_amdgcn_sched_group_barrier(0x8, 18, 0); } while (0)
  unsigned so = 0;
  BAR();
  STAMP_AT(1); STAMP_CY(14);
  G1_RD(tP, gP, uP, so, 0);
  G1_RD(tQ, gQ, uQ, so, 1); G1_MM(tP, gP, uP); G1_CS(gP); G1_SCHED();
#pragma unroll 1
  for (int s = 1; s < 10; ++s) {
    so = so == 2 * G1_SLOT ? 0 : so + G1_SLOT;
    BAR();
    if (s == 2 && taskflag) { WAITV(0); flag_set(p.ws, taskflag, tag, lane); }
    G1_RD(tP, gP, uP, so, 0); G1_MM(tQ, gQ, uQ); G1_CS(gQ); G1_SCHED();
    G1_RD(tQ, gQ, uQ, so, 1); G1_MM(tP, gP, uP); G1_CS(gP); G1_SCHED();
  }
  G1_CS(gQ);
  if (n == 0) { *(LAS f32x4*)(sm + L_CS + (wr * 64 + wc * 32 + 4 * kg) * 4) = cs[0]; *(LAS f32x4*)(sm + L_CS + (wr * 64 + wc * 32 + 16 + 4 * kg) * 4) = cs[1]; }
  BAR();
  f32x4 csg[2], csu[2];
#pragma unroll
  for (int a = 0; a < 2; ++a) { csg[a] = *(LAS f32x4*)(sm + L_CS + (wc * 32 + 16 * a + 4 * kg) * 4); csu[a] = *(LAS f32x4*)(sm + L_CS + (64 + wc * 32 + 16 * a + 4 * kg) * 4); }
  f32x2 st[8];
#pragma unroll
  for (int m = 0; m < 8; ++m) st[m] = *(LAS f32x2*)(sm + L_ST + (wr * 128 + 16 * m + n) * 8);
  G1_MM(tQ, gQ, uQ);
  STAMP_AT(2); STAMP_CY(15);
  if (wr == 0) g1_epilogue<false>(p, sm, rb, ft, wr, wc, lane, a1, a2, csg, csu, st);
  else g1_epilogue<true>(p, sm, rb, ft, wr, wc, lane, a1, a2, csg, csu, st);
  STAMP_AT(3);
}

template <int NPRE> constexpr int g2_w_after(int j) { return (j >= 1 && j + 3 <= 13 && j + 3 >= NPRE) ? 5 : 0; }
template <int NPRE> constexpr int g2_ops_after(int j) { return (j == 5 ? 1 : 0) + (j == 7 ? 3 : 0) + ((j >= 1 && j + 2 <= 13) ? 4 : 0) + g2_w_after<NPRE>(j); }
template <int NPRE> constexpr int g2_wait(int j) {
  return j == 0 ? 4 : (j == 1 ? g2_ops_after<NPRE>(1) : g2_w_after<NPRE>(j - 1) + g2_ops_after<NPRE>(j));
}
template <bool ISP>
DI void g2_loader(const Params& p, LAS char* sm, unsigned smb, int mt, int nt, int w, int lane, unsigned tag, unsigned hmflag) {
  const i32x4 srdD = make_srd(p.wd, F_ * D_ * 2), srdH = make_srd(p.ws + WS_HM, 54 * 128 * 1024), srdC = make_srd(p.ws, WS_KVT);
  unsigned vd[5]; g2_wd_offsets(vd, lane);
  const unsigned vh = lane * 16;
  auto fbof = [&](int j) { const int fb = 4 * j + w; return fb < 54 ? fb : 53; };
  auto issueH = [&](int j) {
    const unsigned sb = mt * 4096u + (j < 13 ? w : (w < 2 ? w : 1)) * 131072u, lb = smb + L_HR + w * 4096;
#pragma unroll
    for (int i = 0; i < 4; ++i) dma16i_sc1(srdH, vh, sb, j * 524288 + i * 1024, lb, (j % 3) * 16384 + i * 1024);
  };
  constexpr int XB = ISP ? 1 : 5, NREG = ISP ? 9 : 7, NPRE = XB + NREG;
  u32x4 wreg[NREG][5];
  if (ISP) g2_issue_w(srdD, vd, smb, nt, w, 0);
  else BAR();
#pragma unroll
  for (int j = 0; j < NREG; ++j) {
#pragma unroll
    for (int i = 0; i < 5; ++i) ld16i<NT_WD>(wreg[j][i], srdD, vd[i], nt * 160u + w * 40960u, (j + XB) * 163840);
    if (!ISP && j == 0) {
      WAITV(5);
      flag_set(p.ws, hmflag, tag, lane);
      STAMP_AT(4);
    }
  }
  {
    const unsigned junk = smb + L_END - 256;
#pragma unroll
    for (int r = 0; r < (14 - NPRE + 3) / 4; ++r) {
      const int idx = r * 64 + lane;
      int j = NPRE + (idx >> 4); j = j < 14 ? j : 13;
      touch4(srdD, (unsigned)(fbof(j) * 32 + (mt & 3) * 8 + ((idx & 15) >> 1)) * 1280u + nt * 160u + (idx & 1) * 128u, 0, junk);
    }
  }
  if (ISP && w == 0) {
    poll_all(p.ws, CW_ST + (mt * 16 + (lane & 15)) * 16, tag);
    u32x2 sv; ld8_sc1(sv, srdC, WS_STATS + (mt * 64 + lane) * 8, 0);
    WAITV(0); PIN(sv);
    *(LAS u32x2*)(sm + L_S2 + lane * 8) = sv;
  }
  if (ISP && blockIdx.x == 255 && w == 3) {
    poll_all(p.ws, CW_ARRIVE + lane * 4, tag); poll_all(p.ws, CW_ARRIVE + 256 + lane * 4, tag);
    poll_all(p.ws, CW_ARRIVE + 512 + lane * 4, tag); poll_all(p.ws, CW_ARRIVE + 768 + lane * 4, tag);
    flag_set(p.ws, CW_EPOCH, tag, lane);
  }
  { const int j = lane < 14 ? lane : 0; poll_all(p.ws, CW_HM + (((mt >> 2) * 2 + ((mt >> 1) & 1)) * 54 + fbof(j)) * 16, tag); }
  STAMP_AT(5);
  { const int j = (lane >> 2) < 14 ? (lane >> 2) : 13;
    touch4_sc1(srdH, (unsigned)(fbof(j) * 128 + mt * 4 + (lane & 3)) * 1024u + nt * 128u, 0, smb + L_END - 256); }
  issueH(0); issueH(1); issueH(2);
  WAITV(8);
#pragma unroll
  for (int j = 0; j < NREG; ++j)
#pragma unroll
    for (int i = 0; i < 5; ++i) PIN(wreg[j][i]);
  BAR();
  STAMP_AT(6);
  unsigned zf = 0; u32x4 zr[3];
  const unsigned zoff = CW_Z + (nt * 80 + w * 20 + (lane < 20 ? lane : 0)) * 16;
  auto put_w = [&](int j) {
#pragma unroll
    for (int i = 0; i < 5; ++i) *(LAS u32x4*)(sm + L_WR + (j & 3) * G2_WSLOT + w * 5120 + i * 1024 + lane * 16) = wreg[j - XB][i];
  };
#pragma unroll
  for (int j = 0; j < 14; ++j) {
    if (j == 0) {
#pragma unroll
      for (int jj = XB; jj < 4; ++jj) put_w(jj);
    } else if (j + 3 >= XB && j + 3 < NPRE) put_w(j + 3);
    if (j == 5) ld4_sc1(zf, srdC, zoff);
    if (j == 7) {
      PIN(zf);
      if (!__all(zf == tag)) poll_all(p.ws, zoff, tag);
      const i32x4 srdZ = make_srd(p.ws + WS_ZT, D_ * S_ * 2);
#pragma unroll
      for (int r = 0; r < 3; ++r) {
        int task = r * 64 + lane; task = task < 160 ? task : 159;
        ld16_sc1(zr[r], srdZ, (unsigned)(nt * 80 + w * 20 + (task >> 3)) * 4096u + (mt * 64 + (task & 7) * 8) * 2, 0);
      }
    }
    if (j >= 1 && j + 2 <= 13) issueH(j + 2);
    if (g2_w_after<NPRE>(j)) g2_issue_w(srdD, vd, smb, nt, w, j + 3);
    if (j == 9) {
#pragma unroll
      for (int r = 0; r < 3; ++r) PIN(zr[r]);
#pragma unroll
      for (int r = 0; r < 3; ++r) { const int task = r * 64 + lane; if (task < 160) *(LAS u32x4*)(sm + L_ZS + (w * 20 + (task >> 3)) * ZS_RS + (task & 7) * 16) = zr[r]; }
    }
    if (j < 13) WAITV(g2_wait<NPRE>(j));
    BAR();
  }
}

template <int NY>
DI void g2_math(const Params& p, LAS char* sm, int w, int lane, unsigned tag, int zc, const u32x4 (&wy)[3][5]) {
  f32x4 acc[5][4];
#pragma unroll
  for (int a = 0; a < 5; ++a)
#pragma unroll
    for (int b = 0; b < 4; ++b) acc[a][b] = (f32x4){0.f, 0.f, 0.f, 0.f};
  const int kg = lane >> 4, q4 = (lane & 15) >> 2, pp = lane & 3;
  LAS char* hrd = sm + L_HR + w * 4096 + lane * 16;
  LAS char* wrd = sm + L_WR + w * 5120 + (4 * kg + q4) * 160 + pp * 8;
  bf16x8 hP[4], wP[5], hQ[4], wQ[5];
#define G2_RD(H, W, j) do { LAS char* hp_ = hrd + ((j) % 3) * 16384; LAS char* wp_ = wrd + ((j) & 3) * G2_WSLOT; \
    W[0] = ldtr(wp_, wp_ + 2560); H[0] = *(LAS bf16x8*)(hp_); H[1] = *(LAS bf16x8*)(hp_ + 1024); H[2] = *(LAS bf16x8*)(hp_ + 2048); H[3] = *(LAS bf16x8*)(hp_ + 3072); \
    _Pragma("unroll") for (int a = 1; a < 5; ++a) W[a] = ldtr(wp_ + a * 32, wp_ + a * 32 + 2560); } while (0)
#define G2_MM(H, W) do { _Pragma("unroll") for (int a = 0; a < 5; ++a) _Pragma("unroll") for (int b = 0; b < 4; ++b) \
    acc[a][b] = __builtin_amdgcn_mfma_f32_16x16x32_bf16(W[a], H[b], acc[a][b], 0, 0, 0); } while (0)
#define G2_SCHED() do { _Pragma("unroll") for (int i_ = 0; i_ < 14; ++i_) { __builtin_amdgcn_sched_group_barrier(0x8, 1, 0); __builtin_amdgcn_sched_group_barrier(0x100, 1, 0); } __builtin_amdgcn_sched_group_barrier(0x8, 6, 0); } while (0)
  if (zc >= 0) WAITV(4); else WAITV(0);
  BAR();
  STAMP_AT(9);
  G2_RD(hP, wP, 0);
  auto put_y = [&](int j) {
#pragma unroll
    for (int i = 0; i < 5; ++i) *(LAS u32x4*)(sm + L_WR + (j & 3) * G2_WSLOT + w * 5120 + i * 1024 + lane * 16) = wy[j - 2][i];
  };
  if (NY) { put_y(2); put_y(3); }
  if (zc >= 0) {
    WAITV(0);
    flag_set(p.ws, CW_Z + zc * 16, tag, lane);
  }
  BAR();
  G2_RD(hQ, wQ, 1); G2_MM(hP, wP); G2_SCHED();
  if (NY) put_y(4);
#pragma unroll
  for (int j = 2; j < 14; j += 2) {
    BAR();
    G2_RD(hP, wP, j); G2_MM(hQ, wQ); G2_SCHED();
    BAR();
    G2_RD(hQ, wQ, j + 1); G2_MM(hP, wP); G2_SCHED();
  }
  BAR();
  if (w < 2) G2_MM(hQ, wQ);
  STAMP_AT(10);
#pragma unroll
  for (int a = 0; a < 5; ++a)
#pragma unroll
    for (int b = 0; b < 4; ++b) *(LAS f32x4*)(sm + w * G2_PART + (16 * b + (lane & 15)) * G2_RS + a * 64 + kg * 16) = acc[a][b];
}
DI void g2_final_task(const Params& p, LAS char* sm, int mt, int nt, int gt, int lane, const u32x4& x) {
  const int b = gt / 10, ch = gt - b * 10, tk = b * 16 + (lane & 15);
  f32x4 a0 = (f32x4){0.f, 0.f, 0.f, 0.f}, a1 = a0;
#pragma unroll
  for (int w = 0; w < 4; ++w) { a0 += *(LAS f32x4*)(sm + w * G2_PART + tk * G2_RS + ch * 32); a1 += *(LAS f32x4*)(sm + w * G2_PART + tk * G2_RS + ch * 32 + 16); }
  LAS char* zp = sm + L_ZS + (ch * 8 + ((lane & 15) >> 2)) * ZS_RS + b * 32 + (lane & 3) * 8;
  const u32x2 z0 = __builtin_bit_cast(u32x2, __builtin_amdgcn_ds_read_tr16_b64_v4i16((LAS s16x4*)zp));
  const u32x2 z1 = __builtin_bit_cast(u32x2, __builtin_amdgcn_ds_read_tr16_b64_v4i16((LAS s16x4*)(zp + 4 * ZS_RS)));
  const f32x2 st = *(LAS f32x2*)(sm + L_S2 + tk * 8);
  const float rs = st.y, nmr = -st.x * rs;
  float o[8];
#pragma unroll
  for (int e = 0; e < 8; ++e) {
    const float xv = (e & 1) ? bhi(x[e >> 1]) : blo(x[e >> 1]);
    const unsigned zw = e < 4 ? z0[(e & 3) >> 1] : z1[(e & 3) >> 1];
    const float z = (e & 1) ? bhi(zw) : blo(zw);
    const float xn = fmaf(xv, rs, nmr);
    o[e] = fmaf(xn, z, xv) + (e < 4 ? a0[e] : a1[e - 4]);
  }
  const u32x4 r = {pk2(o[0], o[1]), pk2(o[2], o[3]), pk2(o[4], o[5]), pk2(o[6], o[7])};
  asm volatile("global_store_dwordx4 %0, %1, off nt\n\ts_nop 1" :: "v"(p.out + (mt * 64 + tk) * 640 + nt * 80 + ch * 8), "v"(r) : "memory");
}
DI u32x4 final_x(const Params& p, int mt, int nt, int gt, int lane) {
  const int b = gt / 10, ch = gt - b * 10;
  return *(const GAS u32x4*)(p.q + (mt * 64 + b * 16 + (lane & 15)) * 640 + nt * 80 + ch * 8);
}

DI void pair_send(LAS char* pb, LAS unsigned* pf, unsigned tag, int lane, const f32x4 (&acc)[8]) {
#pragma unroll
  for (int g = 0; g < 8; ++g) *(LAS f32x4*)(pb + g * 1024 + lane * 16) = acc[g];
  asm volatile("s_waitcnt lgkmcnt(0)" ::: "memory");
  if (lane == 0) *(volatile LAS unsigned*)pf = tag;
}
DI void pair_recv(LAS char* pb, LAS unsigned* pf, unsigned tag, int lane, f32x4 (&acc)[8]) {
#pragma unroll 1
  for (unsigned it = 0; it < (1u << 22); ++it) { if (*(volatile LAS unsigned*)pf == tag) break; __builtin_amdgcn_s_sleep(1); }
  asm volatile("" ::: "memory");
#pragma unroll
  for (int g = 0; g < 8; ++g) acc[g] += *(LAS f32x4*)(pb + g * 1024 + lane * 16);
}

__global__ __launch_bounds__(512, 2) void fused_kernel(Params p_arg) {
  unsigned long long pc0; asm volatile("s_getpc_b64 %0" : "=s"(pc0));
  const KP kp = (KP)__builtin_amdgcn_kernarg_segment_ptr();
  Params p = reload(kp);
  extern __shared__ __attribute__((aligned(16))) char smem_raw[];
  LAS char* sm = (LAS char*)smem_raw;
  const unsigned smb = (unsigned)(size_t)sm;
  const int lane = threadIdx.x & 63, wid = __builtin_amdgcn_readfirstlane(threadIdx.x >> 6);
  const int b = blockIdx.x, x = b & 7, i = b >> 3;
  const int mt = x * 4 + (i & 3), nt = i >> 2;
  STAMP_INIT();
  STAMP_AT(0);
  if (i < 27) {
    const int mm = 27 * (x & 3) + i, ft = mm >> 2, rb = 4 * (x >> 2) + (mm & 3);
    if (wid < 4) {
      const unsigned ep = __hip_atomic_load((gu32*)(p.ws + CW_EPOCH), __ATOMIC_RELAXED, __HIP_MEMORY_SCOPE_AGENT);
      unsigned taskflag = 0;
      const int id = wid < 2 ? 2 * b + wid : 432 + b;
      if (wid < 3 && id < 512) { task_stats<true>(p, id, lane); taskflag = CW_ST + id * 16; }
      else BAR();
      const unsigned tag = __builtin_amdgcn_readfirstlane(ep) + 1u;
      asm volatile("" :: "s"(tag) : "memory");
      STAMP_AT(13);
      g1_math(p, sm, rb, ft, wid, lane, tag, taskflag);
      p = reload(kp);
      g2_loader<false>(p, sm, smb, mt, nt, wid, lane, tag, CW_HM + ((rb * 2 + (wid >> 1)) * 54 + ft * 2 + (wid & 1)) * 16);
    } else {
      const int hw = wid - 4;
      const bool conv = 2 * b < NCH_G;
      const int cch = 2 * b + (hw >> 1);
      unsigned tag, dvb = 0; int zc = -1;
      if (conv) {
        u32x4 wy[3][5];
        g1_loader<true>(kp, p, pc0, sm, smb, rb, ft, mt, nt, hw, lane, tag, cch, dvb, wy);
        p = reload(kp);
        BAR();
        STAMP_AT(7);
        LAS char* cb = sm + L_CVG + (hw >> 1) * CV_SZ;
        LAS char* pb = sm + L_CP + (hw >> 1) * 8192;
        LAS unsigned* pf = (LAS unsigned*)(sm + L_PF + (hw >> 1) * 4);
        f32x4 acc[8];
        conv_mfma<4>(cb, (hw & 1) * 4, lane, acc);
        asm volatile("" :: "v"(acc[0]), "v"(acc[7]));
        STAMP_AT(4);
        if (hw & 1) pair_send(pb, pf, tag, lane, acc);
        else { pair_recv(pb, pf, tag, lane, acc); conv_store(p, cb, cch, lane, dvb, acc); zc = cch; }
        STAMP_AT(8);
        g2_math<3>(p, sm, hw, lane, tag, zc, wy);
      } else {
        u32x4 wy[3][5];
        g1_loader<false>(kp, p, pc0, sm, smb, rb, ft, mt, nt, hw, lane, tag, cch, dvb, wy);
        p = reload(kp);
        BAR();
        g2_math<3>(p, sm, hw, lane, tag, zc, wy);
      }
    }
  } else {
    const int cg = i - 27, pb = x * 5 + cg;
    const unsigned ep = __hip_atomic_load((gu32*)(p.ws + CW_EPOCH), __ATOMIC_RELAXED, __HIP_MEMORY_SCOPE_AGENT);
    const int hw = wid - 4, c0 = NCH_G + pb * NCH_P;
    if (wid < 4) {
      task_kv<0>(p, 2 * cg + (wid & 1), 4 * x + (wid >> 1), lane, [] {});
      const unsigned tag = __builtin_amdgcn_readfirstlane(ep) + 1u;
      asm volatile("" :: "s"(tag) : "memory");
      WAITV(0);
      flag_set(p.ws, CW_KV + ((2 * cg + (wid & 1)) * 32 + 4 * x + (wid >> 1)) * 16, tag, lane);
      BAR();
      if (wid == 0) flag_set(p.ws, CW_ARRIVE + b * 4, tag, lane);
      STAMP_AT(1);
      BAR();
      p = reload(kp);
      touchg(p.wd, (unsigned)((pb * 4 + wid) * 64 + lane) * 128u, smb + L_END - 256);
      if (pb * 4 + wid + 160 < 270) touchg(p.wd, (unsigned)((pb * 4 + wid + 160) * 64 + lane) * 128u, smb + L_END - 256);
      g2_loader<true>(p, sm, smb, mt, nt, wid, lane, tag, 0);
    } else {
      LAS char* cb = sm + L_CVP + hw * CV_SZ;
      const int cA = c0 + hw, cB = c0 + 4 + (hw >> 1);
      ConvIn in, in2;
      task_kv<13>(p, 2 * cg + (wid & 1), 4 * x + (wid >> 1), lane, [&] { conv_load_h(p, in, cA, lane); });
      conv_load_h(p, in2, cB, lane);
      const unsigned tag = __builtin_amdgcn_readfirstlane(ep) + 1u;
      asm volatile("" :: "s"(tag) : "memory");
      if ((hw & 1) == 0 && lane == 0) *(LAS unsigned*)(sm + L_PFP + (hw >> 1) * 4) = ~tag;
      WAITV(13);
      flag_set(p.ws, CW_KV + ((2 * cg + (wid & 1)) * 32 + 4 * x + (wid >> 1)) * 16, tag, lane);
      BAR();
      STAMP_AT(1);
      poll_all(p.ws, CW_KV + (((lane < 32 ? cA : cB) >> 6) * 32 + (lane & 31)) * 16, tag);
      conv_load_u(p, in, cA, lane);
      conv_load_u(p, in2, cB, lane);
      BAR();
      WAITV(4);
      conv_pin(in);
      conv_build(in, cb, lane);
      unsigned dvb = in.dv;
      STAMP_AT(2); STAMP_CY(14);
      f32x4 acc[8];
      conv_mfma<8>(cb, 0, lane, acc);
      asm volatile("" :: "v"(acc[0]), "v"(acc[7]));
      STAMP_AT(4); STAMP_CY(15);
      conv_store(p, cb, cA, lane, dvb, acc);
      STAMP_AT(3);
      WAITV(4);
      conv_pin(in2);
      conv_build(in2, cb, lane);
      dvb = in2.dv;
      STAMP_AT(7);
      conv_mfma<4>(cb, (hw & 1) * 4, lane, acc);
      WAITV(0);
      flag_set(p.ws, CW_Z + cA * 16, tag, lane);
      LAS unsigned* pf = (LAS unsigned*)(sm + L_PFP + (hw >> 1) * 4);
      int zc = -1;
      if (hw & 1) pair_send(cb, pf, tag, lane, acc);
      else { pair_recv(cb + CV_SZ, pf, tag, lane, acc); conv_store(p, cb, cB, lane, dvb, acc); zc = cB; }
      STAMP_AT(8);
      u32x4 wy[3][5];
      g2_math<0>(p, sm, hw, lane, tag, zc, wy);
    }
  }
  p = reload(kp);
  const int gt0 = wid * 4 + (lane >> 4), gt1 = 32 + gt0;
  const u32x4 x0 = final_x(p, mt, nt, gt0, lane);
  u32x4 x1 = x0;
  if (wid < 2) x1 = final_x(p, mt, nt, gt1, lane);
  BAR();
  g2_final_task(p, sm, mt, nt, gt0, lane, x0);
  if (wid < 2) g2_final_task(p, sm, mt, nt, gt1, lane, x1);
  STAMP_AT(11);
  STAMP_FLUSH();
}

extern "C" void kernel_launch(void* const* d_in, const int* in_sizes, int n_in, void* d_out, int out_size, void* d_ws, size_t ws_size, hipStream_t stream) {
  Params p;
  p.q = (const u16*)d_in[0]; p.k = (const u16*)d_in[1]; p.v = (const u16*)d_in[2]; p.sk = (const u16*)d_in[3]; p.h = (const u16*)d_in[4]; p.dv = (const u16*)d_in[5];
  p.wg = (const u16*)d_in[7]; p.wu = (const u16*)d_in[8]; p.wd = (const u16*)d_in[9]; p.out = (u16*)d_out; p.ws = (char*)d_ws;
  (void)hipFuncSetAttribute((const void*)fused_kernel, hipFuncAttributeMaxDynamicSharedMemorySize, L_ALLOC);
  fused_kernel<<<256, 512, L_ALLOC, stream>>>(p);
}
#define HAVE_INTER 1
#define HM_K16 1
```

```cpp
#include <hip/hip_runtime.h>
#include <stdint.h>
typedef unsigned short u16;
typedef __bf16 bf16x8 __attribute__((ext_vector_type(8)));
typedef __bf16 bf16x2 __attribute__((ext_vector_type(2)));
typedef short s16x4 __attribute__((ext_vector_type(4)));
typedef short s16x8 __attribute__((ext_vector_type(8)));
typedef float f32x2 __attribute__((ext_vector_type(2)));
typedef float f32x4 __attribute__((ext_vector_type(4)));
typedef float f32x16 __attribute__((ext_vector_type(16)));
typedef unsigned u32x2 __attribute__((ext_vector_type(2)));
typedef unsigned u32x4 __attribute__((ext_vector_type(4)));
typedef int i32x4 __attribute__((ext_vector_type(4)));
#define LAS __attribute__((address_space(3)))
#define GAS __attribute__((address_space(1)))
#define DI __device__ __forceinline__

#define TOUCH 1
constexpr int S_ = 2048, D_ = 640, F_ = 1728;
constexpr unsigned WS_STATS = 65536;
constexpr unsigned WS_KVT   = 131072;
constexpr unsigned WS_ZT    = WS_KVT + D_ * S_ * 2;
constexpr unsigned WS_HM    = WS_ZT + D_ * S_ * 2;
constexpr unsigned WS_END   = WS_HM + 54 * 128 * 1024;
constexpr unsigned WS_DBG   = 32u << 20;

struct Params {
  const u16 *q, *k, *v, *sk, *h, *dv, *wg, *wu, *wd;
  u16* out; char* ws;
};

DI i32x4 make_srd(const void* p, unsigned bytes) {
  i32x4 r; unsigned long long a = (unsigned long long)p;
  r.x = __builtin_amdgcn_readfirstlane((int)(unsigned)a);
  r.y = __builtin_amdgcn_readfirstlane((int)(unsigned)(a >> 32)) & 0xffff;
  r.z = (int)bytes; r.w = 0x00020000; return r;
}
DI void dma16(const i32x4& srd, unsigned voff, unsigned soff, unsigned lds) {
  asm volatile("s_mov_b32 m0, %3\n\ts_nop 0\n\tbuffer_load_dwordx4 %0, %1, %2 offen lds" :: "v"(voff), "s"(srd), "s"(soff), "s"(lds) : "memory");
}
DI void touch4(const i32x4& srd, unsigned voff, unsigned soff, unsigned lds) {
  asm volatile("s_mov_b32 m0, %3\n\ts_nop 0\n\tbuffer_load_dword %0, %1, %2 offen lds" :: "v"(voff), "s"(srd), "s"(soff), "s"(lds) : "memory");
}
#define WAITV(n) asm volatile("s_waitcnt vmcnt(%0)" :: "i"(n) : "memory")
#define WAITL0() asm volatile("s_waitcnt lgkmcnt(0)" ::: "memory")
#define BAR() asm volatile("s_waitcnt lgkmcnt(0)\n\ts_barrier" ::: "memory")
DI float b2f(u16 x) { return __uint_as_float(((unsigned)x) << 16); }
DI float blo(unsigned x) { return __uint_as_float(x << 16); }
DI float bhi(unsigned x) { return __uint_as_float(x & 0xffff0000u); }
DI unsigned pk2(float lo, float hi) { bf16x2 r = {(__bf16)lo, (__bf16)hi}; return __builtin_bit_cast(unsigned, r); }
DI float rbf(float x) { return (float)(__bf16)x; }
DI float dot2(unsigned a, unsigned b, float c) { return __builtin_amdgcn_fdot2_f32_bf16(__builtin_bit_cast(bf16x2, a), __builtin_bit_cast(bf16x2, b), c, false); }
DI bf16x8 ldtr(LAS char* p0, LAS char* p1) {
  s16x4 a = __builtin_amdgcn_ds_read_tr16_b64_v4i16((LAS s16x4*)p0), b = __builtin_amdgcn_ds_read_tr16_b64_v4i16((LAS s16x4*)p1);
  return __builtin_bit_cast(bf16x8, __builtin_shufflevector(a, b, 0, 1, 2, 3, 4, 5, 6, 7));
}
#define STAMP_CY(p, idx) do {} while (0)
#define STAMP_AT(p, idx) do {} while (0)

DI void prep_body(const Params& p, int tb, int cg, int wid, int lane) {
  {
    const int sub = lane >> 4, l16 = lane & 15;
    const __amdgpu_buffer_rsrc_t rq = __builtin_amdgcn_make_buffer_rsrc((void*)p.q, 0, S_ * D_ * 2, 0x00020000);
    const __amdgpu_buffer_rsrc_t rs = __builtin_amdgcn_make_buffer_rsrc((void*)(p.ws + WS_STATS), 0, S_ * 8, 0x00020000);
    u32x4 x[2][5];
#pragma unroll
    for (int ps = 0; ps < 2; ++ps) {
      int rl = cg * 52 + ps * 32 + wid * 4 + sub; rl = rl < 256 ? rl : 255;
      const unsigned base = (unsigned)(tb * 256 + rl) * 1280u + l16 * 16;
#pragma unroll
      for (int j = 0; j < 5; ++j) x[ps][j] = __builtin_amdgcn_raw_buffer_load_b128(rq, base + j * 256, 0, 0);
    }
#pragma unroll
    for (int ps = 0; ps < 2; ++ps) {
      float s = 0.f;
#pragma unroll
      for (int j = 0; j < 5; ++j) { s = dot2(x[ps][j].x, 0x3f803f80u, s); s = dot2(x[ps][j].y, 0x3f803f80u, s); s = dot2(x[ps][j].z, 0x3f803f80u, s); s = dot2(x[ps][j].w, 0x3f803f80u, s); }
      s += __shfl_xor(s, 1); s += __shfl_xor(s, 2); s += __shfl_xor(s, 4); s += __shfl_xor(s, 8);
      const float mean = s * (1.0f / 640.0f);
      float q2 = 0.f;
#pragma unroll
      for (int j = 0; j < 5; ++j) {
#pragma unroll
        for (int e = 0; e < 4; ++e) { const unsigned w = x[ps][j][e]; const float a = blo(w) - mean, b = bhi(w) - mean; q2 = fmaf(a, a, q2); q2 = fmaf(b, b, q2); }
      }
      q2 += __shfl_xor(q2, 1); q2 += __shfl_xor(q2, 2); q2 += __shfl_xor(q2, 4); q2 += __shfl_xor(q2, 8);
      const float var = q2 * (1.0f / 640.0f);
      const float mb = rbf(mean), sb = rbf(sqrtf(rbf(rbf(var) + 1e-5f)));
      const int rl = cg * 52 + ps * 32 + wid * 4 + sub;
      const bool ok = (ps * 32 + wid * 4 + sub < 52) && rl < 256 && l16 == 0;
      u32x2 o; o.x = __float_as_uint(mb); o.y = __float_as_uint(1.0f / sb);
      if (ok) __builtin_amdgcn_raw_buffer_store_b64(o, rs, (unsigned)(tb * 256 + rl) * 8u, 0, 16);
    }
  }
  {
    const int c0 = cg * 128 + 64 * (wid & 1) + 8 * (lane & 7);
    const int t0 = tb * 256 + 64 * (wid >> 1) + 8 * (lane >> 3);
    const __amdgpu_buffer_rsrc_t rk = __builtin_amdgcn_make_buffer_rsrc((void*)p.k, 0, S_ * D_ * 2, 0x00020000);
    const __amdgpu_buffer_rsrc_t rv = __builtin_amdgcn_make_buffer_rsrc((void*)p.v, 0, S_ * D_ * 2, 0x00020000);
    const __amdgpu_buffer_rsrc_t rsk = __builtin_amdgcn_make_buffer_rsrc((void*)p.sk, 0, D_ * 4 * 2, 0x00020000);
    const __amdgpu_buffer_rsrc_t ro = __builtin_amdgcn_make_buffer_rsrc((void*)(p.ws + WS_KVT), 0, D_ * S_ * 2, 0x00020000);
    u32x4 kk[11], vv[8], ss[4];
#pragma unroll
    for (int j = 0; j < 11; ++j) { const int t = t0 - 3 + j; const unsigned off = t >= 0 ? (unsigned)(t * 1280 + c0 * 2) : 0xfffffff0u; kk[j] = __builtin_amdgcn_raw_buffer_load_b128(rk, off, 0, 0); }
#pragma unroll
    for (int j = 0; j < 8; ++j) vv[j] = __builtin_amdgcn_raw_buffer_load_b128(rv, (unsigned)((t0 + j) * 1280 + c0 * 2), 0, 0);
#pragma unroll
    for (int j = 0; j < 4; ++j) ss[j] = __builtin_amdgcn_raw_buffer_load_b128(rsk, (unsigned)(c0 * 8 + j * 16), 0, 0);
#pragma unroll
    for (int c = 0; c < 8; ++c) {
      const unsigned sA = ss[c >> 1][(c & 1) * 2], sB = ss[c >> 1][(c & 1) * 2 + 1];
      const float s0 = blo(sA), s1 = bhi(sA), s2 = blo(sB), s3 = bhi(sB);
      float kx[11];
#pragma unroll
      for (int j = 0; j < 11; ++j) { const unsigned w = kk[j][c >> 1]; kx[j] = (c & 1) ? bhi(w) : blo(w); }
      float o[8];
#pragma unroll
      for (int j = 0; j < 8; ++j) {
        float a = kx[j] * s3; a = fmaf(kx[j + 1], s2, a); a = fmaf(kx[j + 2], s1, a); a = fmaf(kx[j + 3], s0, a);
        const unsigned w = vv[j][c >> 1]; o[j] = a * ((c & 1) ? bhi(w) : blo(w));
      }
      u32x4 r; r.x = pk2(o[7], o[6]); r.y = pk2(o[5], o[4]); r.z = pk2(o[3], o[2]); r.w = pk2(o[1], o[0]);
      __builtin_amdgcn_raw_buffer_store_b128(r, ro, (unsigned)((c0 + c) * 4096 + (2040 - t0) * 2), 0, 16);
    }
  }
}
__global__ __launch_bounds__(512, 2) void k_prep(Params p) {
  const int wid = __builtin_amdgcn_readfirstlane(threadIdx.x >> 6), lane = threadIdx.x & 63;
  STAMP_AT(p, 0);
  prep_body(p, blockIdx.x & 7, blockIdx.x >> 3, wid, lane);
  STAMP_AT(p, 1);
}

constexpr int G1_SLOT = 49152, G1_CS = 3 * G1_SLOT, G1_ST = G1_CS + 512, G1_LDS = G1_ST + 2048 + 256;

DI void g1_helper(const Params& p, LAS char* sm, unsigned smb, int rb, int ft, int hw, int lane) {
  const i32x4 srdQ = make_srd(p.q, S_ * D_ * 2), srdW = make_srd((hw >> 1) ? p.wu : p.wg, D_ * F_ * 2);
  const unsigned vq0 = (lane >> 3) * 1280 + (((lane & 7) ^ (lane >> 4)) * 16);
  const unsigned vq1 = (lane >> 3) * 1280 + (((lane & 7) ^ (4 + (lane >> 4))) * 16);
  const unsigned vw = (lane >> 3) * 3456 + (((lane & 7) ^ (4 * ((lane >> 4) & 1))) * 16);
  const unsigned qb = (unsigned)(rb * 256 + 64 * hw) * 1280u, wb = (unsigned)(32 * (hw & 1)) * 3456u + ft * 128u;
  const unsigned lq = smb + hw * 8192, lw = smb + 32768 + (hw >> 1) * 8192 + (hw & 1) * 4096;
  auto issue = [&](int s, int slot) {
    const unsigned so = slot * G1_SLOT;
#pragma unroll
    for (int i = 0; i < 4; ++i) dma16(srdW, vw, wb + (s * 64 + 8 * i) * 3456u, lw + so + i * 1024);
#pragma unroll
    for (int i = 0; i < 8; ++i) dma16(srdQ, (i & 1) ? vq1 : vq0, qb + i * 10240u + s * 128u, lq + so + i * 1024);
  };
  issue(0, 0); issue(1, 1); issue(2, 2);
  {
    const int x = blockIdx.x & 7, wv = (blockIdx.x >> 3) * 4 + hw;
    const int ft0 = (27 * (x & 3)) >> 2;
    const i32x4 srdG = make_srd(p.wg, D_ * F_ * 2), srdU = make_srd(p.wu, D_ * F_ * 2);
#pragma unroll
    for (int r = 0; r < 2; ++r) {
      const int j = wv + 108 * r;
      const int kt = 3 + (j >> 5), sub = j & 31;
      if (sub < 16) {
        touch4(srdQ, (unsigned)lane * 1280u, (unsigned)((x >> 2) * 1024 + sub * 64) * 1280u + kt * 128u, smb + G1_ST + 2048);
      } else {
        const int m = sub & 1, kgp = (sub - 16) >> 1;
        unsigned fo = (ft0 + (lane & 7)) * 128u; fo = fo < 3456u ? fo : 3328u;
        const unsigned vo = (lane >> 3) * 3456u + fo;
        if (m) touch4(srdU, vo, (unsigned)(kt * 64 + kgp * 8) * 3456u, smb + G1_ST + 2048);
        else touch4(srdG, vo, (unsigned)(kt * 64 + kgp * 8) * 3456u, smb + G1_ST + 2048);
      }
    }
  }
#define TN 2
  const int tid2 = hw * 64 + lane;
  const int h = lane >> 5, blk = (lane >> 4) & 1, q4 = (lane & 15) >> 2, pp = lane & 3;
  LAS char* wa = sm + 32768 + (hw >> 1) * 8192 + (8 * h + q4) * 128 + (((4 * (hw & 1) + 2 * blk + (pp >> 1)) ^ (4 * ((q4 >> 1) & 1))) * 16) + 8 * (pp & 1);
  float cs = 0.f;
  int slot = 0, nslot = 2;
#pragma unroll 1
  for (int s = 0; s < 10; ++s) {
    if (s == 0) WAITV(24 + TN); else if (s < 3) WAITV(12 + TN); else if (s < 9) WAITV(12); else WAITV(0);
    BAR();
    if (s >= 1 && s <= 7) issue(s + 2, nslot);
    LAS char* w = wa + slot * G1_SLOT;
#pragma unroll
    for (int ks = 0; ks < 4; ++ks) {
#pragma unroll
      for (int t = 0; t < 2; ++t) {
        const u32x2 a = __builtin_bit_cast(u32x2, __builtin_amdgcn_ds_read_tr16_b64_v4i16((LAS s16x4*)(w + ks * 2048 + t * 512)));
        cs = dot2(a.x, 0x3f803f80u, cs); cs = dot2(a.y, 0x3f803f80u, cs);
      }
    }
    slot = slot == 2 ? 0 : slot + 1; nslot = nslot == 2 ? 0 : nslot + 1;
  }
  cs += __shfl_xor(cs, 32);
  if (lane < 32) *(LAS float*)(sm + G1_CS + ((hw >> 1) * 64 + (hw & 1) * 32 + lane) * 4) = cs;
  const u32x2 st = *(const GAS u32x2*)(p.ws + WS_STATS + (rb * 256 + tid2) * 8);
  *(LAS u32x2*)(sm + G1_ST + tid2 * 8) = st;
  BAR();
}

DI void g1_math(const Params& p, LAS char* sm, int rb, int ft, int wid, int lane) {
  const int wr = wid >> 1, wc = wid & 1;
  const int n = lane & 31, h = lane >> 5, blk = (lane >> 4) & 1, q4 = (lane & 15) >> 2, pp = lane & 3;
  f32x16 ag[4], au[4];
#pragma unroll
  for (int m = 0; m < 4; ++m)
#pragma unroll
    for (int i = 0; i < 16; ++i) { ag[m][i] = 0.f; au[m][i] = 0.f; }
  const unsigned tbase = (wr * 128 + n) * 128 + ((h ^ ((n >> 1) & 7)) * 16);
  const unsigned wbase = 32768 + (8 * h + q4) * 128 + (((4 * wc + 2 * blk + (pp >> 1)) ^ (4 * ((q4 >> 1) & 1))) * 16) + 8 * (pp & 1);
  bf16x8 tP[4], tQ[4], gP, gQ, uP, uQ;
#define G1_RD(T, G, U, so, ks) do { LAS char* tp_ = sm + (so) + (tbase ^ ((ks) * 32)); LAS char* wp_ = sm + (so) + wbase + (ks) * 2048; \
    G = ldtr(wp_, wp_ + 512); T[0] = *(LAS bf16x8*)(tp_); U = ldtr(wp_ + 8192, wp_ + 8192 + 512); \
    T[1] = *(LAS bf16x8*)(tp_ + 4096); T[2] = *(LAS bf16x8*)(tp_ + 8192); T[3] = *(LAS bf16x8*)(tp_ + 12288); } while (0)
#define G1_MM(T, G, U) do { _Pragma("unroll") for (int m = 0; m < 4; ++m) { \
    ag[m] = __builtin_amdgcn_mfma_f32_32x32x16_bf16(G, T[m], ag[m], 0, 0, 0); au[m] = __builtin_amdgcn_mfma_f32_32x32x16_bf16(U, T[m], au[m], 0, 0, 0); } } while (0)
#define G1_SCHED() do { _Pragma("unroll") for (int i_ = 0; i_ < 8; ++i_) { __builtin_amdgcn_sched_group_barrier(0x8, 1, 0); __builtin_amdgcn_sched_group_barrier(0x100, 1, 0); } } while (0)
  unsigned so = 0;
  BAR();
  STAMP_AT(p, 3); STAMP_CY(p, 14);
  G1_RD(tP, gP, uP, so, 0);
  G1_RD(tQ, gQ, uQ, so, 1); G1_MM(tP, gP, uP); G1_SCHED();
  G1_RD(tP, gP, uP, so, 2); G1_MM(tQ, gQ, uQ); G1_SCHED();
  G1_RD(tQ, gQ, uQ, so, 3); G1_MM(tP, gP, uP); G1_SCHED();
#pragma unroll 1
  for (int s = 1; s < 10; ++s) {
    so = so == 2 * G1_SLOT ? 0 : so + G1_SLOT;
    BAR();
    G1_RD(tP, gP, uP, so, 0); G1_MM(tQ, gQ, uQ); G1_SCHED();
    G1_RD(tQ, gQ, uQ, so, 1); G1_MM(tP, gP, uP); G1_SCHED();
    G1_RD(tP, gP, uP, so, 2); G1_MM(tQ, gQ, uQ); G1_SCHED();
    G1_RD(tQ, gQ, uQ, so, 3); G1_MM(tP, gP, uP); G1_SCHED();
  }
  BAR();
  f32x4 csg[4], csu[4];
#pragma unroll
  for (int g = 0; g < 4; ++g) { csg[g] = *(LAS f32x4*)(sm + G1_CS + (wc * 32 + 8 * g + 4 * h) * 4); csu[g] = *(LAS f32x4*)(sm + G1_CS + (64 + wc * 32 + 8 * g + 4 * h) * 4); }
  f32x2 st[4];
#pragma unroll
  for (int m = 0; m < 4; ++m) st[m] = *(LAS f32x2*)(sm + G1_ST + (wr * 128 + 32 * m + n) * 8);
  G1_MM(tQ, gQ, uQ);
  STAMP_AT(p, 4); STAMP_CY(p, 15);
  const __amdgpu_buffer_rsrc_t rh = __builtin_amdgcn_make_buffer_rsrc((void*)(p.ws + WS_HM), 0, 54 * 128 * 1024, 0x00020000);
  const unsigned hb = (unsigned)((ft * 2 + wc) * 128 + rb * 16 + wr * 8 + blk) * 1024u + (2 * h * 16 + (lane & 15)) * 16;
#pragma unroll
  for (int m = 0; m < 4; ++m) {
    const float nm = -st[m].x, rs = st[m].y, rsl = rs * -1.44269504089f, rs2 = rs * rs;
    unsigned w[8];
#pragma unroll
    for (int i = 0; i < 16; i += 2) {
      f32x2 tg = {ag[m][i], ag[m][i + 1]}, tu = {au[m][i], au[m][i + 1]};
      const f32x2 cg = {csg[i >> 2][i & 3], csg[i >> 2][(i & 3) + 1]}, cu = {csu[i >> 2][i & 3], csu[i >> 2][(i & 3) + 1]};
      tg = cg * nm + tg; tu = cu * nm + tu;
      const f32x2 gl = tg * rsl;
      f32x2 e; e.x = __builtin_amdgcn_exp2f(gl.x); e.y = __builtin_amdgcn_exp2f(gl.y);
      e = e + 1.0f;
      f32x2 r; r.x = __builtin_amdgcn_rcpf(e.x); r.y = __builtin_amdgcn_rcpf(e.y);
      const f32x2 hv = (tg * tu) * rs2 * r;
      w[i >> 1] = pk2(hv.x, hv.y);
    }
    const u32x4 s0 = {w[0], w[1], w[2], w[3]}, s1 = {w[4], w[5], w[6], w[7]};
    __builtin_amdgcn_raw_buffer_store_b128(s0, rh, hb + m * 2048, 0, 16);
    __builtin_amdgcn_raw_buffer_store_b128(s1, rh, hb + m * 2048 + 256, 0, 16);
  }
  STAMP_AT(p, 5);
  WAITV(0);
  STAMP_AT(p, 6);
}

__global__ __launch_bounds__(512, 2) void k_gemm1(Params p) {
  extern __shared__ __attribute__((aligned(16))) char smem_raw[];
  LAS char* sm = (LAS char*)smem_raw;
  const unsigned smb = (unsigned)(size_t)sm;
  const int lane = threadIdx.x & 63, wid = __builtin_amdgcn_readfirstlane(threadIdx.x >> 6);
  const int b = blockIdx.x, x = b & 7, i = b >> 3, mm = 27 * (x & 3) + i, ft = mm >> 2, rb = 4 * (x >> 2) + (mm & 3);
  STAMP_AT(p, 2);
  if (wid >= 4) g1_helper(p, sm, smb, rb, ft, wid - 4, lane);
  else g1_math(p, sm, rb, ft, wid, lane);
}

constexpr int CV_H = 4160, CV_U = 4 * CV_H, CV_SZ = CV_U + 4576;
DI void conv_build(const Params& p, LAS char* cb, int c, int lane) {
  const __amdgpu_buffer_rsrc_t rh = __builtin_amdgcn_make_buffer_rsrc((void*)p.h, 0, D_ * S_ * 2, 0x00020000);
  const __amdgpu_buffer_rsrc_t ru = __builtin_amdgcn_make_buffer_rsrc((void*)(p.ws + WS_KVT), 0, D_ * S_ * 2, 0x00020000);
  u32x4 L0[4], L1[4], L2[4], U[4];
#pragma unroll
  for (int i = 0; i < 4; ++i) {
    const unsigned o = c * 4096u + (i * 64 + lane) * 16;
    L0[i] = __builtin_amdgcn_raw_buffer_load_b128(rh, o, 0, 0);
    L1[i] = __builtin_amdgcn_raw_buffer_load_b128(rh, o + 4, 0, 0);
    L2[i] = __builtin_amdgcn_raw_buffer_load_b128(rh, o + 8, 0, 0);
    U[i] = __builtin_amdgcn_raw_buffer_load_b128(ru, o, 0, 16);
  }
#pragma unroll
  for (int i = 0; i < 4; ++i) {
    const unsigned o = (i * 64 + lane + 2) * 16;
    u32x4 c0, c2;
#pragma unroll
    for (int e = 0; e < 4; ++e) { c0[e] = __builtin_amdgcn_alignbit(L1[i][e], L0[i][e], 16); c2[e] = __builtin_amdgcn_alignbit(L2[i][e], L1[i][e], 16); }
    *(LAS u32x4*)(cb + 0 * CV_H + o) = c0;
    *(LAS u32x4*)(cb + 1 * CV_H + o) = L1[i];
    *(LAS u32x4*)(cb + 2 * CV_H + o) = c2;
    *(LAS u32x4*)(cb + 3 * CV_H + o) = L2[i];
    *(LAS u32x4*)(cb + CV_U + (i * 64 + lane) * 16) = U[i];
  }
  const u32x4 z4 = {0u, 0u, 0u, 0u};
  if (lane < 30) *(LAS u32x4*)(cb + CV_U + 4096 + lane * 16) = z4;
  if (lane == 0) {
    const unsigned w0 = L0[0].x, w1 = L0[0].y;
#pragma unroll
    for (int cc = 0; cc < 4; ++cc) *(LAS u32x4*)(cb + cc * CV_H) = z4;
    *(LAS u32x4*)(cb + 0 * CV_H + 16) = (u32x4){0u, 0u, 0u, w0 << 16};
    *(LAS u32x4*)(cb + 1 * CV_H + 16) = (u32x4){0u, 0u, 0u, w0};
    *(LAS u32x4*)(cb + 2 * CV_H + 16) = (u32x4){0u, 0u, w0 << 16, __builtin_amdgcn_alignbit(w1, w0, 16)};
    *(LAS u32x4*)(cb + 3 * CV_H + 16) = (u32x4){0u, 0u, w0, w1};
  }
}
DI void conv_compute(const Params& p, LAS char* cb, int c, int lane) {
  const int r = lane & 15, kg = lane >> 4, kap = kg >> 1, bp = kg & 1;
  LAS char* ab = cb + (r & 3) * CV_H + 32 * kap + 16 * bp + 2 * (r & 12);
  LAS char* ub = cb + CV_U + (4064 - 3584) - 32 * r + 32 * kap + 16 * bp;
  f32x4 acc[8];
#pragma unroll
  for (int g = 0; g < 8; ++g) acc[g] = (f32x4){0.f, 0.f, 0.f, 0.f};
#pragma unroll 1
  for (int d = 0; d < 8; ++d) {
    bf16x8 A[8], B[8];
#pragma unroll
    for (int g = 0; g < 8; ++g) {
      const s16x4 a0 = *(LAS s16x4*)(ab + d * 64 + g * 512), a1 = *(LAS s16x4*)(ab + d * 64 + g * 512 + 8);
      A[g] = __builtin_bit_cast(bf16x8, __builtin_shufflevector(a0, a1, 0, 1, 2, 3, 4, 5, 6, 7));
      B[g] = *(LAS bf16x8*)(ub + d * 64 + (7 - g) * 512);
    }
#pragma unroll
    for (int gm = 0; gm < 8; ++gm)
#pragma unroll
      for (int gp = 0; gp < 8 - gm; ++gp) acc[gm + gp] = __builtin_amdgcn_mfma_f32_16x16x32_bf16(A[gm], B[gp], acc[gm + gp], 0, 0, 0);
  }
  const float dv = b2f(p.dv[c]);
  const __amdgpu_buffer_rsrc_t rz = __builtin_amdgcn_make_buffer_rsrc((void*)(p.ws + WS_ZT), 0, D_ * S_ * 2, 0x00020000);
  u32x2 z[8];
#pragma unroll
  for (int g = 0; g < 8; ++g) {
    const int t = 256 * g + 16 * r + 4 * kg;
    const u32x2 kv = *(LAS u32x2*)(cb + CV_U + (2044 - t) * 2);
    const float y0 = fmaf(blo(kv.y >> 16), dv, acc[g][0]), y1 = fmaf(blo(kv.y), dv, acc[g][1]), y2 = fmaf(bhi(kv.x), dv, acc[g][2]), y3 = fmaf(blo(kv.x), dv, acc[g][3]);
    z[g].x = pk2(y0, y1); z[g].y = pk2(y2, y3);
  }
#pragma unroll
  for (int g = 0; g < 8; g += 2) {
    const auto s0 = __builtin_amdgcn_permlane16_swap(z[g].x, z[g + 1].x, false, false);
    const auto s1 = __builtin_amdgcn_permlane16_swap(z[g].y, z[g + 1].y, false, false);
    const u32x4 o = {s0[0], s1[0], s0[1], s1[1]};
    const int gg = g + (kg & 1);
    __builtin_amdgcn_raw_buffer_store_b128(o, rz, c * 4096u + (256 * gg + 16 * r + 4 * (kg & 2)) * 2, 0, 16);
  }
}
__global__ __launch_bounds__(256, 2) void k_conv(Params p) {
  extern __shared__ __attribute__((aligned(16))) char smem_raw[];
  LAS char* sm = (LAS char*)smem_raw;
  const int lane = threadIdx.x & 63, wid = __builtin_amdgcn_readfirstlane(threadIdx.x >> 6);
  const int c = blockIdx.x * 4 + wid;
  STAMP_AT(p, 7);
  LAS char* cb = sm + wid * CV_SZ;
  conv_build(p, cb, c, lane);
  WAITL0();
  STAMP_AT(p, 8);
  conv_compute(p, cb, c, lane);
  STAMP_AT(p, 9);
}

constexpr int G2_R = 4;
constexpr int G2_RING = G2_R * 5120;
constexpr int G2_RS = 336, G2_PART = 64 * G2_RS;
constexpr int G2_LDS = 4 * G2_PART;
DI void ldg16_sc1(u32x4& dst, const i32x4& srd, unsigned voff, unsigned soff) {
  asm volatile("buffer_load_dwordx4 %0, %1, %2, %3 offen sc1" : "=v"(dst) : "v"(voff), "s"(srd), "s"(soff) : "memory");
}
DI void g2_math(const Params& p, LAS char* sm, unsigned smb, int mt, int nt, int w, int lane) {
  const i32x4 srdD = make_srd(p.wd, F_ * D_ * 2), srdH = make_srd(p.ws + WS_HM, 54 * 128 * 1024);
  unsigned vd[5];
#pragma unroll
  for (int i = 0; i < 5; ++i) {
    const int idx = i * 64 + lane, rho = idx / 10, ch = idx - rho * 10;
    const int f = 16 * ((rho >> 2) & 1) + 8 * ((rho >> 4) & 1) + 4 * ((rho >> 3) & 1) + (rho & 3);
    vd[i] = f * 1280 + ch * 16;
  }
  const unsigned ring = smb + w * G2_RING;
  const unsigned dsb = nt * 160u;
  const unsigned vh = lane * 16, hsb = mt * 4096u;
  const int nsteps = w < 2 ? 14 : 13;
  auto issueW = [&](int i) {
    const unsigned fb = 4 * i + w, so = dsb + fb * 32 * 1280u, lo = ring + (i & (G2_R - 1)) * 5120;
#pragma unroll
    for (int j = 0; j < 5; ++j) dma16(srdD, vd[j], so, lo + j * 1024);
  };
  u32x4 hA[3][4];
  auto issueH = [&](u32x4 (&dst)[4], int i) {
    const unsigned fb = 4 * i + w, so = hsb + fb * 131072u;
#pragma unroll
    for (int j = 0; j < 4; ++j) ldg16_sc1(dst[j], srdH, vh, so + j * 1024);
  };
  f32x4 acc[5][4];
#pragma unroll
  for (int a = 0; a < 5; ++a)
#pragma unroll
    for (int b = 0; b < 4; ++b) acc[a][b] = (f32x4){0.f, 0.f, 0.f, 0.f};
  const int kg = lane >> 4, h = kg >> 1, pq = kg & 1, q4 = (lane & 15) >> 2, pp = lane & 3;
  LAS char* wrd = sm + w * G2_RING + (8 * h + 4 * pq + q4) * 160 + pp * 8;
  issueW(0); issueW(1); issueW(2); issueW(3);
  issueH(hA[0], 0); issueH(hA[1], 1);
  STAMP_AT(p, 11);
#define G2_STEP(i, cur, nxt2) do { \
      if ((i) + 2 < nsteps) issueH(hA[nxt2], (i) + 2); \
      \
    { const bool w2 = (i) >= 2 && (i) + 2 < nsteps, w3 = (i) >= 1 && (i) + 3 < nsteps, h1 = (i) + 1 < nsteps, h2 = (i) + 2 < nsteps; \
      const int cnt = (w2 ? 5 : 0) + (w3 ? 5 : 0) + (h1 ? 4 : 0) + (h2 ? 4 : 0); \
      if (cnt == 18) WAITV(18); else if (cnt == 13) WAITV(13); else if (cnt == 8) WAITV(8); else if (cnt == 4) WAITV(4); else WAITV(0); } \
    asm volatile("" : "+v"(hA[cur][0]), "+v"(hA[cur][1]), "+v"(hA[cur][2]), "+v"(hA[cur][3])); \
    LAS char* wp = wrd + ((i) & (G2_R - 1)) * 5120; \
    bf16x8 wf[5]; \
    _Pragma("unroll") for (int a = 0; a < 5; ++a) wf[a] = ldtr(wp + a * 32, wp + a * 32 + 2560); \
    WAITL0(); __builtin_amdgcn_sched_barrier(0); \
    if ((i) + G2_R < nsteps) issueW((i) + G2_R); \
    _Pragma("unroll") for (int a = 0; a < 5; ++a) _Pragma("unroll") for (int b = 0; b < 4; ++b) \
      acc[a][b] = __builtin_amdgcn_mfma_f32_16x16x32_bf16(wf[a], __builtin_bit_cast(bf16x8, hA[cur][b]), acc[a][b], 0, 0, 0); \
  } while (0)
  G2_STEP(0, 0, 2); G2_STEP(1, 1, 0); G2_STEP(2, 2, 1); G2_STEP(3, 0, 2); G2_STEP(4, 1, 0); G2_STEP(5, 2, 1);
  G2_STEP(6, 0, 2); G2_STEP(7, 1, 0); G2_STEP(8, 2, 1); G2_STEP(9, 0, 2); G2_STEP(10, 1, 0); G2_STEP(11, 2, 1);
  G2_STEP(12, 0, 2);
  if (w < 2) G2_STEP(13, 1, 0);
  STAMP_AT(p, 12);
  BAR();
#pragma unroll
  for (int a = 0; a < 5; ++a)
#pragma unroll
    for (int b = 0; b < 4; ++b) *(LAS f32x4*)(sm + w * G2_PART + (16 * b + (lane & 15)) * G2_RS + a * 64 + kg * 16) = acc[a][b];
}
DI void g2_final(const Params& p, LAS char* sm, int mt, int nt, int tid) {
#pragma unroll 1
  for (int task = tid; task < 640; task += 512) {
    const int tk = task / 10, ch = task - tk * 10;
    const int t = mt * 64 + tk, d0 = nt * 80 + ch * 8;
    f32x4 a = (f32x4){0.f, 0.f, 0.f, 0.f}, b = a;
#pragma unroll
    for (int w = 0; w < 4; ++w) { a += *(LAS f32x4*)(sm + w * G2_PART + tk * G2_RS + ch * 32); b += *(LAS f32x4*)(sm + w * G2_PART + tk * G2_RS + ch * 32 + 16); }
    const u32x4 x = *(const GAS u32x4*)(p.q + t * 640 + d0);
    const f32x2 st = *(const GAS f32x2*)(p.ws + WS_STATS + t * 8);
    float o[8];
#pragma unroll
    for (int e = 0; e < 8; ++e) {
      const float xv = (e & 1) ? bhi(x[e >> 1]) : blo(x[e >> 1]);
      const float z = b2f(((const GAS u16*)(p.ws + WS_ZT))[(d0 + e) * 2048 + t]);
      const float xn = rbf(rbf(xv - st.x) * st.y);
      const float base = rbf(xv + rbf(xn * z));
      const float f = e < 4 ? a[e] : b[e - 4];
      o[e] = base + rbf(f);
    }
    const u32x4 r = {pk2(o[0], o[1]), pk2(o[2], o[3]), pk2(o[4], o[5]), pk2(o[6], o[7])};
    *(GAS u32x4*)(p.out + t * 640 + d0) = r;
  }
}
__global__ __launch_bounds__(512, 2) void k_gemm2(Params p) {
  extern __shared__ __attribute__((aligned(16))) char smem_raw[];
  LAS char* sm = (LAS char*)smem_raw;
  const unsigned smb = (unsigned)(size_t)sm;
  const int lane = threadIdx.x & 63, wid = __builtin_amdgcn_readfirstlane(threadIdx.x >> 6);
  const int b = blockIdx.x, x = b & 7, i = b >> 3, mt = x * 4 + (i & 3), nt = i >> 2;
  STAMP_AT(p, 10);
  if (wid < 4) g2_math(p, sm, smb, mt, nt, wid, lane);
  else BAR();
  BAR();
  g2_final(p, sm, mt, nt, threadIdx.x);
  STAMP_AT(p, 13);
}

extern "C" void kernel_launch(void* const* d_in, const int* in_sizes, int n_in, void* d_out, int out_size, void* d_ws, size_t ws_size, hipStream_t stream) {
  Params p;
  p.q = (const u16*)d_in[0]; p.k = (const u16*)d_in[1]; p.v = (const u16*)d_in[2]; p.sk = (const u16*)d_in[3]; p.h = (const u16*)d_in[4]; p.dv = (const u16*)d_in[5];
  p.wg = (const u16*)d_in[7]; p.wu = (const u16*)d_in[8]; p.wd = (const u16*)d_in[9]; p.out = (u16*)d_out; p.ws = (char*)d_ws;
  (void)hipFuncSetAttribute((const void*)k_gemm1, hipFuncAttributeMaxDynamicSharedMemorySize, G1_LDS);
  (void)hipFuncSetAttribute((const void*)k_conv, hipFuncAttributeMaxDynamicSharedMemorySize, 4 * CV_SZ);
  (void)hipFuncSetAttribute((const void*)k_gemm2, hipFuncAttributeMaxDynamicSharedMemorySize, G2_LDS);
  k_prep<<<40, 512, 0, stream>>>(p);
  k_gemm1<<<216, 512, G1_LDS, stream>>>(p);
  k_conv<<<160, 256, 4 * CV_SZ, stream>>>(p);
  k_gemm2<<<256, 512, G2_LDS, stream>>>(p);
}
#define HAVE_INTER 1
```

```cpp
#define STAMP 1
#define DIAG_B 100
#define DIAG_W 0
#define DIAG_I 2
#include <hip/hip_runtime.h>
#include <stdint.h>
typedef unsigned short u16;
typedef __bf16 bf16x8 __attribute__((ext_vector_type(8)));
typedef __bf16 bf16x2 __attribute__((ext_vector_type(2)));
typedef short s16x4 __attribute__((ext_vector_type(4)));
typedef float f32x2 __attribute__((ext_vector_type(2)));
typedef float f32x4 __attribute__((ext_vector_type(4)));
typedef unsigned u32x2 __attribute__((ext_vector_type(2)));
typedef unsigned u32x4 __attribute__((ext_vector_type(4)));
typedef int i32x4 __attribute__((ext_vector_type(4)));
#define LAS __attribute__((address_space(3)))
#define GAS __attribute__((address_space(1)))
#define DI __device__ __forceinline__
typedef GAS unsigned gu32;

#define TOUCH 1
#define TOUCH_CODE 0
#define CODE_BACK 0
#define NT_G1 0
#define NT_KV 1
#define NT_H 1
#define NT_WD 0
#define TLBW 0
constexpr int S_ = 2048, D_ = 640, F_ = 1728;
constexpr unsigned CW_EPOCH = 0;
constexpr unsigned CW_ARRIVE = 1024;
constexpr unsigned CW_KV = 4096;
constexpr unsigned CW_ST = 16384;
constexpr unsigned CW_HM = 24576;
constexpr unsigned CW_Z = 40960;
constexpr unsigned WS_STATS = 65536;
constexpr unsigned WS_KVT = 131072;
constexpr unsigned WS_ZT = WS_KVT + D_ * S_ * 2;
constexpr unsigned WS_HM = WS_ZT + D_ * S_ * 2;
constexpr unsigned WS_END = WS_HM + 54 * 128 * 1024;
constexpr unsigned WS_DBG = 32u << 20;
constexpr int G1_SLOT = 49152;
constexpr int L_CS = 153856, L_ST = L_CS + 512, L_S2 = L_ST + 2048, L_END = L_S2 + 512 + 64 + 256;
constexpr int L_DBG = L_END, L_ALLOC = L_END + 2048;
constexpr int L_HR = 0, L_WR = 49152, G2_WSLOT = 20480;
constexpr int L_CVG = 90112, L_CVP = 69632;
constexpr int L_ZS = 131072, ZS_RS = 144;
constexpr int L_CP = L_CVG + 2 * 21216, L_PF = L_CS - 64;
constexpr int L_PFP = L_S2 + 512 - 0;
constexpr int G2_RS = 336, G2_PART = 64 * G2_RS;
constexpr int CV_H = 4160, CV_U = 4 * CV_H, CV_SZ = CV_U + 4576;
static_assert(L_ZS + 80 * ZS_RS <= L_PF, "lds");
static_assert(L_CP + 2 * 8192 <= L_PF, "lds");
static_assert(L_CVP + 4 * CV_SZ + 64 <= L_S2, "lds");
constexpr int NCH_P = 6;
constexpr int NCH_G = D_ - 40 * NCH_P;
static_assert(NCH_G <= 2 * 216 && NCH_G % 2 == 0, "conv split");
constexpr unsigned SPIN_MAX = 1u << 16;

struct Params {
  const u16 *q, *k, *v, *sk, *h, *dv, *wg, *wu, *wd;
  u16* out; char* ws;
};

typedef const __attribute__((address_space(4))) Params* KP;
DI Params reload(KP kp) {
  asm volatile("" : "+s"(kp));
  Params r; r.q = kp->q; r.k = kp->k; r.v = kp->v; r.sk = kp->sk; r.h = kp->h; r.dv = kp->dv; r.wg = kp->wg; r.wu = kp->wu; r.wd = kp->wd; r.out = kp->out; r.ws = kp->ws;
  return r;
}
DI i32x4 make_srd(const void* p, unsigned bytes) {
  i32x4 r; unsigned long long a = (unsigned long long)p;
  r.x = (int)(unsigned)a;
  r.y = (int)((unsigned)(a >> 32) & 0xffffu);
  r.z = (int)bytes; r.w = 0x00020000; return r;
}
DI void dma16(const i32x4& srd, unsigned voff, unsigned soff, unsigned lds) {
  asm volatile("s_mov_b32 m0, %3\n\ts_nop 0\n\tbuffer_load_dwordx4 %0, %1, %2 offen lds" :: "v"(voff), "s"(srd), "s"(soff), "s"(lds) : "memory");
}
DI void dma16_sc1(const i32x4& srd, unsigned voff, unsigned soff, unsigned lds) {
  asm volatile("s_mov_b32 m0, %3\n\ts_nop 0\n\tbuffer_load_dwordx4 %0, %1, %2 offen sc1 lds" :: "v"(voff), "s"(srd), "s"(soff), "s"(lds) : "memory");
}
template <int NT = 0>
DI void dma16i(const i32x4& srd, unsigned voff, unsigned sbase, int simm, unsigned lbase, int limm) {
  unsigned t;
  if (NT) asm volatile("s_add_u32 m0, %4, %6\n\ts_add_u32 %0, %3, %5\n\tbuffer_load_dwordx4 %1, %2, %0 offen nt lds" : "=&s"(t) : "v"(voff), "s"(srd), "s"(sbase), "s"(lbase), "i"(simm), "i"(limm) : "memory", "scc");
  else asm volatile("s_add_u32 m0, %4, %6\n\ts_add_u32 %0, %3, %5\n\tbuffer_load_dwordx4 %1, %2, %0 offen lds" : "=&s"(t) : "v"(voff), "s"(srd), "s"(sbase), "s"(lbase), "i"(simm), "i"(limm) : "memory", "scc");
}
DI void dma16i_sc1(const i32x4& srd, unsigned voff, unsigned sbase, int simm, unsigned lbase, int limm) {
  unsigned t;
  asm volatile("s_add_u32 m0, %4, %6\n\ts_add_u32 %0, %3, %5\n\tbuffer_load_dwordx4 %1, %2, %0 offen sc1 lds" : "=&s"(t) : "v"(voff), "s"(srd), "s"(sbase), "s"(lbase), "i"(simm), "i"(limm) : "memory", "scc");
}
template <int NT = 0>
DI void ld16i(u32x4& d, const i32x4& srd, unsigned voff, unsigned sbase, int simm) {
  unsigned t;
  if (NT) asm volatile("s_add_u32 %1, %4, %5\n\tbuffer_load_dwordx4 %0, %2, %3, %1 offen nt" : "=v"(d), "=&s"(t) : "v"(voff), "s"(srd), "s"(sbase), "i"(simm) : "memory", "scc");
  else asm volatile("s_add_u32 %1, %4, %5\n\tbuffer_load_dwordx4 %0, %2, %3, %1 offen" : "=v"(d), "=&s"(t) : "v"(voff), "s"(srd), "s"(sbase), "i"(simm) : "memory", "scc");
}
template <int NT = 0>
DI void ld16(u32x4& d, const i32x4& srd, unsigned voff, unsigned soff) {
  if (NT) asm volatile("buffer_load_dwordx4 %0, %1, %2, %3 offen nt" : "=v"(d) : "v"(voff), "s"(srd), "s"(soff) : "memory");
  else asm volatile("buffer_load_dwordx4 %0, %1, %2, %3 offen" : "=v"(d) : "v"(voff), "s"(srd), "s"(soff) : "memory");
}
DI void ld16_sc1(u32x4& d, const i32x4& srd, unsigned voff, unsigned soff) { asm volatile("buffer_load_dwordx4 %0, %1, %2, %3 offen sc1" : "=v"(d) : "v"(voff), "s"(srd), "s"(soff) : "memory"); }
DI void ld8_sc1(u32x2& d, const i32x4& srd, unsigned voff, unsigned soff) { asm volatile("buffer_load_dwordx2 %0, %1, %2, %3 offen sc1" : "=v"(d) : "v"(voff), "s"(srd), "s"(soff) : "memory"); }
DI void ld8(u32x2& d, const i32x4& srd, unsigned voff, unsigned soff) { asm volatile("buffer_load_dwordx2 %0, %1, %2, %3 offen" : "=v"(d) : "v"(voff), "s"(srd), "s"(soff) : "memory"); }
DI void ld4_sc1(unsigned& d, const i32x4& srd, unsigned voff) { asm volatile("buffer_load_dword %0, %1, %2, 0 offen sc1" : "=v"(d) : "v"(voff), "s"(srd) : "memory"); }
DI void touchg(const void* base, unsigned voff, unsigned lds) {
  asm volatile("s_mov_b32 m0, %2\n\ts_nop 0\n\tglobal_load_lds_dword %0, %1" :: "v"(voff), "s"(base), "s"(lds) : "memory");
}
DI void touchg_nt(const void* base, unsigned voff, unsigned lds) {
  asm volatile("s_mov_b32 m0, %2\n\ts_nop 0\n\tglobal_load_lds_dword %0, %1 nt" :: "v"(voff), "s"(base), "s"(lds) : "memory");
}
DI void touch4(const i32x4& srd, unsigned voff, unsigned soff, unsigned lds) {
  asm volatile("s_mov_b32 m0, %3\n\ts_nop 0\n\tbuffer_load_dword %0, %1, %2 offen lds" :: "v"(voff), "s"(srd), "s"(soff), "s"(lds) : "memory");
}
DI void touch4_sc1(const i32x4& srd, unsigned voff, unsigned soff, unsigned lds) {
  asm volatile("s_mov_b32 m0, %3\n\ts_nop 0\n\tbuffer_load_dword %0, %1, %2 offen sc1 lds" :: "v"(voff), "s"(srd), "s"(soff), "s"(lds) : "memory");
}
#define WAITV(n) asm volatile("s_waitcnt vmcnt(%0)" :: "i"(n) : "memory")
#define BAR() asm volatile("s_waitcnt lgkmcnt(0)\n\ts_barrier" ::: "memory")
#define PIN(x) asm volatile("" : "+v"(x))
DI float b2f(u16 x) { return __uint_as_float(((unsigned)x) << 16); }
DI float blo(unsigned x) { return __uint_as_float(x << 16); }
DI float bhi(unsigned x) { return __uint_as_float(x & 0xffff0000u); }
DI unsigned pk2(float lo, float hi) { bf16x2 r = {(__bf16)lo, (__bf16)hi}; return __builtin_bit_cast(unsigned, r); }
DI float rbf(float x) { return (float)(__bf16)x; }
DI float dot2(unsigned a, unsigned b, float c) { return __builtin_amdgcn_fdot2_f32_bf16(__builtin_bit_cast(bf16x2, a), __builtin_bit_cast(bf16x2, b), c, false); }
template <int CTRL> DI float dpp_add(float x) { return x + __int_as_float(__builtin_amdgcn_update_dpp(0, __float_as_int(x), CTRL, 0xf, 0xf, true)); }
DI float row16_sum(float x) { x = dpp_add<0x128>(x); x = dpp_add<0x124>(x); x = dpp_add<0x122>(x); x = dpp_add<0x121>(x); return x; }
DI bf16x8 ldtr(LAS char* p0, LAS char* p1) {
  s16x4 a = __builtin_amdgcn_ds_read_tr16_b64_v4i16((LAS s16x4*)p0), b = __builtin_amdgcn_ds_read_tr16_b64_v4i16((LAS s16x4*)p1);
  return __builtin_bit_cast(bf16x8, __builtin_shufflevector(a, b, 0, 1, 2, 3, 4, 5, 6, 7));
}
DI void poll_all(const char* ws, unsigned off, unsigned tag) {
  gu32* a = (gu32*)(ws + off);
#pragma unroll 1
  for (unsigned it = 0; it < SPIN_MAX; ++it) {
    const unsigned v = __hip_atomic_load(a, __ATOMIC_RELAXED, __HIP_MEMORY_SCOPE_AGENT);
    if (__all(v == tag)) break;
    __builtin_amdgcn_s_sleep(2);
  }
  asm volatile("" ::: "memory");
}
DI void flag_set(const char* ws, unsigned off, unsigned tag, int lane) {
  if (lane == 0) __hip_atomic_store((gu32*)(ws + off), tag, __ATOMIC_RELAXED, __HIP_MEMORY_SCOPE_AGENT);
}
extern __shared__ __attribute__((aligned(16))) char smem_dbg[];
#define STAMP_AT(idx) do { const unsigned long long t_ = __builtin_amdgcn_s_memrealtime(), c_ = __builtin_amdgcn_s_memtime(); \
    if (lane == 0) *(LAS u32x4*)((LAS char*)smem_dbg + L_DBG + ((threadIdx.x >> 6) * 16 + (idx)) * 16) = (u32x4){(unsigned)t_, (unsigned)(t_ >> 32), (unsigned)c_, (unsigned)(c_ >> 32)}; } while (0)
#define STAMP_INIT() do { if (lane < 16) *(LAS u32x4*)((LAS char*)smem_dbg + L_DBG + ((threadIdx.x >> 6) * 16 + lane) * 16) = (u32x4){0u, 0u, 0u, 0u}; } while (0)
#define STAMP_FLUSH() do { if (lane < 16) { const u32x4 v_ = *(LAS u32x4*)((LAS char*)smem_dbg + L_DBG + ((threadIdx.x >> 6) * 16 + lane) * 16); \
    ((u32x2*)(p.ws + WS_DBG))[(blockIdx.x * 8 + (threadIdx.x >> 6)) * 16 + lane] = (u32x2){v_.x, v_.y}; \
    ((u32x2*)(p.ws + WS_DBG + (8 << 20)))[(blockIdx.x * 8 + (threadIdx.x >> 6)) * 16 + lane] = (u32x2){v_.z, v_.w}; } } while (0)
#define STAMP_CY(idx) do {} while (0)

template <int NX, class Hook>
DI void task_kv(const Params& p, int cg, int tb, int lane, Hook hook) {
  const int c0 = cg * 64 + 8 * (lane & 7), t0 = tb * 64 + 8 * (lane >> 3);
  const i32x4 rk = make_srd(p.k, S_ * D_ * 2), rv = make_srd(p.v, S_ * D_ * 2), rsk = make_srd(p.sk, D_ * 8);
  u32x4 kk[11], vv[8], ss[4];
#pragma unroll
  for (int j = 0; j < 4; ++j) ld16(ss[j], rsk, (unsigned)(c0 * 8), j * 16);
#pragma unroll
  for (int j = 0; j < 11; ++j) { const int t = t0 - 3 + j; ld16<NT_KV>(kk[j], rk, t >= 0 ? (unsigned)(t * 1280 + c0 * 2) : 0xfffffff0u, 0); }
#pragma unroll
  for (int j = 0; j < 8; ++j) ld16<NT_KV>(vv[j], rv, (unsigned)(t0 * 1280 + c0 * 2), j * 1280);
  hook();
  const __amdgpu_buffer_rsrc_t ro = __builtin_amdgcn_make_buffer_rsrc((void*)(p.ws + WS_KVT), 0, D_ * S_ * 2, 0x00020000);
  WAITV(NX);
  STAMP_AT(12);
#pragma unroll
  for (int j = 0; j < 4; ++j) PIN(ss[j]);
#pragma unroll
  for (int j = 0; j < 11; ++j) PIN(kk[j]);
#pragma unroll
  for (int j = 0; j < 8; ++j) PIN(vv[j]);
#pragma unroll
  for (int cp = 0; cp < 4; ++cp) {
    const f32x2 s0 = {blo(ss[cp].x), blo(ss[cp].z)}, s1 = {bhi(ss[cp].x), bhi(ss[cp].z)}, s2 = {blo(ss[cp].y), blo(ss[cp].w)}, s3 = {bhi(ss[cp].y), bhi(ss[cp].w)};
    f32x2 kx[11];
#pragma unroll
    for (int j = 0; j < 11; ++j) { const unsigned w = kk[j][cp]; kx[j] = (f32x2){blo(w), bhi(w)}; }
    f32x2 o[8];
    __builtin_amdgcn_sched_barrier(0);
#pragma unroll
    for (int j = 0; j < 8; ++j) o[j] = kx[j] * s3;
    __builtin_amdgcn_sched_barrier(0);
#pragma unroll
    for (int j = 0; j < 8; ++j) o[j] = kx[j + 1] * s2 + o[j];
    __builtin_amdgcn_sched_barrier(0);
#pragma unroll
    for (int j = 0; j < 8; ++j) o[j] = kx[j + 2] * s1 + o[j];
    __builtin_amdgcn_sched_barrier(0);
#pragma unroll
    for (int j = 0; j < 8; ++j) o[j] = kx[j + 3] * s0 + o[j];
    __builtin_amdgcn_sched_barrier(0);
    f32x2 vx[8];
#pragma unroll
    for (int j = 0; j < 8; ++j) { const unsigned w = vv[j][cp]; vx[j] = (f32x2){blo(w), bhi(w)}; }
    __builtin_amdgcn_sched_barrier(0);
#pragma unroll
    for (int j = 0; j < 8; ++j) o[j] = o[j] * vx[j];
    __builtin_amdgcn_sched_barrier(0);
    const u32x4 r0 = {pk2(o[7].x, o[6].x), pk2(o[5].x, o[4].x), pk2(o[3].x, o[2].x), pk2(o[1].x, o[0].x)};
    const u32x4 r1 = {pk2(o[7].y, o[6].y), pk2(o[5].y, o[4].y), pk2(o[3].y, o[2].y), pk2(o[1].y, o[0].y)};
    __builtin_amdgcn_raw_buffer_store_b128(r0, ro, (unsigned)((c0 + 2 * cp) * 4096 + (2040 - t0) * 2), 0, 16);
    __builtin_amdgcn_raw_buffer_store_b128(r1, ro, (unsigned)((c0 + 2 * cp + 1) * 4096 + (2040 - t0) * 2), 0, 16);
  }
}
template <bool PRE>
DI void task_stats(const Params& p, int id, int lane) {
  const i32x4 rq = make_srd(p.q, S_ * D_ * 2);
  const int row = id * 4 + (lane >> 4), l16 = lane & 15;
  u32x4 x[5];
#pragma unroll
  for (int j = 0; j < 5; ++j) ld16<NT_G1>(x[j], rq, (unsigned)row * 1280u + l16 * 16, j * 256);
  const __amdgpu_buffer_rsrc_t rs = __builtin_amdgcn_make_buffer_rsrc((void*)(p.ws + WS_STATS), 0, S_ * 8, 0x00020000);
  if (PRE) BAR();
  WAITV(0);
  STAMP_AT(12);
#pragma unroll
  for (int j = 0; j < 5; ++j) PIN(x[j]);
  float s4[4] = {0.f, 0.f, 0.f, 0.f};
#pragma unroll
  for (int j = 0; j < 5; ++j)
#pragma unroll
    for (int e = 0; e < 4; ++e) s4[e] = dot2(x[j][e], 0x3f803f80u, s4[e]);
  const float mean = row16_sum((s4[0] + s4[1]) + (s4[2] + s4[3])) * (1.0f / 640.0f);
  f32x2 q2[2] = {{0.f, 0.f}, {0.f, 0.f}};
#pragma unroll
  for (int j = 0; j < 5; ++j)
#pragma unroll
    for (int e = 0; e < 4; ++e) { const unsigned w = x[j][e]; const f32x2 d = (f32x2){blo(w), bhi(w)} - mean; q2[e & 1] = d * d + q2[e & 1]; }
  const float var = row16_sum((q2[0].x + q2[0].y) + (q2[1].x + q2[1].y)) * (1.0f / 640.0f);
  const float mb = rbf(mean), sb = rbf(__builtin_amdgcn_sqrtf(rbf(rbf(var) + 1e-5f)));
  u32x2 o; o.x = __float_as_uint(mb); o.y = __float_as_uint(1.0f / sb);
  __builtin_amdgcn_raw_buffer_store_b64(o, rs, l16 == 0 ? (unsigned)row * 8u : 0xfffffff0u, 0, 16);
}

struct ConvIn { u32x4 L0[4], L1[4], L2[4], U[4]; unsigned dv; };
DI void conv_load_h(const Params& p, ConvIn& in, int c, int lane) {
  const i32x4 rh = make_srd(p.h, D_ * S_ * 2), rd = make_srd(p.dv, D_ * 2);
  const unsigned vo = lane * 16, so = c * 4096u;
  asm volatile("buffer_load_ushort %0, %1, %2, 0 offen" : "=v"(in.dv) : "v"(c * 2u), "s"(rd) : "memory");
#pragma unroll
  for (int i = 0; i < 4; ++i) { ld16<NT_H>(in.L0[i], rh, vo, so + i * 1024); ld16(in.L1[i], rh, vo + 4, so + i * 1024); ld16(in.L2[i], rh, vo + 8, so + i * 1024); }
}
DI void conv_load_u(const Params& p, ConvIn& in, int c, int lane) {
  const i32x4 ru = make_srd(p.ws + WS_KVT, D_ * S_ * 2);
#pragma unroll
  for (int i = 0; i < 4; ++i) ld16_sc1(in.U[i], ru, lane * 16, c * 4096u + i * 1024);
}
DI void conv_pin(ConvIn& in) {
#pragma unroll
  for (int i = 0; i < 4; ++i) { PIN(in.L0[i]); PIN(in.L1[i]); PIN(in.L2[i]); PIN(in.U[i]); }
  PIN(in.dv);
}
DI void conv_build(const ConvIn& in, LAS char* cb, int lane) {
#pragma unroll
  for (int i = 0; i < 4; ++i) {
    const unsigned o = (i * 64 + lane + 2) * 16;
    u32x4 c0, c2;
#pragma unroll
    for (int e = 0; e < 4; ++e) { c0[e] = __builtin_amdgcn_alignbit(in.L1[i][e], in.L0[i][e], 16); c2[e] = __builtin_amdgcn_alignbit(in.L2[i][e], in.L1[i][e], 16); }
    *(LAS u32x4*)(cb + 0 * CV_H + o) = c0;
    *(LAS u32x4*)(cb + 1 * CV_H + o) = in.L1[i];
    *(LAS u32x4*)(cb + 2 * CV_H + o) = c2;
    *(LAS u32x4*)(cb + 3 * CV_H + o) = in.L2[i];
    *(LAS u32x4*)(cb + CV_U + (i * 64 + lane) * 16) = in.U[i];
  }
  const u32x4 z4 = {0u, 0u, 0u, 0u};
  if (lane < 30) *(LAS u32x4*)(cb + CV_U + 4096 + lane * 16) = z4;
  if (lane == 0) {
    const unsigned w0 = in.L0[0].x, w1 = in.L0[0].y;
#pragma unroll
    for (int cc = 0; cc < 4; ++cc) *(LAS u32x4*)(cb + cc * CV_H) = z4;
    *(LAS u32x4*)(cb + 0 * CV_H + 16) = (u32x4){0u, 0u, 0u, w0 << 16};
    *(LAS u32x4*)(cb + 1 * CV_H + 16) = (u32x4){0u, 0u, 0u, w0};
    *(LAS u32x4*)(cb + 2 * CV_H + 16) = (u32x4){0u, 0u, w0 << 16, __builtin_amdgcn_alignbit(w1, w0, 16)};
    *(LAS u32x4*)(cb + 3 * CV_H + 16) = (u32x4){0u, 0u, w0, w1};
  }
}
template <int TRIPS>
DI void conv_mfma(LAS char* cb, int d0, int lane, f32x4 (&acc)[8]) {
  const int r = lane & 15, kg = lane >> 4, kap = kg >> 1, bp = kg & 1;
  unsigned a0 = (unsigned)(size_t)cb + (r & 3) * CV_H + 32 * kap + 16 * bp + 2 * (r & 12) + d0 * 64;
  unsigned a1 = a0 + 8;
  unsigned ub = (unsigned)(size_t)cb + CV_U + (4064 - 3584) - 32 * r + 32 * kap + 16 * bp + d0 * 64;
  PIN(a0); PIN(a1); PIN(ub);
#pragma unroll
  for (int g = 0; g < 8; ++g) acc[g] = (f32x4){0.f, 0.f, 0.f, 0.f};
  bf16x8 A[8], B[8];
#define CV_LDA(g) do { const s16x4 x0_ = *(LAS s16x4*)(size_t)(a0 + (g) * 512), x1_ = *(LAS s16x4*)(size_t)(a1 + (g) * 512); \
    A[g] = __builtin_bit_cast(bf16x8, __builtin_shufflevector(x0_, x1_, 0, 1, 2, 3, 4, 5, 6, 7)); } while (0)
#define CV_LDB(g) do { B[g] = *(LAS bf16x8*)(size_t)(ub + (7 - (g)) * 512); } while (0)
#pragma unroll
  for (int g = 0; g < 8; ++g) { CV_LDA(g); CV_LDB(7 - g); }
#pragma unroll 1
  for (int d = 0; d < TRIPS; ++d) {
    a0 += 64; a1 += 64; ub += 64; PIN(a0); PIN(a1); PIN(ub);
#pragma unroll
    for (int gm = 0; gm < 8; ++gm) {
#pragma unroll
      for (int gp = 7 - gm; gp >= 0; --gp) acc[gm + gp] = __builtin_amdgcn_mfma_f32_16x16x32_bf16(A[gm], B[gp], acc[gm + gp], 0, 0, 0);
      __builtin_amdgcn_sched_barrier(0);
      CV_LDA(gm); CV_LDB(7 - gm);
      __builtin_amdgcn_sched_barrier(0);
    }
  }
}
DI void conv_store(const Params& p, LAS char* cb, int c, int lane, unsigned dvb, const f32x4 (&acc)[8]) {
  const int r = lane & 15, kg = lane >> 4;
  const float dv = __uint_as_float(dvb << 16);
  const __amdgpu_buffer_rsrc_t rz = __builtin_amdgcn_make_buffer_rsrc((void*)(p.ws + WS_ZT), 0, D_ * S_ * 2, 0x00020000);
  u32x2 z[8];
#pragma unroll
  for (int g = 0; g < 8; ++g) {
    const int t = 256 * g + 16 * r + 4 * kg;
    const u32x2 kv = *(LAS u32x2*)(cb + CV_U + (2044 - t) * 2);
    const float y0 = fmaf(bhi(kv.y), dv, acc[g][0]), y1 = fmaf(blo(kv.y), dv, acc[g][1]), y2 = fmaf(bhi(kv.x), dv, acc[g][2]), y3 = fmaf(blo(kv.x), dv, acc[g][3]);
    z[g].x = pk2(y0, y1); z[g].y = pk2(y2, y3);
  }
#pragma unroll
  for (int g = 0; g < 8; g += 2) {
    const auto s0 = __builtin_amdgcn_permlane16_swap(z[g].x, z[g + 1].x, false, false);
    const auto s1 = __builtin_amdgcn_permlane16_swap(z[g].y, z[g + 1].y, false, false);
    const u32x4 o = {s0[0], s1[0], s0[1], s1[1]};
    const int gg = g + (kg & 1);
    __builtin_amdgcn_raw_buffer_store_b128(o, rz, c * 4096u + (256 * gg + 16 * r + 4 * (kg & 2)) * 2, 0, 16);
  }
}
struct ConvHalf { u32x4 La[4], Lb[4], U[2]; unsigned dv; };
DI void convh_load_h(const Params& p, ConvHalf& in, int c, int half, int lane) {
  const i32x4 rh = make_srd(p.h, D_ * S_ * 2), rd = make_srd(p.dv, D_ * 2);
  const unsigned vo = lane * 16 + half * 4, so = c * 4096u;
  asm volatile("buffer_load_ushort %0, %1, %2, 0 offen" : "=v"(in.dv) : "v"(c * 2u), "s"(rd) : "memory");
#pragma unroll
  for (int i = 0; i < 4; ++i) { ld16<NT_H>(in.La[i], rh, vo, so + i * 1024); ld16(in.Lb[i], rh, vo + 4, so + i * 1024); }
}
DI void convh_load_u(const Params& p, ConvHalf& in, int c, int half, int lane) {
  const i32x4 ru = make_srd(p.ws + WS_KVT, D_ * S_ * 2);
#pragma unroll
  for (int i = 0; i < 2; ++i) ld16_sc1(in.U[i], ru, lane * 16, c * 4096u + (half * 2 + i) * 1024);
}
DI void convh_build(ConvHalf& in, LAS char* cb, int half, int lane) {
#pragma unroll
  for (int i = 0; i < 4; ++i) { PIN(in.La[i]); PIN(in.Lb[i]); }
  PIN(in.U[0]); PIN(in.U[1]); PIN(in.dv);
  LAS char* hb = cb + 2 * half * CV_H;
#pragma unroll
  for (int i = 0; i < 4; ++i) {
    const unsigned o = (i * 64 + lane + 2) * 16;
    u32x4 c0;
#pragma unroll
    for (int e = 0; e < 4; ++e) c0[e] = __builtin_amdgcn_alignbit(in.Lb[i][e], in.La[i][e], 16);
    *(LAS u32x4*)(hb + o) = c0;
    *(LAS u32x4*)(hb + CV_H + o) = in.Lb[i];
  }
#pragma unroll
  for (int i = 0; i < 2; ++i) *(LAS u32x4*)(cb + CV_U + ((half * 2 + i) * 64 + lane) * 16) = in.U[i];
  if (half == 0) {
    const u32x4 z4 = {0u, 0u, 0u, 0u};
    if (lane < 30) *(LAS u32x4*)(cb + CV_U + 4096 + lane * 16) = z4;
    if (lane == 0) {
      const unsigned w0 = in.La[0].x, w1 = in.La[0].y;
#pragma unroll
      for (int cc = 0; cc < 4; ++cc) *(LAS u32x4*)(cb + cc * CV_H) = z4;
      *(LAS u32x4*)(cb + 0 * CV_H + 16) = (u32x4){0u, 0u, 0u, w0 << 16};
      *(LAS u32x4*)(cb + 1 * CV_H + 16) = (u32x4){0u, 0u, 0u, w0};
      *(LAS u32x4*)(cb + 2 * CV_H + 16) = (u32x4){0u, 0u, w0 << 16, __builtin_amdgcn_alignbit(w1, w0, 16)};
      *(LAS u32x4*)(cb + 3 * CV_H + 16) = (u32x4){0u, 0u, w0, w1};
    }
  }
}

DI void g2_issue_w(const i32x4& srdD, const unsigned (&vd)[5], unsigned smb, int nt, int w, int j) {
  const unsigned sb = nt * 160u + (j < 13 ? w : (w < 2 ? w : 1)) * 40960u, lb = smb + L_WR + w * 5120;
#pragma unroll
  for (int i = 0; i < 5; ++i) dma16i<NT_WD>(srdD, vd[i], sb, j * 163840, lb, (j & 3) * G2_WSLOT + i * 1024);
}
DI void g2_wd_offsets(unsigned (&vd)[5], int lane) {
#pragma unroll
  for (int i = 0; i < 5; ++i) { const int idx = i * 64 + lane, rho = idx / 10, ch = idx - rho * 10; vd[i] = rho * 1280 + ch * 16; }
}
template <bool CONV>
DI void g1_loader(KP kp, Params p, unsigned long long pc0, LAS char* sm, unsigned smb, int rb, int ft, int mt, int nt, int hw, int lane, unsigned& tag, int cch, unsigned& dvb, u32x4 (&wy)[3][5]) {
  const i32x4 srdQ = make_srd(p.q, S_ * D_ * 2), srdW = make_srd((hw >> 1) ? p.wu : p.wg, D_ * F_ * 2), srdC = make_srd(p.ws, WS_KVT);
  const unsigned vq0 = (lane >> 3) * 1280 + (((lane & 7) ^ (lane >> 4)) * 16);
  const unsigned vq1 = (lane >> 3) * 1280 + (((lane & 7) ^ (4 + (lane >> 4))) * 16);
  const unsigned vw0 = (lane >> 3) * 3456 + (((lane & 7) ^ (2 * ((lane >> 4) & 1))) * 16);
  const unsigned vw1 = (lane >> 3) * 3456 + (((lane & 7) ^ (2 * (((lane >> 4) & 1) | 2))) * 16);
  const unsigned qb = (unsigned)(rb * 256 + 64 * hw) * 1280u, wb = (unsigned)(32 * (hw & 1)) * 3456u + ft * 128u;
  const unsigned lq = smb + hw * 8192, lw = smb + 32768 + (hw >> 1) * 8192 + (hw & 1) * 4096;
  auto issue = [&](int s, int slot) {
#pragma unroll
    for (int i = 0; i < 4; ++i) dma16i<NT_G1>(srdW, (i & 1) ? vw1 : vw0, wb, (s * 64 + 8 * i) * 3456, lw, slot * G1_SLOT + i * 1024);
#pragma unroll
    for (int i = 0; i < 8; ++i) dma16i<NT_G1>(srdQ, (i & 1) ? vq1 : vq0, qb, i * 10240 + s * 128, lq, slot * G1_SLOT + i * 1024);
  };
  const int xq = blockIdx.x & 7, wv = (blockIdx.x >> 3) * 4 + hw;
  const unsigned junk = smb + L_END - 256;
  const i32x4 srdD = make_srd(p.wd, F_ * D_ * 2);
  auto touch_slot = [&](int sl) {
    const int g = wv >> 5, sub = wv & 31, gsel = (sl + 1) % 3, st = sl + 3;
    unsigned fo = (((27 * (xq & 3)) >> 2) + (lane & 7)) * 128u; fo = fo < 3456u ? fo : 3328u;
    const unsigned vt = (unsigned)((xq >> 2) * 1024 + (sub & 15) * 64 + lane) * 1280u + st * 128u;
    const unsigned vw = (unsigned)(st * 64 + ((sub & 15) >> 1) * 8 + (lane >> 3)) * 3456u + fo;
    const bool stage = TOUCH && g == gsel;
    const void* base = (!stage || sub < 16) ? (const void*)p.q : ((sub & 1) ? (const void*)p.wu : (const void*)p.wg);
    touchg(base, stage ? (sub < 16 ? vt : vw) : 0u, junk);
  };
  unsigned ep; ld4_sc1(ep, srdC, CW_EPOCH);
  BAR();
  ConvHalf in;
  issue(0, 0); issue(1, 1);
  if (CONV) convh_load_h(p, in, cch, hw & 1, lane);
  touch_slot(-1);
  if (CONV) WAITV(22); else WAITV(13);
  PIN(ep); tag = __builtin_amdgcn_readfirstlane(ep) + 1u;
  if ((hw & 1) == 0 && lane == 0) *(LAS unsigned*)(sm + L_PF + (hw >> 1) * 4) = ~tag;
  BAR();
  if (hw == 0) flag_set(p.ws, CW_ARRIVE + blockIdx.x * 4, tag, lane);
  issue(2, 2); touch_slot(0);
  unsigned vd[5]; g2_wd_offsets(vd, lane);
  unsigned f1 = 0, f2 = 0; u32x2 st, st2;
  const unsigned f1off = lane < 16 ? CW_ST + (rb * 64 + hw * 16 + lane) * 16 : CW_ST + (mt * 16 + (lane & 15)) * 16;
  const unsigned f2off = CW_KV + ((CONV ? (cch >> 6) : 0) * 32 + (lane & 31)) * 16;
#pragma unroll
  for (int s = 1; s < 10; ++s) {
    const int nslot = (s + 2) % 3;
    if (s == 1) { if (CONV) WAITV(23); else WAITV(14); }
    else if (s == 6) WAITV(15);
    else if (s == 8) { if (CONV) WAITV(16); else WAITV(15); }
    else if (s == 9) WAITV(10);
    else WAITV(14);
    BAR();
    if (s == 4 || s == 7) p = reload(kp);
    if (s == 5) ld4_sc1(f1, srdC, f1off);
    if (s == 7) {
      PIN(f1);
      if (!__all(f1 == tag)) poll_all(p.ws, f1off, tag);
      ld8_sc1(st, srdC, WS_STATS + (rb * 256 + hw * 64 + lane) * 8, 0);
      ld8_sc1(st2, srdC, WS_STATS + (mt * 64 + lane) * 8, 0);
      if (CONV) ld4_sc1(f2, srdC, f2off);
    }
    if (s <= 7) issue(s + 2, nslot);
    if (s == 8) { g2_issue_w(srdD, vd, smb, nt, hw, 0); g2_issue_w(srdD, vd, smb, nt, hw, 1); }
    if (s <= 6) touch_slot(s);
    if (s == 9) {
      if (CONV) {
        PIN(f2);
        if (!__all(f2 == tag)) poll_all(p.ws, f2off, tag);
        convh_load_u(p, in, cch, hw & 1, lane);
      }
      PIN(st); PIN(st2);
      *(LAS u32x2*)(sm + L_ST + (hw * 64 + lane) * 8) = st;
      if (hw == 0) *(LAS u32x2*)(sm + L_S2 + lane * 8) = st2;
    }
  }
  BAR();
#pragma unroll
  for (int j = 0; j < 3; ++j)
#pragma unroll
    for (int i = 0; i < 5; ++i) ld16i<NT_WD>(wy[j][i], srdD, vd[i], nt * 160u + hw * 40960u, (j + 2) * 163840);
  if (CONV) { WAITV(15); convh_build(in, sm + L_CVG + (hw >> 1) * CV_SZ, hw & 1, lane); dvb = in.dv; }
  WAITV(0);
#pragma unroll
  for (int j = 0; j < 3; ++j)
#pragma unroll
    for (int i = 0; i < 5; ++i) PIN(wy[j][i]);
}

template <bool SWAP>
DI void g1_epilogue(const Params& p, LAS char* sm, int rb, int ft, int wr, int wc, int lane, const f32x4 (&a1)[2][8], const f32x4 (&a2)[2][8], const f32x4 (&csg)[2], const f32x4 (&csu)[2], const f32x2 (&st)[8]) {
  const __amdgpu_buffer_rsrc_t rh = __builtin_amdgcn_make_buffer_rsrc((void*)(p.ws + WS_HM), 0, 54 * 128 * 1024, 0x00020000);
  const unsigned hb = (unsigned)((ft * 2 + wc) * 128 + rb * 16 + wr * 8) * 1024u + lane * 16;
#pragma unroll
  for (int m = 0; m < 8; ++m) {
    asm volatile("; gated epilogue, operand order %0" :: "i"(SWAP ? 1 : 0));
    const float nm = -st[m].x, rs = st[m].y, rsl = rs * -1.44269504089f, rs2 = rs * rs;
    f32x2 tg[4], tu[4], e[4];
#pragma unroll
    for (int h = 0; h < 4; ++h) {
      const int a = h >> 1, i = (h & 1) * 2;
      const f32x4 ga = SWAP ? a2[a][m] : a1[a][m], ua = SWAP ? a1[a][m] : a2[a][m];
      tg[h] = (f32x2){csg[a][i], csg[a][i + 1]} * nm + (f32x2){ga[i], ga[i + 1]};
      tu[h] = (f32x2){csu[a][i], csu[a][i + 1]} * nm + (f32x2){ua[i], ua[i + 1]};
    }
#pragma unroll
    for (int h = 0; h < 4; ++h) { const f32x2 gl = tg[h] * rsl; e[h].x = __builtin_amdgcn_exp2f(gl.x); e[h].y = __builtin_amdgcn_exp2f(gl.y); }
#pragma unroll
    for (int h = 0; h < 4; ++h) { tu[h] = (tg[h] * tu[h]) * rs2; e[h] = e[h] + 1.0f; }
#pragma unroll
    for (int h = 0; h < 4; ++h) { e[h].x = __builtin_amdgcn_rcpf(e[h].x); e[h].y = __builtin_amdgcn_rcpf(e[h].y); }
    u32x4 w;
#pragma unroll
    for (int h = 0; h < 4; ++h) { const f32x2 hv = tu[h] * e[h]; w[h] = pk2(hv.x, hv.y); }
    __builtin_amdgcn_raw_buffer_store_b128(w, rh, hb + m * 1024, 0, 16);
  }
}
DI void g1_math(const Params& p, LAS char* sm, int rb, int ft, int wid, int lane, unsigned tag, unsigned taskflag) {
  const int wr = wid >> 1, wc = wid & 1;
  const int n = lane & 15, kg = lane >> 4, q4 = (lane & 15) >> 2, pp = lane & 3;
  f32x4 a1[2][8], a2[2][8], cs[2];
#pragma unroll
  for (int a = 0; a < 2; ++a) {
    cs[a] = (f32x4){0.f, 0.f, 0.f, 0.f};
#pragma unroll
    for (int m = 0; m < 8; ++m) { a1[a][m] = (f32x4){0.f, 0.f, 0.f, 0.f}; a2[a][m] = (f32x4){0.f, 0.f, 0.f, 0.f}; }
  }
  u32x4 ones_ = {0x3f803f80u, 0x3f803f80u, 0x3f803f80u, 0x3f803f80u};
  PIN(ones_);
  const bf16x8 ones = __builtin_bit_cast(bf16x8, ones_);
  const unsigned tbase = (wr * 128 + n) * 128 + ((kg ^ ((n >> 1) & 7)) * 16);
  const int sw = 2 * (((q4 >> 1) & 1) | ((kg & 1) << 1));
  const unsigned wb0 = 32768 + (8 * kg + q4) * 128 + (((4 * wc + 0 + (pp >> 1)) ^ sw) * 16) + 8 * (pp & 1);
  const unsigned wb1 = 32768 + (8 * kg + q4) * 128 + (((4 * wc + 2 + (pp >> 1)) ^ sw) * 16) + 8 * (pp & 1);
  const unsigned o1 = wr * 8192, o2 = 8192 - o1;
  bf16x8 tP[8], tQ[8], gP[2], gQ[2], uP[2], uQ[2];
#define G1_RD(T, G, U, so, ks) do { LAS char* tp_ = sm + (so) + (tbase ^ ((ks) * 64)); \
    LAS char* w10_ = sm + (so) + wb0 + o1 + (ks) * 4096; LAS char* w11_ = sm + (so) + wb1 + o1 + (ks) * 4096; \
    LAS char* w20_ = sm + (so) + wb0 + o2 + (ks) * 4096; LAS char* w21_ = sm + (so) + wb1 + o2 + (ks) * 4096; \
    G[0] = ldtr(w10_, w10_ + 512); T[0] = *(LAS bf16x8*)(tp_); U[0] = ldtr(w20_, w20_ + 512); G[1] = ldtr(w11_, w11_ + 512); U[1] = ldtr(w21_, w21_ + 512); \
    _Pragma("unroll") for (int m = 1; m < 8; ++m) T[m] = *(LAS bf16x8*)(tp_ + 2048 * m); } while (0)
#define G1_MM(T, G, U) do { _Pragma("unroll") for (int m = 0; m < 8; ++m) { _Pragma("unroll") for (int a = 0; a < 2; ++a) { \
    a1[a][m] = __builtin_amdgcn_mfma_f32_16x16x32_bf16(G[a], T[m], a1[a][m], 0, 0, 0); a2[a][m] = __builtin_amdgcn_mfma_f32_16x16x32_bf16(U[a], T[m], a2[a][m], 0, 0, 0); } } } while (0)
#define G1_CS(G) do { cs[0] = __builtin_amdgcn_mfma_f32_16x16x32_bf16(G[0], ones, cs[0], 0, 0, 0); cs[1] = __builtin_amdgcn_mfma_f32_16x16x32_bf16(G[1], ones, cs[1], 0, 0, 0); } while (0)
#define G1_SCHED() do { _Pragma("unroll") for (int i_ = 0; i_ < 16; ++i_) { __builtin_amdgcn_sched_group_barrier(0x8, 1, 0); __builtin_amdgcn_sched_group_barrier(0x100, 1, 0); } __builtin_amdgcn_sched_group_barrier(0x8, 18, 0); } while (0)
  unsigned so = 0;
  BAR();
  STAMP_AT(1); STAMP_CY(14);
  G1_RD(tP, gP, uP, so, 0);
  G1_RD(tQ, gQ, uQ, so, 1); G1_MM(tP, gP, uP); G1_CS(gP); G1_SCHED();
#pragma unroll 1
  for (int s = 1; s < 10; ++s) {
    so = so == 2 * G1_SLOT ? 0 : so + G1_SLOT;
    BAR();
    if (s == 2 && taskflag) { WAITV(0); flag_set(p.ws, taskflag, tag, lane); }
    G1_RD(tP, gP, uP, so, 0); G1_MM(tQ, gQ, uQ); G1_CS(gQ); G1_SCHED();
    G1_RD(tQ, gQ, uQ, so, 1); G1_MM(tP, gP, uP); G1_CS(gP); G1_SCHED();
  }
  G1_CS(gQ);
  if (n == 0) { *(LAS f32x4*)(sm + L_CS + (wr * 64 + wc * 32 + 4 * kg) * 4) = cs[0]; *(LAS f32x4*)(sm + L_CS + (wr * 64 + wc * 32 + 16 + 4 * kg) * 4) = cs[1]; }
  BAR();
  f32x4 csg[2], csu[2];
#pragma unroll
  for (int a = 0; a < 2; ++a) { csg[a] = *(LAS f32x4*)(sm + L_CS + (wc * 32 + 16 * a + 4 * kg) * 4); csu[a] = *(LAS f32x4*)(sm + L_CS + (64 + wc * 32 + 16 * a + 4 * kg) * 4); }
  f32x2 st[8];
#pragma unroll
  for (int m = 0; m < 8; ++m) st[m] = *(LAS f32x2*)(sm + L_ST + (wr * 128 + 16 * m + n) * 8);
  G1_MM(tQ, gQ, uQ);
  STAMP_AT(2); STAMP_CY(15);
  if (wr == 0) g1_epilogue<false>(p, sm, rb, ft, wr, wc, lane, a1, a2, csg, csu, st);
  else g1_epilogue<true>(p, sm, rb, ft, wr, wc, lane, a1, a2, csg, csu, st);
  STAMP_AT(3);
}

template <int NPRE> constexpr int g2_w_after(int j) { return (j >= 1 && j + 3 <= 13 && j + 3 >= NPRE) ? 5 : 0; }
template <int NPRE> constexpr int g2_ops_after(int j) { return (j == 5 ? 1 : 0) + (j == 7 ? 3 : 0) + ((j >= 1 && j + 2 <= 13) ? 4 : 0) + g2_w_after<NPRE>(j); }
template <int NPRE> constexpr int g2_wait(int j) {
  return j == 0 ? 4 : (j == 1 ? g2_ops_after<NPRE>(1) : g2_w_after<NPRE>(j - 1) + g2_ops_after<NPRE>(j));
}
template <bool ISP>
DI void g2_loader(const Params& p, LAS char* sm, unsigned smb, int mt, int nt, int w, int lane, unsigned tag, unsigned hmflag) {
  const i32x4 srdD = make_srd(p.wd, F_ * D_ * 2), srdH = make_srd(p.ws + WS_HM, 54 * 128 * 1024), srdC = make_srd(p.ws, WS_KVT);
  unsigned vd[5]; g2_wd_offsets(vd, lane);
  const unsigned vh = lane * 16;
  auto fbof = [&](int j) { const int fb = 4 * j + w; return fb < 54 ? fb : 53; };
  auto issueH = [&](int j) {
    const unsigned sb = mt * 4096u + (j < 13 ? w : (w < 2 ? w : 1)) * 131072u, lb = smb + L_HR + w * 4096;
#pragma unroll
    for (int i = 0; i < 4; ++i) dma16i_sc1(srdH, vh, sb, j * 524288 + i * 1024, lb, (j % 3) * 16384 + i * 1024);
  };
  constexpr int XB = ISP ? 1 : 5, NREG = ISP ? 9 : 7, NPRE = XB + NREG;
  u32x4 wreg[NREG][5];
  if (ISP) g2_issue_w(srdD, vd, smb, nt, w, 0);
  else BAR();
#pragma unroll
  for (int j = 0; j < NREG; ++j) {
#pragma unroll
    for (int i = 0; i < 5; ++i) ld16i<NT_WD>(wreg[j][i], srdD, vd[i], nt * 160u + w * 40960u, (j + XB) * 163840);
    if (!ISP && j == 0) {
      WAITV(5);
      flag_set(p.ws, hmflag, tag, lane);
      STAMP_AT(4);
    }
  }
  {
    const unsigned junk = smb + L_END - 256;
#pragma unroll
    for (int r = 0; r < (14 - NPRE + 3) / 4; ++r) {
      const int idx = r * 64 + lane;
      int j = NPRE + (idx >> 4); j = j < 14 ? j : 13;
      touch4(srdD, (unsigned)(fbof(j) * 32 + (mt & 3) * 8 + ((idx & 15) >> 1)) * 1280u + nt * 160u + (idx & 1) * 128u, 0, junk);
    }
  }
  if (ISP && w == 0) {
    poll_all(p.ws, CW_ST + (mt * 16 + (lane & 15)) * 16, tag);
    u32x2 sv; ld8_sc1(sv, srdC, WS_STATS + (mt * 64 + lane) * 8, 0);
    WAITV(0); PIN(sv);
    *(LAS u32x2*)(sm + L_S2 + lane * 8) = sv;
  }
  if (ISP && blockIdx.x == 255 && w == 3) {
    poll_all(p.ws, CW_ARRIVE + lane * 4, tag); poll_all(p.ws, CW_ARRIVE + 256 + lane * 4, tag);
    poll_all(p.ws, CW_ARRIVE + 512 + lane * 4, tag); poll_all(p.ws, CW_ARRIVE + 768 + lane * 4, tag);
    flag_set(p.ws, CW_EPOCH, tag, lane);
  }
  { const int j = lane < 14 ? lane : 0; poll_all(p.ws, CW_HM + (((mt >> 2) * 2 + ((mt >> 1) & 1)) * 54 + fbof(j)) * 16, tag); }
  STAMP_AT(5);
  { const int j = (lane >> 2) < 14 ? (lane >> 2) : 13;
    touch4_sc1(srdH, (unsigned)(fbof(j) * 128 + mt * 4 + (lane & 3)) * 1024u + nt * 128u, 0, smb + L_END - 256); }
  issueH(0); issueH(1); issueH(2);
  WAITV(8);
#pragma unroll
  for (int j = 0; j < NREG; ++j)
#pragma unroll
    for (int i = 0; i < 5; ++i) PIN(wreg[j][i]);
  BAR();
  STAMP_AT(6);
  unsigned zf = 0; u32x4 zr[3];
  const unsigned zoff = CW_Z + (nt * 80 + w * 20 + (lane < 20 ? lane : 0)) * 16;
  auto put_w = [&](int j) {
#pragma unroll
    for (int i = 0; i < 5; ++i) *(LAS u32x4*)(sm + L_WR + (j & 3) * G2_WSLOT + w * 5120 + i * 1024 + lane * 16) = wreg[j - XB][i];
  };
#pragma unroll
  for (int j = 0; j < 14; ++j) {
    if (j == 0) {
#pragma unroll
      for (int jj = XB; jj < 4; ++jj) put_w(jj);
    } else if (j + 3 >= XB && j + 3 < NPRE) put_w(j + 3);
    if (j == 5) ld4_sc1(zf, srdC, zoff);
    if (j == 7) {
      PIN(zf);
      if (!__all(zf == tag)) poll_all(p.ws, zoff, tag);
      const i32x4 srdZ = make_srd(p.ws + WS_ZT, D_ * S_ * 2);
#pragma unroll
      for (int r = 0; r < 3; ++r) {
        int task = r * 64 + lane; task = task < 160 ? task : 159;
        ld16_sc1(zr[r], srdZ, (unsigned)(nt * 80 + w * 20 + (task >> 3)) * 4096u + (mt * 64 + (task & 7) * 8) * 2, 0);
      }
    }
    if (j >= 1 && j + 2 <= 13) issueH(j + 2);
    if (g2_w_after<NPRE>(j)) g2_issue_w(srdD, vd, smb, nt, w, j + 3);
    if (j == 9) {
#pragma unroll
      for (int r = 0; r < 3; ++r) PIN(zr[r]);
#pragma unroll
      for (int r = 0; r < 3; ++r) { const int task = r * 64 + lane; if (task < 160) *(LAS u32x4*)(sm + L_ZS + (w * 20 + (task >> 3)) * ZS_RS + (task & 7) * 16) = zr[r]; }
    }
    if (j < 13) WAITV(g2_wait<NPRE>(j));
    BAR();
  }
}

template <int NY>
DI void g2_math(const Params& p, LAS char* sm, int w, int lane, unsigned tag, int zc, const u32x4 (&wy)[3][5]) {
  f32x4 acc[5][4];
#pragma unroll
  for (int a = 0; a < 5; ++a)
#pragma unroll
    for (int b = 0; b < 4; ++b) acc[a][b] = (f32x4){0.f, 0.f, 0.f, 0.f};
  const int kg = lane >> 4, q4 = (lane & 15) >> 2, pp = lane & 3;
  LAS char* hrd = sm + L_HR + w * 4096 + lane * 16;
  LAS char* wrd = sm + L_WR + w * 5120 + (4 * kg + q4) * 160 + pp * 8;
  bf16x8 hP[4], wP[5], hQ[4], wQ[5];
#define G2_RD(H, W, j) do { LAS char* hp_ = hrd + ((j) % 3) * 16384; LAS char* wp_ = wrd + ((j) & 3) * G2_WSLOT; \
    W[0] = ldtr(wp_, wp_ + 2560); H[0] = *(LAS bf16x8*)(hp_); H[1] = *(LAS bf16x8*)(hp_ + 1024); H[2] = *(LAS bf16x8*)(hp_ + 2048); H[3] = *(LAS bf16x8*)(hp_ + 3072); \
    _Pragma("unroll") for (int a = 1; a < 5; ++a) W[a] = ldtr(wp_ + a * 32, wp_ + a * 32 + 2560); } while (0)
#define G2_MM(H, W) do { _Pragma("unroll") for (int a = 0; a < 5; ++a) _Pragma("unroll") for (int b = 0; b < 4; ++b) \
    acc[a][b] = __builtin_amdgcn_mfma_f32_16x16x32_bf16(W[a], H[b], acc[a][b], 0, 0, 0); } while (0)
#define G2_SCHED() do { _Pragma("unroll") for (int i_ = 0; i_ < 14; ++i_) { __builtin_amdgcn_sched_group_barrier(0x8, 1, 0); __builtin_amdgcn_sched_group_barrier(0x100, 1, 0); } __builtin_amdgcn_sched_group_barrier(0x8, 6, 0); } while (0)
  if (zc >= 0) WAITV(4); else WAITV(0);
  BAR();
  STAMP_AT(9);
  G2_RD(hP, wP, 0);
  auto put_y = [&](int j) {
#pragma unroll
    for (int i = 0; i < 5; ++i) *(LAS u32x4*)(sm + L_WR + (j & 3) * G2_WSLOT + w * 5120 + i * 1024 + lane * 16) = wy[j - 2][i];
  };
  if (NY) { put_y(2); put_y(3); }
  if (zc >= 0) {
    WAITV(0);
    flag_set(p.ws, CW_Z + zc * 16, tag, lane);
  }
  BAR();
  G2_RD(hQ, wQ, 1); G2_MM(hP, wP); G2_SCHED();
  if (NY) put_y(4);
#pragma unroll
  for (int j = 2; j < 14; j += 2) {
    BAR();
    G2_RD(hP, wP, j); G2_MM(hQ, wQ); G2_SCHED();
    BAR();
    G2_RD(hQ, wQ, j + 1); G2_MM(hP, wP); G2_SCHED();
  }
  BAR();
  if (w < 2) G2_MM(hQ, wQ);
  STAMP_AT(10);
#pragma unroll
  for (int a = 0; a < 5; ++a)
#pragma unroll
    for (int b = 0; b < 4; ++b) *(LAS f32x4*)(sm + w * G2_PART + (16 * b + (lane & 15)) * G2_RS + a * 64 + kg * 16) = acc[a][b];
}
DI void g2_final_task(const Params& p, LAS char* sm, int mt, int nt, int gt, int lane, const u32x4& x) {
  const int b = gt / 10, ch = gt - b * 10, tk = b * 16 + (lane & 15);
  f32x4 a0 = (f32x4){0.f, 0.f, 0.f, 0.f}, a1 = a0;
#pragma unroll
  for (int w = 0; w < 4; ++w) { a0 += *(LAS f32x4*)(sm + w * G2_PART + tk * G2_RS + ch * 32); a1 += *(LAS f32x4*)(sm + w * G2_PART + tk * G2_RS + ch * 32 + 16); }
  LAS char* zp = sm + L_ZS + (ch * 8 + ((lane & 15) >> 2)) * ZS_RS + b * 32 + (lane & 3) * 8;
  const u32x2 z0 = __builtin_bit_cast(u32x2, __builtin_amdgcn_ds_read_tr16_b64_v4i16((LAS s16x4*)zp));
  const u32x2 z1 = __builtin_bit_cast(u32x2, __builtin_amdgcn_ds_read_tr16_b64_v4i16((LAS s16x4*)(zp + 4 * ZS_RS)));
  const f32x2 st = *(LAS f32x2*)(sm + L_S2 + tk * 8);
  const float rs = st.y, nmr = -st.x * rs;
  float o[8];
#pragma unroll
  for (int e = 0; e < 8; ++e) {
    const float xv = (e & 1) ? bhi(x[e >> 1]) : blo(x[e >> 1]);
    const unsigned zw = e < 4 ? z0[(e & 3) >> 1] : z1[(e & 3) >> 1];
    const float z = (e & 1) ? bhi(zw) : blo(zw);
    const float xn = fmaf(xv, rs, nmr);
    o[e] = fmaf(xn, z, xv) + (e < 4 ? a0[e] : a1[e - 4]);
  }
  const u32x4 r = {pk2(o[0], o[1]), pk2(o[2], o[3]), pk2(o[4], o[5]), pk2(o[6], o[7])};
  *(GAS u32x4*)(p.out + (mt * 64 + tk) * 640 + nt * 80 + ch * 8) = r;
}
DI u32x4 final_x(const Params& p, int mt, int nt, int gt, int lane) {
  const int b = gt / 10, ch = gt - b * 10;
  return *(const GAS u32x4*)(p.q + (mt * 64 + b * 16 + (lane & 15)) * 640 + nt * 80 + ch * 8);
}

DI void pair_send(LAS char* pb, LAS unsigned* pf, unsigned tag, int lane, const f32x4 (&acc)[8]) {
#pragma unroll
  for (int g = 0; g < 8; ++g) *(LAS f32x4*)(pb + g * 1024 + lane * 16) = acc[g];
  asm volatile("s_waitcnt lgkmcnt(0)" ::: "memory");
  if (lane == 0) *(volatile LAS unsigned*)pf = tag;
}
DI void pair_recv(LAS char* pb, LAS unsigned* pf, unsigned tag, int lane, f32x4 (&acc)[8]) {
#pragma unroll 1
  for (unsigned it = 0; it < (1u << 22); ++it) { if (*(volatile LAS unsigned*)pf == tag) break; __builtin_amdgcn_s_sleep(1); }
  asm volatile("" ::: "memory");
#pragma unroll
  for (int g = 0; g < 8; ++g) acc[g] += *(LAS f32x4*)(pb + g * 1024 + lane * 16);
}

__global__ __launch_bounds__(512, 2) void fused_kernel(Params p_arg) {
  unsigned long long pc0; asm volatile("s_getpc_b64 %0" : "=s"(pc0));
  const KP kp = (KP)__builtin_amdgcn_kernarg_segment_ptr();
  Params p = reload(kp);
  extern __shared__ __attribute__((aligned(16))) char smem_raw[];
  LAS char* sm = (LAS char*)smem_raw;
  const unsigned smb = (unsigned)(size_t)sm;
  const int lane = threadIdx.x & 63, wid = __builtin_amdgcn_readfirstlane(threadIdx.x >> 6);
  const int b = blockIdx.x, x = b & 7, i = b >> 3;
  const int mt = x * 4 + (i & 3), nt = i >> 2;
  STAMP_INIT();
  STAMP_AT(0);
  if (i < 27) {
    const int mm = 27 * (x & 3) + i, ft = mm >> 2, rb = 4 * (x >> 2) + (mm & 3);
    if (wid < 4) {
      const unsigned ep = __hip_atomic_load((gu32*)(p.ws + CW_EPOCH), __ATOMIC_RELAXED, __HIP_MEMORY_SCOPE_AGENT);
      unsigned taskflag = 0;
      const int id = wid < 2 ? 2 * b + wid : 432 + b;
      if (wid < 3 && id < 512) { task_stats<true>(p, id, lane); taskflag = CW_ST + id * 16; }
      else BAR();
      const unsigned tag = __builtin_amdgcn_readfirstlane(ep) + 1u;
      asm volatile("" :: "s"(tag) : "memory");
      STAMP_AT(13);
      g1_math(p, sm, rb, ft, wid, lane, tag, taskflag);
      p = reload(kp);
      g2_loader<false>(p, sm, smb, mt, nt, wid, lane, tag, CW_HM + ((rb * 2 + (wid >> 1)) * 54 + ft * 2 + (wid & 1)) * 16);
    } else {
      const int hw = wid - 4;
      const bool conv = 2 * b < NCH_G;
      const int cch = 2 * b + (hw >> 1);
      unsigned tag, dvb = 0; int zc = -1;
      if (conv) {
        u32x4 wy[3][5];
        g1_loader<true>(kp, p, pc0, sm, smb, rb, ft, mt, nt, hw, lane, tag, cch, dvb, wy);
        p = reload(kp);
        BAR();
        STAMP_AT(7);
        LAS char* cb = sm + L_CVG + (hw >> 1) * CV_SZ;
        LAS char* pb = sm + L_CP + (hw >> 1) * 8192;
        LAS unsigned* pf = (LAS unsigned*)(sm + L_PF + (hw >> 1) * 4);
        f32x4 acc[8];
        conv_mfma<4>(cb, (hw & 1) * 4, lane, acc);
        asm volatile("" :: "v"(acc[0]), "v"(acc[7]));
        STAMP_AT(4);
        if (hw & 1) pair_send(pb, pf, tag, lane, acc);
        else { pair_recv(pb, pf, tag, lane, acc); conv_store(p, cb, cch, lane, dvb, acc); zc = cch; }
        STAMP_AT(8);
        g2_math<3>(p, sm, hw, lane, tag, zc, wy);
      } else {
        u32x4 wy[3][5];
        g1_loader<false>(kp, p, pc0, sm, smb, rb, ft, mt, nt, hw, lane, tag, cch, dvb, wy);
        p = reload(kp);
        BAR();
        g2_math<3>(p, sm, hw, lane, tag, zc, wy);
      }
    }
  } else {
    const int cg = i - 27, pb = x * 5 + cg;
    const unsigned ep = __hip_atomic_load((gu32*)(p.ws + CW_EPOCH), __ATOMIC_RELAXED, __HIP_MEMORY_SCOPE_AGENT);
    const int hw = wid - 4, c0 = NCH_G + pb * NCH_P;
    if (wid < 4) {
      task_kv<0>(p, 2 * cg + (wid & 1), 4 * x + (wid >> 1), lane, [] {});
      const unsigned tag = __builtin_amdgcn_readfirstlane(ep) + 1u;
      asm volatile("" :: "s"(tag) : "memory");
      WAITV(0);
      flag_set(p.ws, CW_KV + ((2 * cg + (wid & 1)) * 32 + 4 * x + (wid >> 1)) * 16, tag, lane);
      BAR();
      if (wid == 0) flag_set(p.ws, CW_ARRIVE + b * 4, tag, lane);
      STAMP_AT(1);
      BAR();
      p = reload(kp);
      touchg(p.wd, (unsigned)((pb * 4 + wid) * 64 + lane) * 128u, smb + L_END - 256);
      if (pb * 4 + wid + 160 < 270) touchg(p.wd, (unsigned)((pb * 4 + wid + 160) * 64 + lane) * 128u, smb + L_END - 256);
      g2_loader<true>(p, sm, smb, mt, nt, wid, lane, tag, 0);
    } else {
      LAS char* cb = sm + L_CVP + hw * CV_SZ;
      const int cA = c0 + hw, cB = c0 + 4 + (hw >> 1);
      ConvIn in, in2;
      task_kv<13>(p, 2 * cg + (wid & 1), 4 * x + (wid >> 1), lane, [&] { conv_load_h(p, in, cA, lane); });
      conv_load_h(p, in2, cB, lane);
      const unsigned tag = __builtin_amdgcn_readfirstlane(ep) + 1u;
      asm volatile("" :: "s"(tag) : "memory");
      if ((hw & 1) == 0 && lane == 0) *(LAS unsigned*)(sm + L_PFP + (hw >> 1) * 4) = ~tag;
      WAITV(13);
      flag_set(p.ws, CW_KV + ((2 * cg + (wid & 1)) * 32 + 4 * x + (wid >> 1)) * 16, tag, lane);
      BAR();
      STAMP_AT(1);
      poll_all(p.ws, CW_KV + (((lane < 32 ? cA : cB) >> 6) * 32 + (lane & 31)) * 16, tag);
      conv_load_u(p, in, cA, lane);
      conv_load_u(p, in2, cB, lane);
      BAR();
      WAITV(4);
      conv_pin(in);
      conv_build(in, cb, lane);
      unsigned dvb = in.dv;
      STAMP_AT(2); STAMP_CY(14);
      f32x4 acc[8];
      conv_mfma<8>(cb, 0, lane, acc);
      asm volatile("" :: "v"(acc[0]), "v"(acc[7]));
      STAMP_AT(4); STAMP_CY(15);
      conv_store(p, cb, cA, lane, dvb, acc);
      STAMP_AT(3);
      WAITV(4);
      conv_pin(in2);
      conv_build(in2, cb, lane);
      dvb = in2.dv;
      STAMP_AT(7);
      conv_mfma<4>(cb, (hw & 1) * 4, lane, acc);
      WAITV(0);
      flag_set(p.ws, CW_Z + cA * 16, tag, lane);
      LAS unsigned* pf = (LAS unsigned*)(sm + L_PFP + (hw >> 1) * 4);
      int zc = -1;
      if (hw & 1) pair_send(cb, pf, tag, lane, acc);
      else { pair_recv(cb + CV_SZ, pf, tag, lane, acc); conv_store(p, cb, cB, lane, dvb, acc); zc = cB; }
      STAMP_AT(8);
      u32x4 wy[3][5];
      g2_math<0>(p, sm, hw, lane, tag, zc, wy);
    }
  }
  p = reload(kp);
  const int gt0 = wid * 4 + (lane >> 4), gt1 = 32 + gt0;
  const u32x4 x0 = final_x(p, mt, nt, gt0, lane);
  u32x4 x1 = x0;
  if (wid < 2) x1 = final_x(p, mt, nt, gt1, lane);
  BAR();
  g2_final_task(p, sm, mt, nt, gt0, lane, x0);
  if (wid < 2) g2_final_task(p, sm, mt, nt, gt1, lane, x1);
  STAMP_AT(11);
  if (blockIdx.x == DIAG_B && wid == DIAG_W) {
    const u32x4 a_ = *(LAS u32x4*)((LAS char*)smem_dbg + L_DBG + (DIAG_W * 16 + 0) * 16), b_ = *(LAS u32x4*)((LAS char*)smem_dbg + L_DBG + (DIAG_W * 16 + DIAG_I) * 16);
    const unsigned d_ = __builtin_amdgcn_readfirstlane(b_.x - a_.x) * 4u;
    const unsigned long long t0_ = __builtin_amdgcn_s_memrealtime();
    for (unsigned it = 0; it < (1u << 20); ++it) { if (__builtin_amdgcn_s_memrealtime() - t0_ >= d_ || d_ > 100000u) break; __builtin_amdgcn_s_sleep(1); }
  }
}

extern "C" void kernel_launch(void* const* d_in, const int* in_sizes, int n_in, void* d_out, int out_size, void* d_ws, size_t ws_size, hipStream_t stream) {
  Params p;
  p.q = (const u16*)d_in[0]; p.k = (const u16*)d_in[1]; p.v = (const u16*)d_in[2]; p.sk = (const u16*)d_in[3]; p.h = (const u16*)d_in[4]; p.dv = (const u16*)d_in[5];
  p.wg = (const u16*)d_in[7]; p.wu = (const u16*)d_in[8]; p.wd = (const u16*)d_in[9]; p.out = (u16*)d_out; p.ws = (char*)d_ws;
  (void)hipFuncSetAttribute((const void*)fused_kernel, hipFuncAttributeMaxDynamicSharedMemorySize, L_ALLOC);
  fused_kernel<<<256, 512, L_ALLOC, stream>>>(p);
}
#define NCLS 4
static inline int stamp_class(int b, int w) { return ((b >> 3) < 27 ? 0 : 2) + (w < 4 ? 0 : 1); }
static inline const char* cls_name(int c) { return c == 0 ? "GX" : c == 1 ? "GY" : c == 2 ? "PX" : "PY"; }
#define HAVE_INTER 1
#define HM_K16 1
```

```cpp
#include <hip/hip_runtime.h>
#include <stdint.h>
typedef unsigned short u16;
typedef __bf16 bf16x8 __attribute__((ext_vector_type(8)));
typedef __bf16 bf16x2 __attribute__((ext_vector_type(2)));
typedef short s16x4 __attribute__((ext_vector_type(4)));
typedef float f32x2 __attribute__((ext_vector_type(2)));
typedef float f32x4 __attribute__((ext_vector_type(4)));
typedef unsigned u32x2 __attribute__((ext_vector_type(2)));
typedef unsigned u32x4 __attribute__((ext_vector_type(4)));
typedef int i32x4 __attribute__((ext_vector_type(4)));
#define LAS __attribute__((address_space(3)))
#define GAS __attribute__((address_space(1)))
#define DI __device__ __forceinline__
typedef GAS unsigned gu32;

#define TOUCH 1
#define TOUCH_CODE 1
#define CODE_PIECES 7
#define NT_G1 0
#define NT_KV 1
#define NT_H 1
#define NT_WD 0
#define KVT 1
#define OUT_POL 0
#define NREG_G 6
#define NREG_P 5
#define PACE 1
#define PRIO_Y 0
#define PRIO_X 2
#define LATE_CONV 0
#define MALLW 0
#define WS_TAIL 0
#define WS_BASE(n) (WS_TAIL ? ((n) - (size_t)(WS_TAIL << 20)) & ~(size_t)0x1fffff : (size_t)0)
constexpr int S_ = 2048, D_ = 640, F_ = 1728;
constexpr unsigned CW_EPOCH = 0;
constexpr unsigned CW_ARRIVE = 1024;
constexpr unsigned CW_KV = 4096;
constexpr unsigned CW_ST = 16384;
constexpr unsigned CW_HM = 24576;
constexpr unsigned CW_Z = 40960;
constexpr unsigned WS_STATS = 65536;
constexpr unsigned WS_KVT = 131072;
constexpr unsigned WS_ZT = WS_KVT + D_ * S_ * 2;
constexpr unsigned WS_HM = WS_ZT + D_ * S_ * 2;
constexpr unsigned WS_END = WS_HM + 54 * 128 * 1024;
constexpr unsigned WS_DBG = 32u << 20;
constexpr int G1_SLOT = 49152;
constexpr int L_CS = 153856, L_ST = L_CS + 512, L_S2 = L_ST + 2048, L_END = L_S2 + 512 + 64 + 256;
constexpr int L_ALLOC = L_END;
constexpr int G2_WSLOT = 20480;
template <bool ISP> constexpr int g2_slot(int k) { return ISP ? (k < 3 ? 69632 + k * G2_WSLOT : (k - 3) * G2_WSLOT) : (k < 2 ? 49152 + k * G2_WSLOT : (k < 4 ? (k - 2) * G2_WSLOT : 90112 + (k - 4) * G2_WSLOT)); }
constexpr int L_CVG = 90112, L_CVP = 69632;
constexpr int L_ZS = 131072, ZS_RS = 144;
constexpr int L_CP = L_CVG + 2 * 21216, L_PF = L_CS - 64;
constexpr int L_CPP = 131072;
constexpr int L_PFP = L_S2 + 512 - 0;
constexpr int L_EX = 0, L_BS = L_ZS + 80 * ZS_RS;
constexpr int CV_H = 4160, CV_U = 4 * CV_H, CV_SZ = CV_U + 4576;
static_assert(L_BS + 4 * 5 * 512 <= L_PF, "lds");
static_assert(L_CP + 2 * 8192 <= L_PF, "lds");
static_assert(L_CVP + 4 * CV_SZ + 64 <= L_S2, "lds");
constexpr int NCH_P = 6;
constexpr int NCH_G = D_ - 40 * NCH_P;
static_assert(NCH_G <= 2 * 216 && NCH_G % 2 == 0, "conv split");
constexpr unsigned SPIN_MAX = 1u << 16;

struct Params {
  const u16 *q, *k, *v, *sk, *h, *dv, *wg, *wu, *wd;
  u16* out; char* ws;
};

typedef const __attribute__((address_space(4))) Params* KP;
DI Params reload(KP kp) {
  asm volatile("" : "+s"(kp));
  Params r; r.q = kp->q; r.k = kp->k; r.v = kp->v; r.sk = kp->sk; r.h = kp->h; r.dv = kp->dv; r.wg = kp->wg; r.wu = kp->wu; r.wd = kp->wd; r.out = kp->out; r.ws = kp->ws;
  return r;
}
DI i32x4 make_srd(const void* p, unsigned bytes) {
  i32x4 r; unsigned long long a = (unsigned long long)p;
  r.x = (int)(unsigned)a;
  r.y = (int)((unsigned)(a >> 32) & 0xffffu);
  r.z = (int)bytes; r.w = 0x00020000; return r;
}
DI void dma16(const i32x4& srd, unsigned voff, unsigned soff, unsigned lds) {
  asm volatile("s_mov_b32 m0, %3\n\ts_nop 0\n\tbuffer_load_dwordx4 %0, %1, %2 offen lds" :: "v"(voff), "s"(srd), "s"(soff), "s"(lds) : "memory");
}
DI void dma16_sc1(const i32x4& srd, unsigned voff, unsigned soff, unsigned lds) {
  asm volatile("s_mov_b32 m0, %3\n\ts_nop 0\n\tbuffer_load_dwordx4 %0, %1, %2 offen sc1 lds" :: "v"(voff), "s"(srd), "s"(soff), "s"(lds) : "memory");
}
template <int NT = 0>
DI void dma16i(const i32x4& srd, unsigned voff, unsigned sbase, int simm, unsigned lbase, int limm) {
  unsigned t;
  if (NT) asm volatile("s_add_u32 m0, %4, %6\n\ts_add_u32 %0, %3, %5\n\tbuffer_load_dwordx4 %1, %2, %0 offen nt lds" : "=&s"(t) : "v"(voff), "s"(srd), "s"(sbase), "s"(lbase), "i"(simm), "i"(limm) : "memory", "scc");
  else asm volatile("s_add_u32 m0, %4, %6\n\ts_add_u32 %0, %3, %5\n\tbuffer_load_dwordx4 %1, %2, %0 offen lds" : "=&s"(t) : "v"(voff), "s"(srd), "s"(sbase), "s"(lbase), "i"(simm), "i"(limm) : "memory", "scc");
}
DI void dma16i_sc1(const i32x4& srd, unsigned voff, unsigned sbase, int simm, unsigned lbase, int limm) {
  unsigned t;
  asm volatile("s_add_u32 m0, %4, %6\n\ts_add_u32 %0, %3, %5\n\tbuffer_load_dwordx4 %1, %2, %0 offen sc1 lds" : "=&s"(t) : "v"(voff), "s"(srd), "s"(sbase), "s"(lbase), "i"(simm), "i"(limm) : "memory", "scc");
}
template <int NT = 0>
DI void ld16i(u32x4& d, const i32x4& srd, unsigned voff, unsigned sbase, int simm) {
  unsigned t;
  if (NT) asm volatile("s_add_u32 %1, %4, %5\n\tbuffer_load_dwordx4 %0, %2, %3, %1 offen nt" : "=v"(d), "=&s"(t) : "v"(voff), "s"(srd), "s"(sbase), "i"(simm) : "memory", "scc");
  else asm volatile("s_add_u32 %1, %4, %5\n\tbuffer_load_dwordx4 %0, %2, %3, %1 offen" : "=v"(d), "=&s"(t) : "v"(voff), "s"(srd), "s"(sbase), "i"(simm) : "memory", "scc");
}
template <int NT = 0>
DI void ld16(u32x4& d, const i32x4& srd, unsigned voff, unsigned soff) {
  if (NT) asm volatile("buffer_load_dwordx4 %0, %1, %2, %3 offen nt" : "=v"(d) : "v"(voff), "s"(srd), "s"(soff) : "memory");
  else asm volatile("buffer_load_dwordx4 %0, %1, %2, %3 offen" : "=v"(d) : "v"(voff), "s"(srd), "s"(soff) : "memory");
}
DI void ld16_sc1(u32x4& d, const i32x4& srd, unsigned voff, unsigned soff) { asm volatile("buffer_load_dwordx4 %0, %1, %2, %3 offen sc1" : "=v"(d) : "v"(voff), "s"(srd), "s"(soff) : "memory"); }
DI void ld8_sc1(u32x2& d, const i32x4& srd, unsigned voff, unsigned soff) { asm volatile("buffer_load_dwordx2 %0, %1, %2, %3 offen sc1" : "=v"(d) : "v"(voff), "s"(srd), "s"(soff) : "memory"); }
DI void ld8(u32x2& d, const i32x4& srd, unsigned voff, unsigned soff) { asm volatile("buffer_load_dwordx2 %0, %1, %2, %3 offen" : "=v"(d) : "v"(voff), "s"(srd), "s"(soff) : "memory"); }
DI void ld8i(u32x2& d, const i32x4& srd, unsigned voff, unsigned sbase, int simm) {
  unsigned t;
  asm volatile("s_add_u32 %1, %4, %5\n\tbuffer_load_dwordx2 %0, %2, %3, %1 offen" : "=v"(d), "=&s"(t) : "v"(voff), "s"(srd), "s"(sbase), "i"(simm) : "memory", "scc");
}
DI void ld4_sc1(unsigned& d, const i32x4& srd, unsigned voff) { asm volatile("buffer_load_dword %0, %1, %2, 0 offen sc1" : "=v"(d) : "v"(voff), "s"(srd) : "memory"); }
DI void touchg(const void* base, unsigned voff, unsigned lds) {
  asm volatile("s_mov_b32 m0, %2\n\ts_nop 0\n\tglobal_load_lds_dword %0, %1" :: "v"(voff), "s"(base), "s"(lds) : "memory");
}
DI void touchg_nt(const void* base, unsigned voff, unsigned lds) {
  asm volatile("s_mov_b32 m0, %2\n\ts_nop 0\n\tglobal_load_lds_dword %0, %1 nt" :: "v"(voff), "s"(base), "s"(lds) : "memory");
}
DI void touch4(const i32x4& srd, unsigned voff, unsigned soff, unsigned lds) {
  asm volatile("s_mov_b32 m0, %3\n\ts_nop 0\n\tbuffer_load_dword %0, %1, %2 offen lds" :: "v"(voff), "s"(srd), "s"(soff), "s"(lds) : "memory");
}
DI void touch4_sc1(const i32x4& srd, unsigned voff, unsigned soff, unsigned lds) {
  asm volatile("s_mov_b32 m0, %3\n\ts_nop 0\n\tbuffer_load_dword %0, %1, %2 offen sc1 lds" :: "v"(voff), "s"(srd), "s"(soff), "s"(lds) : "memory");
}
#define WAITV(n) asm volatile("s_waitcnt vmcnt(%0)" :: "i"(n) : "memory")
#define BAR() asm volatile("s_waitcnt lgkmcnt(0)\n\ts_barrier" ::: "memory")
#define PIN(x) asm volatile("" : "+v"(x))
DI float b2f(u16 x) { return __uint_as_float(((unsigned)x) << 16); }
DI float blo(unsigned x) { return __uint_as_float(x << 16); }
DI float bhi(unsigned x) { return __uint_as_float(x & 0xffff0000u); }
DI unsigned pk2(float lo, float hi) { bf16x2 r = {(__bf16)lo, (__bf16)hi}; return __builtin_bit_cast(unsigned, r); }
DI float rbf(float x) { return (float)(__bf16)x; }
DI float dot2(unsigned a, unsigned b, float c) { return __builtin_amdgcn_fdot2_f32_bf16(__builtin_bit_cast(bf16x2, a), __builtin_bit_cast(bf16x2, b), c, false); }
template <int CTRL> DI float dpp_add(float x) { return x + __int_as_float(__builtin_amdgcn_update_dpp(0, __float_as_int(x), CTRL, 0xf, 0xf, true)); }
DI float row16_sum(float x) { x = dpp_add<0x128>(x); x = dpp_add<0x124>(x); x = dpp_add<0x122>(x); x = dpp_add<0x121>(x); return x; }
DI bf16x8 ldtr(LAS char* p0, LAS char* p1) {
  s16x4 a = __builtin_amdgcn_ds_read_tr16_b64_v4i16((LAS s16x4*)p0), b = __builtin_amdgcn_ds_read_tr16_b64_v4i16((LAS s16x4*)p1);
  return __builtin_bit_cast(bf16x8, __builtin_shufflevector(a, b, 0, 1, 2, 3, 4, 5, 6, 7));
}
DI void poll_all(const char* ws, unsigned off, unsigned tag) {
  gu32* a = (gu32*)(ws + off);
#pragma unroll 1
  for (unsigned it = 0; it < SPIN_MAX; ++it) {
    const unsigned v = __hip_atomic_load(a, __ATOMIC_RELAXED, __HIP_MEMORY_SCOPE_AGENT);
    if (__all(v == tag)) break;
    __builtin_amdgcn_s_sleep(2);
  }
  asm volatile("" ::: "memory");
}
DI void flag_set(const char* ws, unsigned off, unsigned tag, int lane) {
  if (lane == 0) __hip_atomic_store((gu32*)(ws + off), tag, __ATOMIC_RELAXED, __HIP_MEMORY_SCOPE_AGENT);
}
#define STAMP_CY(idx) do {} while (0)
#define STAMP_AT(idx) do {} while (0)
#define STAMP_INIT() do {} while (0)
#define STAMP_FLUSH() do {} while (0)

template <int NX, class Hook>
DI void task_kv(const Params& p, int cg, int tb, int lane, Hook hook) {
  const int c0 = cg * 64 + 8 * (lane & 7), t0 = tb * 64 + 8 * (lane >> 3);
  const i32x4 rk = make_srd(p.k, S_ * D_ * 2), rv = make_srd(p.v, S_ * D_ * 2), rsk = make_srd(p.sk, D_ * 8);
  u32x4 kk[11], vv[8], ss[4];
#pragma unroll
  for (int j = 0; j < 4; ++j) ld16(ss[j], rsk, (unsigned)(c0 * 8), j * 16);
#pragma unroll
  for (int j = 0; j < 11; ++j) { const int t = t0 - 3 + j; ld16<NT_KV>(kk[j], rk, t >= 0 ? (unsigned)(t * 1280 + c0 * 2) : 0xfffffff0u, 0); }
#pragma unroll
  for (int j = 0; j < 8; ++j) ld16<NT_KV>(vv[j], rv, (unsigned)(t0 * 1280 + c0 * 2), j * 1280);
  hook();
  const __amdgpu_buffer_rsrc_t ro = __builtin_amdgcn_make_buffer_rsrc((void*)(p.ws + WS_KVT), 0, D_ * S_ * 2, 0x00020000);
  WAITV(NX);
  STAMP_AT(12);
#pragma unroll
  for (int j = 0; j < 4; ++j) PIN(ss[j]);
#pragma unroll
  for (int j = 0; j < 11; ++j) PIN(kk[j]);
#pragma unroll
  for (int j = 0; j < 8; ++j) PIN(vv[j]);
#pragma unroll
  for (int cp = 0; cp < 4; ++cp) {
    const f32x2 s0 = {blo(ss[cp].x), blo(ss[cp].z)}, s1 = {bhi(ss[cp].x), bhi(ss[cp].z)}, s2 = {blo(ss[cp].y), blo(ss[cp].w)}, s3 = {bhi(ss[cp].y), bhi(ss[cp].w)};
    f32x2 kx[11];
#pragma unroll
    for (int j = 0; j < 11; ++j) { const unsigned w = kk[j][cp]; kx[j] = (f32x2){blo(w), bhi(w)}; }
    f32x2 o[8];
    __builtin_amdgcn_sched_barrier(0);
#pragma unroll
    for (int j = 0; j < 8; ++j) o[j] = kx[j] * s3;
    __builtin_amdgcn_sched_barrier(0);
#pragma unroll
    for (int j = 0; j < 8; ++j) o[j] = kx[j + 1] * s2 + o[j];
    __builtin_amdgcn_sched_barrier(0);
#pragma unroll
    for (int j = 0; j < 8; ++j) o[j] = kx[j + 2] * s1 + o[j];
    __builtin_amdgcn_sched_barrier(0);
#pragma unroll
    for (int j = 0; j < 8; ++j) o[j] = kx[j + 3] * s0 + o[j];
    __builtin_amdgcn_sched_barrier(0);
    f32x2 vx[8];
#pragma unroll
    for (int j = 0; j < 8; ++j) { const unsigned w = vv[j][cp]; vx[j] = (f32x2){blo(w), bhi(w)}; }
    __builtin_amdgcn_sched_barrier(0);
#pragma unroll
    for (int j = 0; j < 8; ++j) o[j] = o[j] * vx[j];
    __builtin_amdgcn_sched_barrier(0);
    const u32x4 r0 = {pk2(o[7].x, o[6].x), pk2(o[5].x, o[4].x), pk2(o[3].x, o[2].x), pk2(o[1].x, o[0].x)};
    const u32x4 r1 = {pk2(o[7].y, o[6].y), pk2(o[5].y, o[4].y), pk2(o[3].y, o[2].y), pk2(o[1].y, o[0].y)};
    __builtin_amdgcn_raw_buffer_store_b128(r0, ro, (unsigned)((c0 + 2 * cp) * 4096 + (2040 - t0) * 2), 0, 16);
    __builtin_amdgcn_raw_buffer_store_b128(r1, ro, (unsigned)((c0 + 2 * cp + 1) * 4096 + (2040 - t0) * 2), 0, 16);
  }
}
template <bool PRE>
DI void task_stats(const Params& p, int id, int lane) {
  const i32x4 rq = make_srd(p.q, S_ * D_ * 2);
  const int row = id * 4 + (lane >> 4), l16 = lane & 15;
  u32x4 x[5];
#pragma unroll
  for (int j = 0; j < 5; ++j) ld16<NT_G1>(x[j], rq, (unsigned)row * 1280u + l16 * 16, j * 256);
  const __amdgpu_buffer_rsrc_t rs = __builtin_amdgcn_make_buffer_rsrc((void*)(p.ws + WS_STATS), 0, S_ * 8, 0x00020000);
  if (PRE) BAR();
  WAITV(0);
  STAMP_AT(12);
#pragma unroll
  for (int j = 0; j < 5; ++j) PIN(x[j]);
  f32x2 xv[20];
#pragma unroll
  for (int j = 0; j < 5; ++j)
#pragma unroll
    for (int e = 0; e < 4; ++e) xv[j * 4 + e] = (f32x2){blo(x[j][e]), bhi(x[j][e])};
  f32x2 s4[4] = {xv[0], xv[1], xv[2], xv[3]};
#pragma unroll
  for (int j = 4; j < 20; ++j) s4[j & 3] += xv[j];
  const f32x2 s2 = (s4[0] + s4[1]) + (s4[2] + s4[3]);
  const float mean = row16_sum(s2.x + s2.y) * (1.0f / 640.0f);
  f32x2 q4[4] = {{0.f, 0.f}, {0.f, 0.f}, {0.f, 0.f}, {0.f, 0.f}};
#pragma unroll
  for (int j = 0; j < 20; ++j) { const f32x2 d = xv[j] - mean; q4[j & 3] = d * d + q4[j & 3]; }
  const f32x2 q2 = (q4[0] + q4[1]) + (q4[2] + q4[3]);
  const float var = row16_sum(q2.x + q2.y) * (1.0f / 640.0f);
  const float mb = rbf(mean), sb = rbf(__builtin_amdgcn_sqrtf(rbf(rbf(var) + 1e-5f)));
  u32x2 o; o.x = __float_as_uint(mb); o.y = __float_as_uint(__builtin_amdgcn_rcpf(sb));
  __builtin_amdgcn_raw_buffer_store_b64(o, rs, l16 == 0 ? (unsigned)row * 8u : 0xfffffff0u, 0, 16);
}

struct ConvIn { u32x4 L0[4], L1[4], L2[4], U[4]; unsigned dv; };
DI void conv_load_h(const Params& p, ConvIn& in, int c, int lane) {
  const i32x4 rh = make_srd(p.h, D_ * S_ * 2), rd = make_srd(p.dv, D_ * 2);
  const unsigned vo = lane * 16, so = c * 4096u;
  asm volatile("buffer_load_ushort %0, %1, %2, 0 offen" : "=v"(in.dv) : "v"(c * 2u), "s"(rd) : "memory");
#pragma unroll
  for (int i = 0; i < 4; ++i) { ld16<NT_H>(in.L0[i], rh, vo, so + i * 1024); ld16(in.L1[i], rh, vo + 4, so + i * 1024); ld16(in.L2[i], rh, vo + 8, so + i * 1024); }
}
DI void conv_load_u(const Params& p, ConvIn& in, int c, int lane) {
  const i32x4 ru = make_srd(p.ws + WS_KVT, D_ * S_ * 2);
#pragma unroll
  for (int i = 0; i < 4; ++i) ld16_sc1(in.U[i], ru, lane * 16, c * 4096u + i * 1024);
}
DI void conv_pin(ConvIn& in) {
#pragma unroll
  for (int i = 0; i < 4; ++i) { PIN(in.L0[i]); PIN(in.L1[i]); PIN(in.L2[i]); PIN(in.U[i]); }
  PIN(in.dv);
}
DI void conv_build(const ConvIn& in, LAS char* cb, int lane) {
#pragma unroll
  for (int i = 0; i < 4; ++i) {
    const unsigned o = (i * 64 + lane + 2) * 16;
    u32x4 c0, c2;
#pragma unroll
    for (int e = 0; e < 4; ++e) { c0[e] = __builtin_amdgcn_alignbit(in.L1[i][e], in.L0[i][e], 16); c2[e] = __builtin_amdgcn_alignbit(in.L2[i][e], in.L1[i][e], 16); }
    *(LAS u32x4*)(cb + 0 * CV_H + o) = c0;
    *(LAS u32x4*)(cb + 1 * CV_H + o) = in.L1[i];
    *(LAS u32x4*)(cb + 2 * CV_H + o) = c2;
    *(LAS u32x4*)(cb + 3 * CV_H + o) = in.L2[i];
    *(LAS u32x4*)(cb + CV_U + (i * 64 + lane) * 16) = in.U[i];
  }
  const u32x4 z4 = {0u, 0u, 0u, 0u};
  if (lane < 30) *(LAS u32x4*)(cb + CV_U + 4096 + lane * 16) = z4;
  if (lane == 0) {
    const unsigned w0 = in.L0[0].x, w1 = in.L0[0].y;
#pragma unroll
    for (int cc = 0; cc < 4; ++cc) *(LAS u32x4*)(cb + cc * CV_H) = z4;
    *(LAS u32x4*)(cb + 0 * CV_H + 16) = (u32x4){0u, 0u, 0u, w0 << 16};
    *(LAS u32x4*)(cb + 1 * CV_H + 16) = (u32x4){0u, 0u, 0u, w0};
    *(LAS u32x4*)(cb + 2 * CV_H + 16) = (u32x4){0u, 0u, w0 << 16, __builtin_amdgcn_alignbit(w1, w0, 16)};
    *(LAS u32x4*)(cb + 3 * CV_H + 16) = (u32x4){0u, 0u, w0, w1};
  }
}
template <int TRIPS>
DI void conv_mfma(LAS char* cb, int d0, int lane, f32x4 (&acc)[8]) {
  const int r = lane & 15, kg = lane >> 4, kap = kg >> 1, bp = kg & 1;
  unsigned a0 = (unsigned)(size_t)cb + (r & 3) * CV_H + 32 * kap + 16 * bp + 2 * (r & 12) + d0 * 64;
  unsigned a1 = a0 + 8;
  unsigned ub = (unsigned)(size_t)cb + CV_U + (4064 - 3584) - 32 * r + 32 * kap + 16 * bp + d0 * 64;
  PIN(a0); PIN(a1); PIN(ub);
  bf16x8 A0[8], B0[8], A1[8], B1[8];
#define CV_RD(A, B, off) do { _Pragma("unroll") for (int g = 0; g < 8; ++g) { \
    B[7 - g] = *(LAS bf16x8*)(size_t)(ub + (off) + g * 512); \
    const s16x4 x0_ = *(volatile LAS s16x4*)(size_t)(a0 + (off) + g * 512), x1_ = *(volatile LAS s16x4*)(size_t)(a1 + (off) + g * 512); \
    A[g] = __builtin_bit_cast(bf16x8, __builtin_shufflevector(x0_, x1_, 0, 1, 2, 3, 4, 5, 6, 7)); } } while (0)
#define CV_MM(A, B) do { _Pragma("unroll") for (int gm = 0; gm < 8; ++gm) _Pragma("unroll") for (int gp = 7 - gm; gp >= 0; --gp) \
    acc[gm + gp] = __builtin_amdgcn_mfma_f32_16x16x32_bf16(A[gm], B[gp], acc[gm + gp], 0, 0, 0); } while (0)
#define CV_SCHED() do { _Pragma("unroll") for (int i_ = 0; i_ < 12; ++i_) { __builtin_amdgcn_sched_group_barrier(0x8, 2, 0); __builtin_amdgcn_sched_group_barrier(0x100, 2, 0); } \
    __builtin_amdgcn_sched_group_barrier(0x8, 12, 0); } while (0)
  CV_RD(A0, B0, 0);
#pragma unroll
  for (int g = 0; g < 8; ++g) acc[g] = (f32x4){0.f, 0.f, 0.f, 0.f};
#pragma unroll 1
  for (int d = 0; d < TRIPS; d += 2) {
    CV_RD(A1, B1, 64); CV_MM(A0, B0); CV_SCHED();
    __builtin_amdgcn_sched_barrier(0);
    a0 += 128; a1 += 128; ub += 128; PIN(a0); PIN(a1); PIN(ub);
    CV_RD(A0, B0, 0); CV_MM(A1, B1); CV_SCHED();
    __builtin_amdgcn_sched_barrier(0);
  }
}
DI void conv_store(const Params& p, LAS char* cb, int c, int lane, unsigned dvb, const f32x4 (&acc)[8]) {
  const int r = lane & 15, kg = lane >> 4;
  const float dv = __uint_as_float(dvb << 16);
  const __amdgpu_buffer_rsrc_t rz = __builtin_amdgcn_make_buffer_rsrc((void*)(p.ws + WS_ZT), 0, D_ * S_ * 2, 0x00020000);
  u32x2 z[8];
#pragma unroll
  for (int g = 0; g < 8; ++g) {
    const int t = 256 * g + 16 * r + 4 * kg;
    const u32x2 kv = *(LAS u32x2*)(cb + CV_U + (2044 - t) * 2);
    const float y0 = fmaf(bhi(kv.y), dv, acc[g][0]), y1 = fmaf(blo(kv.y), dv, acc[g][1]), y2 = fmaf(bhi(kv.x), dv, acc[g][2]), y3 = fmaf(blo(kv.x), dv, acc[g][3]);
    z[g].x = pk2(y0, y1); z[g].y = pk2(y2, y3);
  }
#pragma unroll
  for (int g = 0; g < 8; g += 2) {
    const auto s0 = __builtin_amdgcn_permlane16_swap(z[g].x, z[g + 1].x, false, false);
    const auto s1 = __builtin_amdgcn_permlane16_swap(z[g].y, z[g + 1].y, false, false);
    const u32x4 o = {s0[0], s1[0], s0[1], s1[1]};
    const int gg = g + (kg & 1);
    __builtin_amdgcn_raw_buffer_store_b128(o, rz, c * 4096u + (256 * gg + 16 * r + 4 * (kg & 2)) * 2, 0, 16);
  }
}
struct ConvHalf { u32x4 La[4], Lb[4], U[2]; unsigned dv; };
DI void convh_load_h(const Params& p, ConvHalf& in, int c, int half, int lane, int part) {
  const i32x4 rh = make_srd(p.h, D_ * S_ * 2);
  const unsigned vo = lane * 16 + half * 4, so = c * 4096u;
  if (part == 0) {
    const i32x4 rd = make_srd(p.dv, D_ * 2);
    asm volatile("buffer_load_ushort %0, %1, %2, 0 offen" : "=v"(in.dv) : "v"(c * 2u), "s"(rd) : "memory");
    ld16i<NT_H>(in.La[0], rh, vo, so, 0); ld16i(in.Lb[0], rh, vo + 4, so, 0);
  } else if (part == 1) {
    ld16i<NT_H>(in.La[1], rh, vo, so, 1024); ld16i(in.Lb[1], rh, vo + 4, so, 1024); ld16i<NT_H>(in.La[2], rh, vo, so, 2048);
  } else {
    ld16i(in.Lb[2], rh, vo + 4, so, 2048); ld16i<NT_H>(in.La[3], rh, vo, so, 3072); ld16i(in.Lb[3], rh, vo + 4, so, 3072);
  }
}
DI void convh_load_u(const Params& p, ConvHalf& in, int c, int half, int lane) {
  const i32x4 ru = make_srd(p.ws + WS_KVT, D_ * S_ * 2);
#pragma unroll
  for (int i = 0; i < 2; ++i) ld16_sc1(in.U[i], ru, lane * 16, c * 4096u + (half * 2 + i) * 1024);
}
DI void convh_build(ConvHalf& in, LAS char* cb, int half, int lane) {
#pragma unroll
  for (int i = 0; i < 4; ++i) { PIN(in.La[i]); PIN(in.Lb[i]); }
  PIN(in.U[0]); PIN(in.U[1]); PIN(in.dv);
  LAS char* hb = cb + 2 * half * CV_H;
#pragma unroll
  for (int i = 0; i < 4; ++i) {
    const unsigned o = (i * 64 + lane + 2) * 16;
    u32x4 c0;
#pragma unroll
    for (int e = 0; e < 4; ++e) c0[e] = __builtin_amdgcn_alignbit(in.Lb[i][e], in.La[i][e], 16);
    *(LAS u32x4*)(hb + o) = c0;
    *(LAS u32x4*)(hb + CV_H + o) = in.Lb[i];
  }
#pragma unroll
  for (int i = 0; i < 2; ++i) *(LAS u32x4*)(cb + CV_U + ((half * 2 + i) * 64 + lane) * 16) = in.U[i];
  if (half == 0) {
    const u32x4 z4 = {0u, 0u, 0u, 0u};
    if (lane < 30) *(LAS u32x4*)(cb + CV_U + 4096 + lane * 16) = z4;
    if (lane == 0) {
      const unsigned w0 = in.La[0].x, w1 = in.La[0].y;
#pragma unroll
      for (int cc = 0; cc < 4; ++cc) *(LAS u32x4*)(cb + cc * CV_H) = z4;
      *(LAS u32x4*)(cb + 0 * CV_H + 16) = (u32x4){0u, 0u, 0u, w0 << 16};
      *(LAS u32x4*)(cb + 1 * CV_H + 16) = (u32x4){0u, 0u, 0u, w0};
      *(LAS u32x4*)(cb + 2 * CV_H + 16) = (u32x4){0u, 0u, w0 << 16, __builtin_amdgcn_alignbit(w1, w0, 16)};
      *(LAS u32x4*)(cb + 3 * CV_H + 16) = (u32x4){0u, 0u, w0, w1};
    }
  }
}

template <bool ISP>
DI void g2_issue_w(const i32x4& srdD, const unsigned (&vd)[5], unsigned smb, int nt, int w, int j) {
  const unsigned sb = nt * 160u + (j < 13 ? w : (w < 2 ? w : 1)) * 40960u, lb = smb + w * 5120;
#pragma unroll
  for (int i = 0; i < 5; ++i) dma16i<NT_WD>(srdD, vd[i], sb, j * 163840, lb, g2_slot<ISP>(j % 6) + i * 1024);
}
DI void g2_wd_offsets(unsigned (&vd)[5], int lane) {
#pragma unroll
  for (int i = 0; i < 5; ++i) { const int idx = i * 64 + lane, rho = idx / 10, ch = idx - rho * 10; vd[i] = rho * 1280 + ch * 16; }
}
template <bool CONV>
DI void g1_loader(KP kp, Params p, LAS char* sm, unsigned smb, int rb, int ft, int mt, int nt, int hw, int lane, unsigned& tag, int cch, unsigned& dvb) {
  const i32x4 srdQ = make_srd(p.q, S_ * D_ * 2), srdW = make_srd((hw >> 1) ? p.wu : p.wg, D_ * F_ * 2), srdC = make_srd(p.ws, WS_KVT);
  const unsigned vq0 = (lane >> 3) * 1280 + (((lane & 7) ^ (lane >> 4)) * 16);
  const unsigned vq1 = (lane >> 3) * 1280 + (((lane & 7) ^ (4 + (lane >> 4))) * 16);
  const unsigned vw0 = (lane >> 3) * 3456 + (((lane & 7) ^ (2 * ((lane >> 4) & 1))) * 16);
  const unsigned vw1 = (lane >> 3) * 3456 + (((lane & 7) ^ (2 * (((lane >> 4) & 1) | 2))) * 16);
  const unsigned qb = (unsigned)(rb * 256 + 64 * hw) * 1280u, wb = (unsigned)(32 * (hw & 1)) * 3456u + ft * 128u;
  const unsigned lq = smb + hw * 8192, lw = smb + 32768 + (hw >> 1) * 8192 + (hw & 1) * 4096;
  auto issue = [&](int s, int slot) {
#pragma unroll
    for (int i = 0; i < 4; ++i) dma16i<NT_G1>(srdW, (i & 1) ? vw1 : vw0, wb, (s * 64 + 8 * i) * 3456, lw, slot * G1_SLOT + i * 1024);
#pragma unroll
    for (int i = 0; i < 8; ++i) dma16i<NT_G1>(srdQ, (i & 1) ? vq1 : vq0, qb, i * 10240 + s * 128, lq, slot * G1_SLOT + i * 1024);
  };
  const int xq = blockIdx.x & 7, wv = (blockIdx.x >> 3) * 4 + hw;
  const unsigned junk = smb + L_END - 256;
  const i32x4 srdD = make_srd(p.wd, F_ * D_ * 2);
  auto touch_slot = [&](int sl) {
    const int g = wv % 3, sub = wv / 3, gsel = (sl + 1) % 3, st = sl + 3;
    unsigned fo = (((27 * (xq & 3)) >> 2) + (lane & 7)) * 128u; fo = fo < 3456u ? fo : 3328u;
    const unsigned vt = (unsigned)((xq >> 2) * 1024 + (sub & 15) * 64 + lane) * 1280u + st * 128u;
    const unsigned vw = (unsigned)(st * 64 + ((sub & 15) >> 1) * 8 + (lane >> 3)) * 3456u + fo;
    const bool stage = TOUCH && g == gsel && sub < 32;
    const int kid = sl == -1 ? (g ? sub * 2 + g - 1 : 999) : (sl == 0 && g == 0 && sub < 9 ? 72 + sub : 999);
    const unsigned n = (unsigned)kid * 64u + lane;
    const bool kv = KVT && sl <= 0 && !stage && kid < 81 && n < 5150u, isk = n < 2590u;
    const unsigned lkv = isk ? (xq ? xq * 2560u - 30u : 0u) + n : xq * 2560u + (n - 2590u);
    const char* base = stage ? (sub < 16 ? (const char*)p.q : ((sub & 1) ? (const char*)p.wu : (const char*)p.wg)) : (const char*)p.q;
    const unsigned off = stage ? (sub < 16 ? vt : vw) : 0u;
    if (KVT && sl <= 0) {
      const unsigned long long a = kv ? (unsigned long long)(isk ? (const char*)p.k : (const char*)p.v) + lkv * 128u : (unsigned long long)base + off;
      asm volatile("s_mov_b32 m0, %1\n\ts_nop 0\n\tglobal_load_lds_dword %0, off" :: "v"(a), "s"(junk) : "memory");
    } else if (NT_G1) touchg_nt(base, off, junk); else touchg(base, off, junk);
  };
  unsigned ep; ld4_sc1(ep, srdC, CW_EPOCH);
  BAR();
  ConvHalf in;
  issue(0, 0); issue(1, 1);
  touch_slot(-1);
  WAITV(13);
  PIN(ep); tag = __builtin_amdgcn_readfirstlane(ep) + 1u;
  if (lane == 0) { *(LAS unsigned*)(sm + L_PF + 16 + hw * 4) = ~tag; if ((hw & 1) == 0) *(LAS unsigned*)(sm + L_PF + (hw >> 1) * 4) = ~tag; }
  BAR();
  if (hw == 0) flag_set(p.ws, CW_ARRIVE + blockIdx.x * 4, tag, lane);
  issue(2, 2); touch_slot(0);
  unsigned vd[5]; g2_wd_offsets(vd, lane);
  unsigned f1 = 0, f2 = 0; u32x2 st, st2;
  const unsigned f1off = lane < 16 ? CW_ST + (rb * 64 + hw * 16 + lane) * 16 : CW_ST + (mt * 16 + (lane & 15)) * 16;
  const unsigned f2off = CW_KV + ((CONV ? (cch >> 6) : 0) * 32 + (lane & 31)) * 16;
#pragma unroll
  for (int s = 1; s < 10; ++s) {
    const int nslot = (s + 2) % 3;
    if (s >= 2 && s <= 4) { if (CONV) WAITV(17); else WAITV(14); }
    else if (s == 6) WAITV(15);
    else if (s == 7) { if (CONV) WAITV(15); else WAITV(14); }
    else if (s == 8) WAITV(15);
    else if (s == 9) { if (CONV) WAITV(12); else WAITV(10); }
    else if (s == 1) WAITV(14 + MALLW);
    else WAITV(14);
    BAR();
    if (s >= 1 && s <= 3 && CONV) convh_load_h(p, in, cch, hw & 1, lane, s - 1);
    if (s == 5) ld4_sc1(f1, srdC, f1off);
    if (s == 7) {
      PIN(f1);
      if (!__all(f1 == tag)) poll_all(p.ws, f1off, tag);
      ld8_sc1(st, srdC, WS_STATS + (rb * 256 + hw * 64 + lane) * 8, 0);
      ld8_sc1(st2, srdC, WS_STATS + (mt * 64 + lane) * 8, 0);
    }
    if (s == 6 && CONV) ld4_sc1(f2, srdC, f2off);
    if (s == 8 && CONV) {
      PIN(f2);
      if (!__all(f2 == tag)) poll_all(p.ws, f2off, tag);
      convh_load_u(p, in, cch, hw & 1, lane);
    }
    if (s <= 7) issue(s + 2, nslot);
    if (s <= 6) touch_slot(s);
    if (s == 8) { g2_issue_w<false>(srdD, vd, smb, nt, hw, 0); g2_issue_w<false>(srdD, vd, smb, nt, hw, 1); }
    if (s == 9) {
      PIN(st); PIN(st2);
      *(LAS u32x2*)(sm + L_ST + (hw * 64 + lane) * 8) = st;
      if (hw == 0) *(LAS u32x2*)(sm + L_S2 + lane * 8) = st2;
    }
  }
  BAR();
  g2_issue_w<false>(srdD, vd, smb, nt, hw, 2); g2_issue_w<false>(srdD, vd, smb, nt, hw, 3);
  if (CONV) {
    WAITV(20);
    convh_build(in, sm + L_CVG + (hw >> 1) * CV_SZ, hw & 1, lane); dvb = in.dv;
    asm volatile("s_waitcnt lgkmcnt(0)" ::: "memory");
    if (lane == 0) *(volatile LAS unsigned*)(sm + L_PF + 16 + hw * 4) = tag;
#pragma unroll 1
    for (unsigned it = 0; it < (1u << 22); ++it) { if (*(volatile LAS unsigned*)(sm + L_PF + 16 + (hw ^ 1) * 4) == tag) break; __builtin_amdgcn_s_sleep(1); }
    asm volatile("" ::: "memory");
  }
}

DI void g1_epi_tile(const __amdgpu_buffer_rsrc_t& rh, unsigned hb, int m, const f32x4 (&a1)[2][8], const f32x4 (&a2)[2][8], const f32x4 (&csg)[2], const f32x4 (&csu)[2], f32x2 st) {
  const float nm = -st.x, rs = st.y, rsl = rs * -1.44269504089f, irs2 = __builtin_amdgcn_rcpf(rs * rs);
  f32x2 tg[4], tu[4], e[4];
#pragma unroll
  for (int h = 0; h < 4; ++h) {
    const int a = h >> 1, i = (h & 1) * 2;
    const f32x4 ga = a1[a][m], ua = a2[a][m];
    tg[h] = (f32x2){csg[a][i], csg[a][i + 1]} * nm + (f32x2){ga[i], ga[i + 1]};
    tu[h] = (f32x2){csu[a][i], csu[a][i + 1]} * nm + (f32x2){ua[i], ua[i + 1]};
  }
#pragma unroll
  for (int h = 0; h < 4; ++h) { const f32x2 gl = tg[h] * rsl; e[h].x = __builtin_amdgcn_exp2f(gl.x); e[h].y = __builtin_amdgcn_exp2f(gl.y); }
#pragma unroll
  for (int h = 0; h < 4; ++h) { tu[h] = tg[h] * tu[h]; e[h] = e[h] * irs2 + irs2; }
#pragma unroll
  for (int h = 0; h < 4; ++h) { e[h].x = __builtin_amdgcn_rcpf(e[h].x); e[h].y = __builtin_amdgcn_rcpf(e[h].y); }
  u32x4 w;
#pragma unroll
  for (int h = 0; h < 4; ++h) { const f32x2 hv = tu[h] * e[h]; w[h] = pk2(hv.x, hv.y); }
  __builtin_amdgcn_raw_buffer_store_b128(w, rh, hb + m * 1024, 0, 16);
}
DI void g1_tail(const Params& p, LAS char* sm, int rb, int ft, int wr, int wc, int lane, f32x4 (&a1)[2][8], f32x4 (&a2)[2][8], const bf16x8 (&tQ)[8],
                const bf16x8 (&gQ)[2], const bf16x8 (&uQ)[2], const f32x4 (&csg)[2], const f32x4 (&csu)[2]) {
  const __amdgpu_buffer_rsrc_t rh = __builtin_amdgcn_make_buffer_rsrc((void*)(p.ws + WS_HM), 0, 54 * 128 * 1024, 0x00020000);
  const unsigned hb = (unsigned)((ft * 2 + wc) * 128 + rb * 16 + wr * 8) * 1024u + lane * 16;
  LAS char* sp = sm + L_ST + (wr * 128 + (lane & 15)) * 8;
#define G1_MT(m) do { _Pragma("unroll") for (int a = 0; a < 2; ++a) { \
    a1[a][m] = __builtin_amdgcn_mfma_f32_16x16x32_bf16(gQ[a], tQ[m], a1[a][m], 0, 0, 0); a2[a][m] = __builtin_amdgcn_mfma_f32_16x16x32_bf16(uQ[a], tQ[m], a2[a][m], 0, 0, 0); } } while (0)
  f32x2 st0 = *(LAS f32x2*)(sp), st1 = *(LAS f32x2*)(sp + 128);
  G1_MT(0); G1_MT(1);
  __builtin_amdgcn_sched_barrier(0);
#pragma unroll
  for (int m = 0; m < 8; ++m) {
    const f32x2 stc = st0; st0 = st1;
    if (m + 2 < 8) { st1 = *(LAS f32x2*)(sp + (m + 2) * 128); G1_MT(m + 2); }
    g1_epi_tile(rh, hb, m, a1, a2, csg, csu, stc);
    if (m + 2 < 8) {
#pragma unroll
      for (int i_ = 0; i_ < 4; ++i_) { __builtin_amdgcn_sched_group_barrier(0x8, 1, 0); __builtin_amdgcn_sched_group_barrier(0x2 | 0x400, 10, 0); }
    }
    __builtin_amdgcn_sched_barrier(0);
  }
}
DI void g1_math(const Params& p, LAS char* sm, int rb, int ft, int wid, int lane, unsigned tag, unsigned taskflag) {
  const int wr = wid >> 1, wc = wid & 1;
  const int n = lane & 15, kg = lane >> 4, q4 = (lane & 15) >> 2, pp = lane & 3;
  f32x4 a1[2][8], a2[2][8], cs[2];
#pragma unroll
  for (int a = 0; a < 2; ++a) {
    cs[a] = (f32x4){0.f, 0.f, 0.f, 0.f};
#pragma unroll
    for (int m = 0; m < 8; ++m) { a1[a][m] = (f32x4){0.f, 0.f, 0.f, 0.f}; a2[a][m] = (f32x4){0.f, 0.f, 0.f, 0.f}; }
  }
  u32x4 ones_ = {0x3f803f80u, 0x3f803f80u, 0x3f803f80u, 0x3f803f80u};
  PIN(ones_);
  const bf16x8 ones = __builtin_bit_cast(bf16x8, ones_);
  const unsigned tbase = (wr * 128 + n) * 128 + ((kg ^ ((n >> 1) & 7)) * 16);
  const int sw = 2 * (((q4 >> 1) & 1) | ((kg & 1) << 1));
  const unsigned wb0 = 32768 + (8 * kg + q4) * 128 + (((4 * wc + 2 * wr + (pp >> 1)) ^ sw) * 16) + 8 * (pp & 1);
  const unsigned wb1 = 32768 + (8 * kg + q4) * 128 + (((4 * wc + 2 * (1 - wr) + (pp >> 1)) ^ sw) * 16) + 8 * (pp & 1);
  bf16x8 tP[8], tQ[8], gP[2], gQ[2], uP[2], uQ[2];
#define G1_RD(T, G, U, so, ks) do { LAS char* tp_ = sm + (so) + (tbase ^ ((ks) * 64)); LAS char* w0_ = sm + (so) + wb0 + (ks) * 4096; LAS char* w1_ = sm + (so) + wb1 + (ks) * 4096; \
    G[0] = ldtr(w0_, w0_ + 512); T[0] = *(LAS bf16x8*)(tp_); U[0] = ldtr(w0_ + 8192, w0_ + 8192 + 512); G[1] = ldtr(w1_, w1_ + 512); U[1] = ldtr(w1_ + 8192, w1_ + 8192 + 512); \
    _Pragma("unroll") for (int m = 1; m < 8; ++m) T[m] = *(LAS bf16x8*)(tp_ + 2048 * m); } while (0)
#define G1_MM(T, G, U) do { _Pragma("unroll") for (int m = 0; m < 8; ++m) { _Pragma("unroll") for (int a = 0; a < 2; ++a) { \
    a1[a][m] = __builtin_amdgcn_mfma_f32_16x16x32_bf16(G[a], T[m], a1[a][m], 0, 0, 0); a2[a][m] = __builtin_amdgcn_mfma_f32_16x16x32_bf16(U[a], T[m], a2[a][m], 0, 0, 0); } } } while (0)
#define G1_CS(G, U) do { cs[0] = __builtin_amdgcn_mfma_f32_16x16x32_bf16(G[0], ones, cs[0], 0, 0, 0); cs[1] = __builtin_amdgcn_mfma_f32_16x16x32_bf16(U[0], ones, cs[1], 0, 0, 0); } while (0)
#define G1_SCHED() do { _Pragma("unroll") for (int i_ = 0; i_ < 16; ++i_) { __builtin_amdgcn_sched_group_barrier(0x8, 1, 0); __builtin_amdgcn_sched_group_barrier(0x100, 1, 0); } __builtin_amdgcn_sched_group_barrier(0x8, 18, 0); } while (0)
  unsigned so = 0;
  __builtin_amdgcn_s_setprio(PRIO_X);
  BAR();
  STAMP_AT(1); STAMP_CY(14);
  G1_RD(tP, gP, uP, so, 0);
  G1_RD(tQ, gQ, uQ, so, 1); G1_MM(tP, gP, uP); G1_CS(gP, uP); G1_SCHED();
#pragma unroll 1
  for (int s = 1; s < 10; ++s) {
    so = so == 2 * G1_SLOT ? 0 : so + G1_SLOT;
    BAR();
    if (s == 3 && taskflag) { WAITV(0); flag_set(p.ws, taskflag, tag, lane); }
    G1_RD(tP, gP, uP, so, 0); G1_MM(tQ, gQ, uQ); G1_CS(gQ, uQ); G1_SCHED();
    G1_RD(tQ, gQ, uQ, so, 1); G1_MM(tP, gP, uP); G1_CS(gP, uP); G1_SCHED();
  }
  G1_CS(gQ, uQ);
  if (n == 0) { *(LAS f32x4*)(sm + L_CS + (wc * 32 + 16 * wr + 4 * kg) * 4) = cs[0]; *(LAS f32x4*)(sm + L_CS + (64 + wc * 32 + 16 * wr + 4 * kg) * 4) = cs[1]; }
  BAR();
  STAMP_AT(2);
  f32x4 csg[2], csu[2];
#pragma unroll
  for (int a = 0; a < 2; ++a) { csg[a] = *(LAS f32x4*)(sm + L_CS + (wc * 32 + 16 * (a ^ wr) + 4 * kg) * 4); csu[a] = *(LAS f32x4*)(sm + L_CS + (64 + wc * 32 + 16 * (a ^ wr) + 4 * kg) * 4); }
  g1_tail(p, sm, rb, ft, wr, wc, lane, a1, a2, tQ, gQ, uQ, csg, csu);
  __builtin_amdgcn_s_setprio(0);
  STAMP_AT(3);
}

template <bool ISP>
DI void g2_loader(const Params& p, LAS char* sm, unsigned smb, int mt, int nt, int w, int lane, unsigned tag, unsigned hmflag) {
  const i32x4 srdD = make_srd(p.wd, F_ * D_ * 2), srdC = make_srd(p.ws, WS_KVT);
  unsigned vd[5]; g2_wd_offsets(vd, lane);
  auto fbof = [&](int j) { const int fb = 4 * j + w; return fb < 54 ? fb : 53; };
  constexpr int XB = ISP ? 3 : 4, NREG = ISP ? NREG_P : NREG_G, NPRE = XB + NREG;
  u32x4 wreg[NREG][5];
  if (ISP && w == 0) {
    poll_all(p.ws, CW_ST + (mt * 16 + (lane & 15)) * 16, tag);
    u32x2 sv; ld8_sc1(sv, srdC, WS_STATS + (mt * 64 + lane) * 8, 0);
    WAITV(0); PIN(sv);
    *(LAS u32x2*)(sm + L_S2 + lane * 8) = sv;
  }
#pragma unroll
  for (int j = 0; j < NREG; ++j) {
#pragma unroll
    for (int i = 0; i < 5; ++i) ld16i<NT_WD>(wreg[j][i], srdD, vd[i], nt * 160u + w * 40960u, (j + XB) * 163840);
    if (!ISP && j == 0) {
      WAITV(5);
      flag_set(p.ws, hmflag, tag, lane);
      STAMP_AT(4);
    }
  }
  u32x2 xr[5];
  {
    const i32x4 srdQ = make_srd(p.q, S_ * D_ * 2);
#pragma unroll
    for (int a = 0; a < 5; ++a) ld8i(xr[a], srdQ, (unsigned)(mt * 64 + w * 16 + (lane & 15)) * 1280u + (lane >> 4) * 8, nt * 160u, a * 32);
  }
#pragma unroll
  for (int r = 0; r < (14 - NPRE + 3) / 4; ++r) {
    const int idx = r * 64 + lane;
    int j = NPRE + (idx >> 4); j = j < 14 ? j : 13;
    touch4(srdD, (unsigned)(fbof(j) * 32 + (mt & 3) * 8 + ((idx & 15) >> 1)) * 1280u + nt * 160u + (idx & 1) * 128u, 0, smb + L_END - 256);
  }
  if (ISP) {
    BAR();
    g2_issue_w<true>(srdD, vd, smb, nt, w, 0); g2_issue_w<true>(srdD, vd, smb, nt, w, 1); g2_issue_w<true>(srdD, vd, smb, nt, w, 2);
  }
  if (ISP) WAITV(0);
  BAR();
  STAMP_AT(6);
  WAITV(0);
#pragma unroll
  for (int j = 0; j < NREG; ++j)
#pragma unroll
    for (int i = 0; i < 5; ++i) PIN(wreg[j][i]);
#pragma unroll
  for (int a = 0; a < 5; ++a) PIN(xr[a]);
  STAMP_AT(5);
  unsigned zf = 0; u32x4 zr[3];
  const unsigned zoff = CW_Z + (nt * 80 + w * 20 + (lane < 20 ? lane : 0)) * 16;
  auto fill = [&](int j) {
    if (j < NPRE) {
#pragma unroll
      for (int i = 0; i < 5; ++i) *(LAS u32x4*)(sm + g2_slot<ISP>(j % 6) + w * 5120 + i * 1024 + lane * 16) = wreg[j < NPRE ? j - XB : 0][i];
    } else g2_issue_w<ISP>(srdD, vd, smb, nt, w, j);
  };
#pragma unroll
  for (int j = XB; j < 6; ++j) fill(j);
  constexpr int n89 = (8 >= NPRE ? 5 : 0) + (9 >= NPRE ? 5 : 0), n1011 = (10 >= NPRE ? 5 : 0) + (11 >= NPRE ? 5 : 0);
  BAR();
  fill(6); fill(7);
  BAR();
  fill(8); fill(9);
  WAITV(n89);
  BAR();
  fill(10); fill(11);
  WAITV(n1011);
  BAR();
  ld4_sc1(zf, srdC, zoff);
  fill(12); fill(13);
  WAITV(10);
  BAR();
  PIN(zf);
  if (!__all(zf == tag)) poll_all(p.ws, zoff, tag);
  {
    const i32x4 srdZ = make_srd(p.ws + WS_ZT, D_ * S_ * 2);
#pragma unroll
    for (int r = 0; r < 3; ++r) {
      int task = r * 64 + lane; task = task < 160 ? task : 159;
      ld16_sc1(zr[r], srdZ, (unsigned)(nt * 80 + w * 20 + (task >> 3)) * 4096u + (mt * 64 + (task & 7) * 8) * 2, 0);
    }
  }
  WAITV(3);
  BAR();
  WAITV(0);
#pragma unroll
  for (int r = 0; r < 3; ++r) PIN(zr[r]);
#pragma unroll
  for (int r = 0; r < 3; ++r) { const int task = r * 64 + lane; if (task < 160) *(LAS u32x4*)(sm + L_ZS + (w * 20 + (task >> 3)) * ZS_RS + (task & 7) * 16) = zr[r]; }
  BAR();
  {
    const int n = lane & 15, q = lane >> 4;
    const f32x2 st = *(LAS f32x2*)(sm + L_S2 + (w * 16 + n) * 8);
    const float rs = st.y, nmr = -st.x * rs;
#pragma unroll
    for (int a = 0; a < 5; ++a) {
      const u32x2 z = __builtin_bit_cast(u32x2, __builtin_amdgcn_ds_read_tr16_b64_v4i16((LAS s16x4*)(sm + L_ZS + (16 * a + 4 * q + (n >> 2)) * ZS_RS + w * 32 + (n & 3) * 8)));
      const f32x2 x0 = {blo(xr[a].x), bhi(xr[a].x)}, x1 = {blo(xr[a].y), bhi(xr[a].y)};
      const f32x2 b0 = (x0 * rs + nmr) * (f32x2){blo(z.x), bhi(z.x)} + x0, b1 = (x1 * rs + nmr) * (f32x2){blo(z.y), bhi(z.y)} + x1;
      *(LAS u32x2*)(sm + L_BS + (w * 5 + a) * 512 + lane * 8) = (u32x2){pk2(b0.x, b0.y), pk2(b1.x, b1.y)};
    }
  }
  BAR();
  if (ISP && blockIdx.x == 255 && w == 3) {
    poll_all(p.ws, CW_ARRIVE + lane * 4, tag); poll_all(p.ws, CW_ARRIVE + 256 + lane * 4, tag);
    poll_all(p.ws, CW_ARRIVE + 512 + lane * 4, tag); poll_all(p.ws, CW_ARRIVE + 768 + lane * 4, tag);
    flag_set(p.ws, CW_EPOCH, tag, lane);
  }
}

template <bool ISP>
DI void g2_math(KP kp, Params p, LAS char* sm, int mt, int nt, int w, int lane, unsigned tag, int zc) {
  f32x4 acc[5][4];
#pragma unroll
  for (int a = 0; a < 5; ++a)
#pragma unroll
    for (int b = 0; b < 4; ++b) acc[a][b] = (f32x4){0.f, 0.f, 0.f, 0.f};
  const int kg = lane >> 4, q4 = (lane & 15) >> 2, pp = lane & 3;
  const i32x4 srdH = make_srd(p.ws + WS_HM, 54 * 128 * 1024);
  unsigned vh[4];
#pragma unroll
  for (int b = 0; b < 4; ++b) vh[b] = lane * 16 + ((b + w) & 3) * 1024;
  const unsigned hsb = mt * 4096u + w * 131072u, hsl = mt * 4096u + (w < 2 ? w : 1) * 131072u;
  constexpr int WB1 = 69632;
  const int xw = (mt >> 1) & 1;
  unsigned wa0 = (unsigned)(size_t)sm + w * 5120 + (4 * kg + q4) * 160 + pp * 8 + xw * 2560, wa1 = wa0 + WB1;
  unsigned wb0 = wa0 + 2560 - xw * 5120, wb1 = wb0 + WB1;
  PIN(wa0); PIN(wa1); PIN(wb0); PIN(wb1);
  u32x4 H[4][4]; bf16x8 W[2][5];
#define G2_LDH(s, i) do { unsigned t_; asm volatile("s_add_u32 %1, %4, %5\n\tbuffer_load_dwordx4 %0, %2, %3, %1 offen sc1" : "=v"(H[(s) & 3][i]), "=&s"(t_) : "v"(vh[i]), "s"(srdH), "s"((s) < 13 ? hsb : hsl), "i"((s) * 524288) : "memory", "scc"); } while (0)
#define G2_RDW(s) do { const int so_ = g2_slot<ISP>((s) % 6); LAS char* wp_ = so_ >= WB1 ? (LAS char*)(size_t)wa1 + (so_ - WB1) : (LAS char*)(size_t)wa0 + so_; \
    LAS char* wq_ = so_ >= WB1 ? (LAS char*)(size_t)wb1 + (so_ - WB1) : (LAS char*)(size_t)wb0 + so_; \
    _Pragma("unroll") for (int a = 0; a < 5; ++a) W[(s) & 1][a] = ldtr(wp_ + a * 32, wq_ + a * 32); } while (0)
#define G2_MMA(s, a) do { _Pragma("unroll") for (int b = 0; b < 4; ++b) \
    acc[a][b] = __builtin_amdgcn_mfma_f32_16x16x32_bf16(W[(s) & 1][a], __builtin_bit_cast(bf16x8, H[(s) & 3][b]), acc[a][b], 0, 0, 0); } while (0)
#define G2_PINA(a) asm volatile("" : "+v"(acc[a][0]), "+v"(acc[a][1]), "+v"(acc[a][2]), "+v"(acc[a][3]))
  if (zc >= 0) WAITV(4); else WAITV(0);
  BAR();
  { const int j = lane < 14 ? lane : 0, fb = 4 * j + w < 54 ? 4 * j + w : 53; poll_all(p.ws, CW_HM + (((mt >> 2) * 2 + xw) * 54 + fb) * 16, tag); }
  WAITV(0);
  STAMP_AT(9);
#pragma unroll
  for (int s = 0; s < 3; ++s)
#pragma unroll
    for (int i = 0; i < 4; ++i) G2_LDH(s, i);
  G2_RDW(0);
  if (zc >= 0) flag_set(p.ws, CW_Z + zc * 16, tag, lane);
#pragma unroll
  for (int s = 1; s <= 14; ++s) {
    if ((s & 1) == 0) BAR();
    if (s <= 12) WAITV(8); else if (s == 13) WAITV(4); else WAITV(0);
#pragma unroll
    for (int i = 0; i < 4; ++i) PIN(H[(s - 1) & 3][i]);
    if (s <= 13) G2_RDW(s);
    if (s < 14 || w < 2) {
      G2_MMA(s - 1, 0); G2_MMA(s - 1, 1);
      if (s <= 13) {
#pragma unroll
        for (int i_ = 0; i_ < 8; ++i_) { __builtin_amdgcn_sched_group_barrier(0x8, 1, 0); __builtin_amdgcn_sched_group_barrier(0x100, 1, 0); }
        __builtin_amdgcn_sched_group_barrier(0x100, 2, 0);
      }
      G2_PINA(0);
#pragma unroll
      for (int a = 2; a < 5; ++a) {
        if (s + 2 <= 13) { __builtin_amdgcn_sched_barrier(0); G2_LDH(s + 2, a - 2); __builtin_amdgcn_sched_barrier(0); }
        G2_MMA(s - 1, a);
        G2_PINA(a - 1);
      }
      if (s + 2 <= 13) { __builtin_amdgcn_sched_barrier(0); G2_LDH(s + 2, 3); __builtin_amdgcn_sched_barrier(0); }
      G2_PINA(4);
    }
  }
  STAMP_AT(10);
#pragma unroll
  for (int b = 1; b < 4; ++b) {
    LAS char* ex = sm + L_EX + ((((b + w) & 3) * 3 + (b - 1)) * 5) * 1024 + lane * 16;
#pragma unroll
    for (int a = 0; a < 5; ++a) *(LAS f32x4*)(ex + a * 1024) = acc[a][b];
  }
  BAR();
  p = reload(kp);
  u16* op = p.out + (mt * 64 + w * 16 + (lane & 15)) * 640 + nt * 80 + kg * 4;
#pragma unroll
  for (int a = 0; a < 5; ++a) {
    LAS char* ex = sm + L_EX + (w * 15 + a) * 1024 + lane * 16;
    const f32x4 s = (acc[a][0] + *(LAS f32x4*)(ex)) + (*(LAS f32x4*)(ex + 5 * 1024) + *(LAS f32x4*)(ex + 10 * 1024));
    const u32x2 bs = *(LAS u32x2*)(sm + L_BS + (w * 5 + a) * 512 + lane * 8);
    const u32x2 r = {pk2(blo(bs.x) + s[0], bhi(bs.x) + s[1]), pk2(blo(bs.y) + s[2], bhi(bs.y) + s[3])};
    *(GAS u32x2*)(op + a * 16) = r;
  }
}

DI void pair_send(LAS char* pb, LAS unsigned* pf, unsigned tag, int lane, const f32x4 (&acc)[8]) {
#pragma unroll
  for (int g = 0; g < 8; ++g) *(LAS f32x4*)(pb + g * 1024 + lane * 16) = acc[g];
  asm volatile("s_waitcnt lgkmcnt(0)" ::: "memory");
  if (lane == 0) *(volatile LAS unsigned*)pf = tag;
}
DI void pair_recv(LAS char* pb, LAS unsigned* pf, unsigned tag, int lane, f32x4 (&acc)[8]) {
#pragma unroll 1
  for (unsigned it = 0; it < (1u << 22); ++it) { if (*(volatile LAS unsigned*)pf == tag) break; __builtin_amdgcn_s_sleep(1); }
  asm volatile("" ::: "memory");
#pragma unroll
  for (int g = 0; g < 8; ++g) acc[g] += *(LAS f32x4*)(pb + g * 1024 + lane * 16);
}

__global__ __launch_bounds__(512, 2) void fused_kernel(Params p_arg) {
  {
    unsigned long long pc0; asm volatile("s_getpc_b64 %0" : "=s"(pc0));
    const unsigned piece = (blockIdx.x >> 3) % CODE_PIECES;
    if ((threadIdx.x >> 6) == 3 && (blockIdx.x >> 3) < 27) asm volatile("s_mov_b32 m0, %2\n\ts_nop 0\n\tglobal_load_lds_dword %0, %1" :: "v"(piece * 8192u + (threadIdx.x & 63) * 128u), "s"(pc0 & ~127ull), "s"(L_END - 256) : "memory");
  }
  const KP kp = (KP)__builtin_amdgcn_kernarg_segment_ptr();
  Params p = reload(kp);
  extern __shared__ __attribute__((aligned(16))) char smem_raw[];
  LAS char* sm = (LAS char*)smem_raw;
  const unsigned smb = (unsigned)(size_t)sm;
  const int lane = threadIdx.x & 63, wid = __builtin_amdgcn_readfirstlane(threadIdx.x >> 6);
  const int b = blockIdx.x, x = b & 7, i = b >> 3;
  const int mt = x * 4 + (i & 3), nt = i >> 2;
  STAMP_INIT();
  STAMP_AT(0);
  if (__builtin_expect(i < 27, 1)) {
    const int mm = 27 * (x & 3) + i, ft = mm >> 2, rb = 4 * (x >> 2) + (mm & 3);
    if (__builtin_expect(wid < 4, 0)) {
      const unsigned ep = __hip_atomic_load((gu32*)(p.ws + CW_EPOCH), __ATOMIC_RELAXED, __HIP_MEMORY_SCOPE_AGENT);
      unsigned taskflag = 0;
      const int id = wid < 2 ? 2 * b + wid : 432 + b;
      if (wid < 3 && id < 512) { task_stats<true>(p, id, lane); taskflag = CW_ST + id * 16; }
      else BAR();
      const unsigned tag = __builtin_amdgcn_readfirstlane(ep) + 1u;
      asm volatile("" :: "s"(tag) : "memory");
      STAMP_AT(13);
      g1_math(p, sm, rb, ft, wid, lane, tag, taskflag);
      p = reload(kp);
      int lane_b; asm volatile("v_mbcnt_lo_u32_b32 %0, -1, 0\n\tv_mbcnt_hi_u32_b32 %0, -1, %0" : "=v"(lane_b));
      g2_loader<false>(p, sm, smb, mt, nt, wid, lane_b, tag, CW_HM + ((rb * 2 + (wid >> 1)) * 54 + ft * 2 + (wid & 1)) * 16);
    } else {
      const int hw = wid - 4;
      const bool conv = 2 * b < NCH_G;
      const int cch = 2 * b + (hw >> 1);
      unsigned tag, dvb = 0; int zc = -1;
      if (__builtin_expect(conv, 1)) {
        g1_loader<true>(kp, p, sm, smb, rb, ft, mt, nt, hw, lane, tag, cch, dvb);
        p = reload(kp);
        STAMP_AT(7);
        LAS char* cb = sm + L_CVG + (hw >> 1) * CV_SZ;
        LAS char* pb = sm + L_CP + (hw >> 1) * 8192;
        LAS unsigned* pf = (LAS unsigned*)(sm + L_PF + (hw >> 1) * 4);
        f32x4 acc[8];
        conv_mfma<4>(cb, (hw & 1) * 4, lane, acc);
        asm volatile("" :: "v"(acc[0]), "v"(acc[7]));
        STAMP_AT(4);
        if (hw & 1) pair_send(pb, pf, tag, lane, acc);
        else { pair_recv(pb, pf, tag, lane, acc); conv_store(p, cb, cch, lane, dvb, acc); zc = cch; }
        STAMP_AT(8);
        g2_math<false>(kp, p, sm, mt, nt, hw, lane, tag, zc);
      } else {
        g1_loader<false>(kp, p, sm, smb, rb, ft, mt, nt, hw, lane, tag, cch, dvb);
        p = reload(kp);
        g2_math<false>(kp, p, sm, mt, nt, hw, lane, tag, zc);
      }
    }
  } else {
    const int cg = i - 27, pb = x * 5 + cg;
    const unsigned ep = __hip_atomic_load((gu32*)(p.ws + CW_EPOCH), __ATOMIC_RELAXED, __HIP_MEMORY_SCOPE_AGENT);
    const int hw = wid - 4, c0 = NCH_G + pb * NCH_P;
    if (wid < 4) {
      task_kv<0>(p, 2 * cg + (wid & 1), 4 * x + (wid >> 1), lane, [] {});
      const unsigned tag = __builtin_amdgcn_readfirstlane(ep) + 1u;
      asm volatile("" :: "s"(tag) : "memory");
      WAITV(0);
      flag_set(p.ws, CW_KV + ((2 * cg + (wid & 1)) * 32 + 4 * x + (wid >> 1)) * 16, tag, lane);
      STAMP_AT(1);
      BAR();
      if (wid == 0) flag_set(p.ws, CW_ARRIVE + b * 4, tag, lane);
      p = reload(kp);
      {
        const int cB = c0 + 4 + (wid >> 1);
        ConvHalf in;
        convh_load_h(p, in, cB, wid & 1, lane, 0); convh_load_h(p, in, cB, wid & 1, lane, 1); convh_load_h(p, in, cB, wid & 1, lane, 2);
        poll_all(p.ws, CW_KV + ((cB >> 6) * 32 + (lane & 31)) * 16, tag);
        convh_load_u(p, in, cB, wid & 1, lane);
        WAITV(0);
        convh_build(in, sm + (wid >> 1) * CV_SZ, wid & 1, lane);
      }
      touchg(p.wd, (unsigned)((pb * 4 + wid) * 64 + lane) * 128u, smb + L_END - 256);
      if (pb * 4 + wid + 160 < 270) touchg(p.wd, (unsigned)((pb * 4 + wid + 160) * 64 + lane) * 128u, smb + L_END - 256);
      g2_loader<true>(p, sm, smb, mt, nt, wid, lane, tag, 0);
    } else {
      LAS char* cb = sm + L_CVP + hw * CV_SZ;
      const int cA = c0 + hw, cB = c0 + 4 + (hw >> 1);
      ConvIn in;
      task_kv<0>(p, 2 * cg + (wid & 1), 4 * x + (wid >> 1), lane, [] {});
      const unsigned tag = __builtin_amdgcn_readfirstlane(ep) + 1u;
      asm volatile("" :: "s"(tag) : "memory");
      if ((hw & 1) == 0 && lane == 0) *(LAS unsigned*)(sm + L_PFP + (hw >> 1) * 4) = ~tag;
      WAITV(0);
      flag_set(p.ws, CW_KV + ((2 * cg + (wid & 1)) * 32 + 4 * x + (wid >> 1)) * 16, tag, lane);
      STAMP_AT(1);
      conv_load_h(p, in, cA, lane);
      unsigned dvB;
      { const i32x4 rd = make_srd(p.dv, D_ * 2); asm volatile("buffer_load_ushort %0, %1, %2, 0 offen" : "=v"(dvB) : "v"(cB * 2u), "s"(rd) : "memory"); }
      poll_all(p.ws, CW_KV + ((cA >> 6) * 32 + (lane & 31)) * 16, tag);
      conv_load_u(p, in, cA, lane);
      BAR();
      WAITV(0);
      conv_pin(in); PIN(dvB);
      conv_build(in, cb, lane);
      STAMP_AT(2); STAMP_CY(14);
      f32x4 acc[8];
      conv_mfma<8>(cb, 0, lane, acc);
      asm volatile("" :: "v"(acc[0]), "v"(acc[7]));
      STAMP_AT(4); STAMP_CY(15);
      conv_store(p, cb, cA, lane, in.dv, acc);
      STAMP_AT(3);
      BAR();
      STAMP_AT(7);
      LAS char* cb2 = sm + (hw >> 1) * CV_SZ;
      conv_mfma<4>(cb2, (hw & 1) * 4, lane, acc);
      WAITV(0);
      flag_set(p.ws, CW_Z + cA * 16, tag, lane);
      LAS unsigned* pf = (LAS unsigned*)(sm + L_PFP + (hw >> 1) * 4);
      LAS char* pb2 = sm + L_CPP + (hw >> 1) * 8192;
      int zc = -1;
      if (hw & 1) pair_send(pb2, pf, tag, lane, acc);
      else { pair_recv(pb2, pf, tag, lane, acc); conv_store(p, cb2, cB, lane, dvB, acc); zc = cB; }
      STAMP_AT(8);
      g2_math<true>(kp, p, sm, mt, nt, hw, lane, tag, zc);
    }
  }
  STAMP_AT(11);
  STAMP_FLUSH();
}

extern "C" void kernel_launch(void* const* d_in, const int* in_sizes, int n_in, void* d_out, int out_size, void* d_ws, size_t ws_size, hipStream_t stream) {
  Params p;
  p.q = (const u16*)d_in[0]; p.k = (const u16*)d_in[1]; p.v = (const u16*)d_in[2]; p.sk = (const u16*)d_in[3]; p.h = (const u16*)d_in[4]; p.dv = (const u16*)d_in[5];
  p.wg = (const u16*)d_in[7]; p.wu = (const u16*)d_in[8]; p.wd = (const u16*)d_in[9]; p.out = (u16*)d_out; p.ws = (char*)d_ws + WS_BASE(ws_size);
  (void)hipFuncSetAttribute((const void*)fused_kernel, hipFuncAttributeMaxDynamicSharedMemorySize, L_ALLOC);
  fused_kernel<<<256, 512, L_ALLOC, stream>>>(p);
}
#define HAVE_INTER 1
#define HM_K16 1
```
